```python
import torch

def _softmax(x, axis=-1):
    return torch.softmax(x, dim=axis)
def _gelu(x):
    return torch.nn.functional.gelu(x, approximate="tanh")
def _silu(x):
    return torch.nn.functional.silu(x)
def _sigmoid(x):
    return torch.sigmoid(x)
def _layernorm(x, axis=-1, eps=1e-5):
    m = x.mean(dim=axis, keepdim=True)
    v = x.var(dim=axis, keepdim=True, unbiased=False)
    return (x - m) / torch.sqrt(v + eps)
def _scatter_add(tgt, idx, src, axis=0):
    return tgt.clone().index_add_(axis, idx, src)
def _segment_sum(src, seg, n):
    out = torch.zeros((n,) + src.shape[1:], dtype=src.dtype)
    return out.index_add_(0, seg, src)
def _pad(x, pw):
    flat = tuple(v for pair in reversed(pw) for v in pair)
    return torch.nn.functional.pad(x, flat)

kernel_name = 'concat_heads__mha_block_cached__mha_block_cached__near_ridge'

def setup_inputs():

    _w = (lambda a: (lambda t: t.to(torch.bfloat16) if t.is_floating_point() else t)(torch.as_tensor(a)))
    def _A_setup():
        B, Sq, D = 8, 192, 512
        H, Sk, Dh = 32, 1024, 128
        return {
            "x": _w((lambda *s: torch.randn(*s, dtype=torch.bfloat16) * 0.8)(B, Sq, D)),
            "k_cache": _w((lambda *s: torch.randn(*s, dtype=torch.bfloat16) * 0.8)(B, H, Sk, Dh)),
            "v_cache": _w((lambda *s: torch.randn(*s, dtype=torch.bfloat16) * 0.05)(B, H, Sk, Dh)),
            "wq": _w((lambda *s: torch.randn(*s, dtype=torch.bfloat16) * 0.8)(D, H * Dh) * (D ** -0.5)),
            "wo": _w((lambda *s: torch.randn(*s, dtype=torch.bfloat16) * 0.05)(H * Dh, D) * ((H * Dh) ** -0.5)),
        }
    _A_in = _A_setup()

    def _B_setup():
        B, Sq, D = 8, 192, 512
        H, Sk, Dh = 32, 1024, 128
        return {
            "x": _w((lambda *s: torch.randn(*s, dtype=torch.bfloat16) * 0.8)(B, Sq, D)),
            "k_cache": _w((lambda *s: torch.randn(*s, dtype=torch.bfloat16) * 0.8)(B, H, Sk, Dh)),
            "v_cache": _w((lambda *s: torch.randn(*s, dtype=torch.bfloat16) * 0.05)(B, H, Sk, Dh)),
            "wq": _w((lambda *s: torch.randn(*s, dtype=torch.bfloat16) * 0.8)(D, H * Dh) * (D ** -0.5)),
            "wo": _w((lambda *s: torch.randn(*s, dtype=torch.bfloat16) * 0.05)(H * Dh, D) * ((H * Dh) ** -0.5)),
        }
    _B_in = _B_setup()

    return {
        "A_x": _A_in["x"],
        "A_k_cache": _A_in["k_cache"],
        "A_v_cache": _A_in["v_cache"],
        "A_wq": _A_in["wq"],
        "A_wo": _A_in["wo"],
        "B_x": _B_in["x"],
        "B_k_cache": _B_in["k_cache"],
        "B_v_cache": _B_in["v_cache"],
        "B_wq": _B_in["wq"],
        "B_wo": _B_in["wo"],
    }

def reference(A_x, A_k_cache, A_v_cache, A_wq, A_wo, B_x, B_k_cache, B_v_cache, B_wq, B_wo):
    def _A_ref(x, k_cache, v_cache, wq, wo):
        B, Sq, D = x.shape
        H, Dh = 32, 128
        q = (lambda x, *d: x.permute(*d))((x @ wq).reshape(B, Sq, H, Dh), 0, 2, 1, 3)
        scores = torch.einsum("bhqd,bhkd->bhqk", q, k_cache) * (Dh ** -0.5)
        p = _softmax(scores, axis=-1)
        o = torch.einsum("bhqk,bhkd->bhqd", p, v_cache)
        return (lambda x, *d: x.permute(*d))(o, 0, 2, 1, 3).reshape(B, Sq, H * Dh) @ wo

    def _B_ref(x, k_cache, v_cache, wq, wo):
        B, Sq, D = x.shape
        H, Dh = 32, 128
        q = (lambda x, *d: x.permute(*d))((x @ wq).reshape(B, Sq, H, Dh), 0, 2, 1, 3)
        scores = torch.einsum("bhqd,bhkd->bhqk", q, k_cache) * (Dh ** -0.5)
        p = _softmax(scores, axis=-1)
        o = torch.einsum("bhqk,bhkd->bhqd", p, v_cache)
        return (lambda x, *d: x.permute(*d))(o, 0, 2, 1, 3).reshape(B, Sq, H * Dh) @ wo

    a = _A_ref(A_x, A_k_cache, A_v_cache, A_wq, A_wo)
    b = _B_ref(B_x, B_k_cache, B_v_cache, B_wq, B_wo)
    return torch.concatenate([a, b], axis=-1)
```

```cpp
#include <hip/hip_runtime.h>
#include <hip/hip_bf16.h>
typedef unsigned short u16;
using bf16x8 = __attribute__((ext_vector_type(8))) short;
using s16x4  = __attribute__((ext_vector_type(4))) short;
using f32x16 = __attribute__((ext_vector_type(16))) float;
using u32x4  = __attribute__((ext_vector_type(4))) unsigned;
#define DI __device__ __forceinline__
#define MFMA(a, b, c) __builtin_amdgcn_mfma_f32_32x32x16_bf16((a), (b), (c), 0, 0, 0)
DI int crow(int reg, int h) { return (reg & 3) + 8 * (reg >> 2) + 4 * h; }
DI unsigned cvtpk(float lo, float hi) { unsigned r; asm volatile("v_cvt_pk_bf16_f32 %0, %1, %2\n\ts_nop 1" : "=v"(r) : "v"(lo), "v"(hi)); return r; }
DI u16 tobf(float x) { return (u16)(cvtpk(x, 0.f) & 0xffff); }

constexpr int NB = 8, SQ = 192, DM = 512, NH = 32, SK = 1024, DH = 128, HD = NH * DH;

__global__ __launch_bounds__(256) void gemm_simple(const u16* __restrict__ A0, const u16* __restrict__ B0, u16* __restrict__ C0,
                                                   const u16* __restrict__ A1, const u16* __restrict__ B1, u16* __restrict__ C1,
                                                   int M, int N, int K, int ldc, float scale) {
  const u16* A = blockIdx.z ? A1 : A0; const u16* B = blockIdx.z ? B1 : B0; u16* C = blockIdx.z ? C1 : C0;
  __shared__ __attribute__((aligned(16))) u16 As[64][40];
  __shared__ __attribute__((aligned(16))) u16 Bs[64][40];
  const int tid = threadIdx.x, wid = tid >> 6, lane = tid & 63, r = lane & 31, h = lane >> 5;
  const int m0 = blockIdx.y * 64, n0 = blockIdx.x * 64, wm = (wid >> 1) * 32, wn = (wid & 1) * 32;
  f32x16 acc = {};
  for (int k0 = 0; k0 < K; k0 += 32) {
    { int row = tid >> 2, c = (tid & 3) * 8; *(bf16x8*)&As[row][c] = *(const bf16x8*)&A[(size_t)(m0 + row) * K + k0 + c]; }
    { int kk = tid >> 3, c = (tid & 7) * 8; bf16x8 v = *(const bf16x8*)&B[(size_t)(k0 + kk) * N + n0 + c];
      for (int j = 0; j < 8; ++j) Bs[c + j][kk] = (u16)v[j]; }
    __syncthreads();
    for (int s = 0; s < 2; ++s) {
      bf16x8 a = *(const bf16x8*)&As[wm + r][16 * s + 8 * h];
      bf16x8 b = *(const bf16x8*)&Bs[wn + r][16 * s + 8 * h];
      acc = MFMA(a, b, acc);
    }
    __syncthreads();
  }
  for (int i = 0; i < 16; ++i) C[(size_t)(m0 + wm + crow(i, h)) * ldc + n0 + wn + r] = tobf(acc[i] * scale);
}

__global__ __launch_bounds__(384) void attn_simple(const u16* __restrict__ Q, const u16* __restrict__ K0, const u16* __restrict__ V0,
                                                   const u16* __restrict__ K1, const u16* __restrict__ V1, u16* __restrict__ O) {
  __shared__ __attribute__((aligned(16))) char Ks[32 * 256];
  __shared__ __attribute__((aligned(16))) char Vs[32 * 256];
  const int item = blockIdx.x, blk = item >> 8, b = (item >> 5) & 7, hh = item & 31;
  const u16* Kh = (blk ? K1 : K0) + ((size_t)(b * NH + hh)) * SK * DH;
  const u16* Vh = (blk ? V1 : V0) + ((size_t)(b * NH + hh)) * SK * DH;
  const int tid = threadIdx.x, wid = tid >> 6, lane = tid & 63, r = lane & 31, h = lane >> 5;
  const size_t qrow = (size_t)blk * NB * SQ + b * SQ + wid * 32 + r;
  bf16x8 qf[8];
#pragma unroll
  for (int s = 0; s < 8; ++s) qf[s] = *(const bf16x8*)&Q[qrow * HD + hh * DH + 16 * s + 8 * h];
  f32x16 o[4] = {};
  float m = -1e30f, l = 0.f;
  const unsigned vbase = (unsigned)(size_t)Vs + 64 * (4 * h + ((lane & 15) >> 2)) + 32 * ((lane >> 4) & 1) + 8 * (lane & 3);
  for (int t = 0; t < SK / 32; ++t) {
    __syncthreads();
    for (int c = tid; c < 512; c += 384) {
      int row = c >> 4, ch = c & 15;
      bf16x8 kv = *(const bf16x8*)&Kh[(size_t)(t * 32 + row) * DH + ch * 8];
      bf16x8 vv = *(const bf16x8*)&Vh[(size_t)(t * 32 + row) * DH + ch * 8];
      *(bf16x8*)(Ks + 2048 * (row >> 3) + 512 * (ch >> 2) + 64 * (row & 7) + 16 * ((ch & 3) ^ ((row >> 2) & 3))) = kv;
      *(bf16x8*)(Vs + 2048 * (row >> 3) + 512 * (ch >> 2) + 64 * (row & 7) + 16 * (ch & 3)) = vv;
    }
    __syncthreads();
    f32x16 s_ = {};
#pragma unroll
    for (int s = 0; s < 8; ++s) {
      int ch = 2 * s + h;
      bf16x8 kf = *(const bf16x8*)(Ks + 2048 * (r >> 3) + 512 * (ch >> 2) + 64 * (r & 7) + 16 * ((ch & 3) ^ ((r >> 2) & 3)));
      s_ = MFMA(kf, qf[s], s_);
    }
    float mx = s_[0];
#pragma unroll
    for (int i = 1; i < 16; ++i) mx = fmaxf(mx, s_[i]);
    mx = fmaxf(mx, __shfl_xor(mx, 32));
    float mn = fmaxf(m, mx), alpha = exp2f(m - mn); m = mn;
    float ps = 0.f;
#pragma unroll
    for (int i = 0; i < 16; ++i) { s_[i] = exp2f(s_[i] - mn); ps += s_[i]; }
    l = l * alpha + ps;
#pragma unroll
    for (int d = 0; d < 4; ++d)
#pragma unroll
      for (int i = 0; i < 16; ++i) o[d][i] *= alpha;
    bf16x8 pf[2];
#pragma unroll
    for (int s = 0; s < 2; ++s) { u32x4 w = {cvtpk(s_[8*s], s_[8*s+1]), cvtpk(s_[8*s+2], s_[8*s+3]), cvtpk(s_[8*s+4], s_[8*s+5]), cvtpk(s_[8*s+6], s_[8*s+7])}; pf[s] = __builtin_bit_cast(bf16x8, w); }
#pragma unroll
    for (int d = 0; d < 4; ++d)
#pragma unroll
      for (int s = 0; s < 2; ++s) {
        s16x4 lo, hi;
        asm volatile("ds_read_b64_tr_b16 %0, %2 offset:%3\n\tds_read_b64_tr_b16 %1, %2 offset:%4\n\ts_waitcnt lgkmcnt(0)"
                     : "=&v"(lo), "=&v"(hi) : "v"(vbase), "i"(4096 * s + 512 * d), "i"(4096 * s + 2048 + 512 * d) : "memory");
        bf16x8 vf = __builtin_shufflevector(lo, hi, 0, 1, 2, 3, 4, 5, 6, 7);
        o[d] = MFMA(vf, pf[s], o[d]);
      }
  }
  l += __shfl_xor(l, 32);
  float il = 1.f / l;
#pragma unroll
  for (int d = 0; d < 4; ++d)
#pragma unroll
    for (int i = 0; i < 16; ++i) O[qrow * HD + hh * DH + 32 * d + crow(i, h)] = tobf(o[d][i] * il);
}

extern "C" void kernel_launch(void* const* d_in, const int* in_sizes, int n_in, void* d_out, int out_size, void* d_ws, size_t ws_size, hipStream_t stream) {
  const u16* xA = (const u16*)d_in[0]; const u16* kA = (const u16*)d_in[1]; const u16* vA = (const u16*)d_in[2]; const u16* wqA = (const u16*)d_in[3]; const u16* woA = (const u16*)d_in[4];
  const u16* xB = (const u16*)d_in[5]; const u16* kB = (const u16*)d_in[6]; const u16* vB = (const u16*)d_in[7]; const u16* wqB = (const u16*)d_in[8]; const u16* woB = (const u16*)d_in[9];
  u16* q = (u16*)d_ws; u16* o = q + (size_t)2 * NB * SQ * HD; u16* out = (u16*)d_out;
  const size_t half = (size_t)NB * SQ * HD;
  const float c = 0.088388347648318440f * 1.4426950408889634f;
  gemm_simple<<<dim3(HD / 64, NB * SQ / 64, 2), 256, 0, stream>>>(xA, wqA, q, xB, wqB, q + half, NB * SQ, HD, DM, HD, c);
  attn_simple<<<512, 384, 0, stream>>>(q, kA, vA, kB, vB, o);
  gemm_simple<<<dim3(DM / 64, NB * SQ / 64, 2), 256, 0, stream>>>(o, woA, out, o + half, woB, out + DM, NB * SQ, DM, HD, 2 * DM, 1.f);
}
```

```cpp
#include <hip/hip_runtime.h>

__global__ __launch_bounds__(256, 1) void k_fused(const char* __restrict__ xA, const char* __restrict__ kA, const char* __restrict__ vA, const char* __restrict__ wqA, const char* __restrict__ woA,
                                                  const char* __restrict__ xB, const char* __restrict__ kB, const char* __restrict__ vB, const char* __restrict__ wqB, const char* __restrict__ woB,
                                                  char* __restrict__ ws, char* __restrict__ out) {
  __shared__ __attribute__((aligned(1024))) char lds[163840];
  const unsigned wid = __builtin_amdgcn_readfirstlane(threadIdx.x >> 6);
  const unsigned ldsb = (unsigned)(size_t)lds;
  asm volatile(
"s_bfe_u32 s27, %1, 0x10002\n"
"s_mul_i32 s28, s27, 40\n"
"s_load_dwordx2 s[40:41], %0, s28\n"
"s_add_u32 s29, s28, 24\n"
"s_load_dwordx2 s[44:45], %0, s29\n"
"s_add_u32 s29, s28, 8\n"
"s_load_dwordx4 s[8:11], %0, s29\n"
"s_add_u32 s28, s28, 32\n"
"s_load_dwordx2 s[88:89], %0, s28\n"
"s_load_dwordx4 s[20:23], %0, 0x50\n"
"s_memrealtime s[86:87]\n"
"v_mbcnt_lo_u32_b32 v255, -1, 0\n"
"v_mbcnt_hi_u32_b32 v255, -1, v255\n"
"s_mov_b32 s67, %2\n"
"s_getpc_b64 s[28:29]\n"
".Lk1_pc0:\n"
"s_and_b32 s29, s29, 0xffff\n"
"s_mov_b32 s30, .Lk1_rare0-.Lk1_pc0\n"
"s_mov_b32 s31, 0x20000\n"
"s_bfe_u32 s71, %1, 0x40003\n"
"s_lshl_b32 s71, s71, 2\n"
"s_add_u32 s71, s71, s67\n"
"s_lshl_b32 s71, s71, 10\n"
"v_lshl_add_u32 v232, v255, 4, s71\n"
"buffer_load_dword v233, v232, s[28:31], 0 offen\n"
"s_mov_b32 s66, %3\n"
"s_and_b32 s64, s67, 1\n"
"s_lshr_b32 s65, s67, 1\n"
"s_and_b32 s84, %1, 3\n"
"s_bfe_u32 s83, %1, 0x20003\n"
"s_lshr_b32 s24, %1, 5\n"
"s_lshl_b32 s25, s84, 2\n"
"s_add_u32 s25, s25, s83\n"
"s_mul_i32 s36, s24, 196608\n"
"s_mov_b32 s42, 196608\n"
"s_mov_b32 s43, 0x20000\n"
"s_andn2_b32 s71, 1, s83\n"
"s_xor_b32 s71, s71, s67\n"
"s_mul_i32 s71, s71, 6144\n"
"s_add_u32 s69, s71, s66\n"
"s_lshl_b32 s37, s25, 9\n"
"s_lshl_b32 s71, s65, 8\n"
"s_add_u32 s37, s37, s71\n"
"s_sub_u32 s46, 0x400400, s37\n"
"s_mov_b32 s47, 0x20000\n"
"s_lshl_b32 s71, s65, 13\n"
"s_lshl_b32 s72, s64, 12\n"
"s_add_u32 s68, s71, s72\n"
"s_add_u32 s68, s68, s66\n"
"v_and_b32 v216, 3, v255\n"
"v_bfe_u32 v217, v255, 2, 3\n"
"v_lshrrev_b32 v218, 5, v255\n"
"v_lshlrev_b32 v219, 4, v216\n"
"v_lshl_add_u32 v219, v218, 6, v219\n"
"s_mul_i32 s71, s67, 49152\n"
"v_lshl_add_u32 v222, v217, 10, v219\n"
"v_add_u32 v222, s71, v222\n"
"v_xor_b32 v223, 48, v222\n"
"v_add_u32 v192, 0x0, v222\n"
"v_add_u32 v193, 0x1c00, v223\n"
"v_add_u32 v194, 0x3800, v222\n"
"v_add_u32 v195, 0x5400, v223\n"
"v_add_u32 v196, 0x8000, v222\n"
"v_add_u32 v197, 0x9c00, v223\n"
"v_lshl_add_u32 v222, v217, 13, v219\n"
"s_lshl_b32 s71, s64, 17\n"
"v_add_u32 v222, s71, v222\n"
"v_xor_b32 v223, 48, v222\n"
"v_add_u32 v200, 0x400, v222\n"
"v_add_u32 v201, 0x80, v222\n"
"v_add_u32 v202, 0xfc00, v223\n"
"v_add_u32 v203, 0xf880, v223\n"
"s_waitcnt lgkmcnt(0)\n"
"s_mov_b32 s87, s86\n"
"s_add_u32 s40, s40, s36\n"
"s_addc_u32 s41, s41, 0\n"
"s_and_b32 s41, s41, 0xffff\n"
"s_mov_b32 s70, 0\n"
"s_add_u32 m0, s69, 0x10000\n"
"s_nop 0\n"
"buffer_load_dwordx4 v192, s[40:43], s70 offen offset:0 lds\n"
"s_add_u32 s44, s44, s37\n"
"s_addc_u32 s45, s45, 0\n"
"s_sub_u32 s44, s44, 1024\n"
"s_subb_u32 s45, s45, 0\n"
"s_and_b32 s45, s45, 0xffff\n"
"buffer_load_dwordx4 v193, s[40:43], s70 offen offset:1024 lds\n"
"buffer_load_dwordx4 v194, s[40:43], s70 offen offset:2048 lds\n"
"buffer_load_dwordx4 v195, s[40:43], s70 offen offset:3072 lds\n"
"s_add_u32 m0, s69, 0x11000\n"
"s_nop 0\n"
"buffer_load_dwordx4 v196, s[40:43], s70 offen offset:0 lds\n"
"buffer_load_dwordx4 v197, s[40:43], s70 offen offset:1024 lds\n"
"s_add_u32 m0, s68, 0x20000\n"
"s_mov_b32 s75, 0x0\n"
"buffer_load_dwordx4 v200, s[44:47], s75 offen offset:0 lds\n"
"buffer_load_dwordx4 v201, s[44:47], s75 offen offset:1024 lds\n"
"buffer_load_dwordx4 v202, s[44:47], s75 offen offset:2048 lds\n"
"buffer_load_dwordx4 v203, s[44:47], s75 offen offset:3072 lds\n"
"s_add_u32 m0, s68, 0x24000\n"
"s_mov_b32 s75, 0x40000\n"
"s_lshl_b32 s85, s27, 3\n"
"buffer_load_dwordx4 v200, s[44:47], s75 offen offset:0 lds\n"
"s_add_u32 s85, s85, s24\n"
"s_lshl_b32 s86, s85, 4\n"
"s_add_u32 s86, s86, s25\n"
"s_mul_i32 s71, s85, 0x300000\n"
"s_mul_i32 s72, s67, 3072\n"
"s_add_u32 s71, s71, s72\n"
"s_add_u32 s71, s71, 0x4800000\n"
"s_add_u32 s48, s20, s71\n"
"s_addc_u32 s49, s21, 0\n"
"s_lshl_b32 s72, s25, 1\n"
"s_lshl_b32 s71, s24, 5\n"
"s_add_u32 s71, s71, s72\n"
"s_lshl_b32 s71, s71, 18\n"
"s_add_u32 s28, s8, s71\n"
"s_addc_u32 s29, s9, 0\n"
"s_add_u32 s36, s10, s71\n"
"s_addc_u32 s37, s11, 0\n"
"s_andn2_b32 s71, s25, 1\n"
"buffer_load_dwordx4 v201, s[44:47], s75 offen offset:1024 lds\n"
"s_lshl_b32 s71, s71, 18\n"
"s_add_u32 s88, s88, s71\n"
"s_addc_u32 s89, s89, 0\n"
"s_lshl_b32 s71, s24, 2\n"
"s_lshl_b32 s73, s64, 1\n"
"s_add_u32 s71, s71, s73\n"
"s_and_b32 s73, s25, 1\n"
"s_add_u32 s71, s71, s73\n"
"s_mul_i32 s71, s71, 0x18000\n"
"s_lshl_b32 s73, s27, 10\n"
"s_add_u32 s71, s71, s73\n"
"s_andn2_b32 s73, s25, 1\n"
"s_add_u32 s73, s73, s65\n"
"s_lshl_b32 s73, s73, 6\n"
"s_add_u32 s71, s71, s73\n"
"s_add_u32 s90, s22, s71\n"
"s_addc_u32 s91, s23, 0\n"
"s_mov_b64 s[92:93], s[20:21]\n"
"buffer_load_dwordx4 v202, s[44:47], s75 offen offset:2048 lds\n"
"s_add_u32 s94, s25, 2\n"
"s_lshl_b32 s94, s94, 6\n"
"s_and_b32 s96, s25, 1\n"
"s_lshl_b32 s96, s96, 18\n"
"s_xor_b32 s97, s96, 0x40000\n"
"s_lshl_b32 s71, s65, 18\n"
"s_lshl_b32 s72, s64, 13\n"
"s_add_u32 s71, s71, s72\n"
"s_add_u32 s28, s28, s71\n"
"s_addc_u32 s29, s29, 0\n"
"s_sub_u32 s28, s28, 1024\n"
"s_subb_u32 s29, s29, 0\n"
"s_and_b32 s29, s29, 0xffff\n"
"s_sub_u32 s30, 263168, s72\n"
"s_mov_b32 s31, 0x20000\n"
"s_add_u32 s36, s36, s71\n"
"s_addc_u32 s37, s37, 0\n"
"s_sub_u32 s36, s36, 1024\n"
"s_subb_u32 s37, s37, 0\n"
"s_and_b32 s37, s37, 0xffff\n"
"buffer_load_dwordx4 v203, s[44:47], s75 offen offset:3072 lds\n"
"s_mov_b32 s38, s30\n"
"s_mov_b32 s39, 0x20000\n"
"s_and_b32 s49, s49, 0xffff\n"
"s_mov_b32 s50, 0x2cdc00\n"
"s_mov_b32 s51, 0x20000\n"
"s_add_u32 s52, s92, 0x2000000\n"
"s_addc_u32 s53, s93, 0\n"
"s_and_b32 s53, s53, 0xffff\n"
"s_mov_b32 s54, 0x20000\n"
"s_mov_b32 s55, 0x20000\n"
"s_lshl_b32 s71, s65, 14\n"
"s_add_u32 s59, s66, s71\n"
"s_add_u32 s59, s59, s72\n"
"s_add_u32 s81, s59, 0x28000\n"
"s_lshl_b32 s86, s86, 2\n"
"s_add_u32 s86, s86, s67\n"
"s_lshl_b32 s86, s86, 7\n"
"v_mov_b32 v231, 0\n"
"s_mov_b32 s70, 128\n"
"s_add_u32 m0, s69, 0x8000\n"
"buffer_load_dword v229, v231, s[52:55], s86 offen sc1\n"
"buffer_load_dwordx4 v192, s[40:43], s70 offen offset:0 lds\n"
"v_lshl_add_u32 v222, v217, 8, v219\n"
"v_xor_b32 v223, 48, v222\n"
"v_add_u32 v246, 1024, v222\n"
"v_add_u32 v247, 128, v222\n"
"v_add_u32 v248, 1024, v223\n"
"v_add_u32 v249, 128, v223\n"
"v_and_b32 v244, 7, v255\n"
"v_lshrrev_b32 v220, 4, v255\n"
"v_bfe_u32 v221, v255, 3, 1\n"
"v_mul_u32_u24 v222, 3, v221\n"
"v_xor_b32 v222, v222, v220\n"
"v_lshlrev_b32 v222, 4, v222\n"
"v_lshl_add_u32 v222, v244, 6, v222\n"
"v_lshl_add_u32 v222, v221, 10, v222\n"
"s_mul_i32 s71, s64, 12288\n"
"buffer_load_dwordx4 v193, s[40:43], s70 offen offset:1024 lds\n"
"s_add_u32 s71, s71, s66\n"
"s_add_u32 s71, s71, 0x10000\n"
"v_add_u32 v198, s71, v222\n"
"v_subrev_u32 v199, 0x8000, v198\n"
"v_bfe_u32 v223, v255, 2, 2\n"
"v_and_b32 v224, 1, v220\n"
"v_mul_u32_u24 v222, 48, v224\n"
"v_lshlrev_b32 v225, 3, v216\n"
"v_xor_b32 v222, v222, v225\n"
"v_lshl_add_u32 v222, v223, 6, v222\n"
"v_lshl_add_u32 v222, v220, 11, v222\n"
"s_lshl_b32 s71, s65, 13\n"
"s_add_u32 s71, s71, s66\n"
"v_add_u32 v204, s71, v222\n"
"v_xor_b32 v207, 32, v204\n"
"v_add_u32 v205, 0x10000, v204\n"
"buffer_load_dwordx4 v194, s[40:43], s70 offen offset:2048 lds\n"
"v_add_u32 v206, 0x20000, v204\n"
"v_add_u32 v208, 0x10000, v207\n"
"v_add_u32 v209, 0x20000, v207\n"
"s_lshl_b32 s71, s65, 14\n"
"s_add_u32 s71, s71, s66\n"
"s_add_u32 s72, s71, 0x8000\n"
"v_add_u32 v241, s72, v222\n"
"v_xor_b32 v242, 32, v241\n"
"v_lshl_add_u32 v222, v224, 1, v218\n"
"v_and_b32 v226, 1, v223\n"
"v_mul_u32_u24 v226, 3, v226\n"
"v_xor_b32 v222, v222, v226\n"
"v_lshlrev_b32 v222, 4, v222\n"
"v_lshl_add_u32 v222, v216, 6, v222\n"
"v_lshl_add_u32 v222, v223, 11, v222\n"
"v_add_u32 v240, s71, v222\n"
"v_bfe_u32 v222, v255, 3, 2\n"
"buffer_load_dwordx4 v195, s[40:43], s70 offen offset:3072 lds\n"
"s_lshl_b32 s71, s65, 1\n"
"v_xor_b32 v222, s71, v222\n"
"v_lshl_add_u32 v222, v222, 5, v225\n"
"v_lshrrev_b32 v226, 2, v255\n"
"v_lshl_add_u32 v243, v226, 7, v222\n"
"v_min_u32 v222, 7, v255\n"
"s_lshr_b32 s71, s94, 6\n"
"v_lshl_add_u32 v222, v222, 1, s71\n"
"v_and_b32 v222, 15, v222\n"
"s_andn2_b32 s71, s86, 0x1e00\n"
"v_lshl_add_u32 v245, v222, 9, s71\n"
"s_xor_b32 s71, s86, 0x200\n"
"s_xor_b32 s72, s86, 0x300\n"
"v_writelane_b32 v245, s71, 8\n"
"v_writelane_b32 v245, s72, 9\n"
"v_and_b32 v226, 15, v255\n"
"s_add_u32 m0, s69, 0x9000\n"
"v_lshl_add_u32 v253, v226, 2, v245\n"
"buffer_load_dwordx4 v196, s[40:43], s70 offen offset:0 lds\n"
"v_lshlrev_b32 v252, 4, v255\n"
"s_mov_b32 s76, 0x3e0293ee\n"
"s_mov_b32 s60, 0x40004000\n"
"s_mov_b32 s79, 0x10000\n"
"s_mov_b32 s80, 0xfffe8000\n"
"s_mov_b32 s77, 0\n"
"s_mov_b32 s78, 0x8000\n"
"s_lshr_b32 s71, s86, 9\n"
"s_lshr_b32 s72, s71, 4\n"
"s_mul_i32 s72, s72, 5\n"
"s_add_u32 s71, s71, s72\n"
"s_lshl_b32 s72, s65, 3\n"
"s_add_u32 s71, s71, s72\n"
"s_and_b32 s71, s71, 15\n"
"s_lshl_b32 s56, s71, 14\n"
"buffer_load_dwordx4 v197, s[40:43], s70 offen offset:1024 lds\n"
"s_mov_b32 s57, s56\n"
"s_add_u32 s71, s24, s83\n"
"s_mul_i32 s72, s71, 11\n"
"s_lshr_b32 s72, s72, 5\n"
"s_mul_i32 s72, s72, 3\n"
"s_sub_u32 s71, s71, s72\n"
"s_lshl_b32 s71, s71, 2\n"
"s_mov_b32 s33, s71\n"
"s_lshr_b32 s71, 0x2132100, s33\n"
"s_and_b32 s22, s71, 0x300\n"
"s_lshl_b32 s25, s22, 12\n"
"s_lshr_b32 s71, s71, 4\n"
"s_and_b32 s23, s71, 0x300\n"
"s_lshr_b32 s71, s71, 4\n"
"s_and_b32 s24, s71, 0x300\n"
"s_lshl_b32 s26, s23, 12\n"
"s_lshl_b32 s27, s24, 12\n"
"s_waitcnt vmcnt(11) lgkmcnt(0)\n"
"s_barrier\n"
"ds_read_b64_tr_b16 a[0:1], v206 offset:0\n"
"ds_read_b64_tr_b16 a[2:3], v206 offset:256\n"
"ds_read_b64_tr_b16 a[4:5], v209 offset:0\n"
"ds_read_b64_tr_b16 a[6:7], v209 offset:256\n"
"ds_read_b64_tr_b16 a[8:9], v206 offset:512\n"
"ds_read_b64_tr_b16 a[10:11], v206 offset:768\n"
"ds_read_b64_tr_b16 a[12:13], v209 offset:512\n"
"ds_read_b64_tr_b16 a[14:15], v209 offset:768\n"
"ds_read_b64_tr_b16 a[16:17], v206 offset:1024\n"
"ds_read_b64_tr_b16 a[18:19], v206 offset:1280\n"
"ds_read_b64_tr_b16 a[20:21], v209 offset:1024\n"
"ds_read_b64_tr_b16 a[22:23], v209 offset:1280\n"
"ds_read_b64_tr_b16 a[24:25], v206 offset:1536\n"
"ds_read_b64_tr_b16 a[26:27], v206 offset:1792\n"
"ds_read_b64_tr_b16 a[28:29], v209 offset:1536\n"
"ds_read_b64_tr_b16 a[30:31], v209 offset:1792\n"
"ds_read_b128 a[64:67], v198 offset:0\n"
"ds_read_b128 a[68:71], v198 offset:2048\n"
"ds_read_b128 a[72:75], v198 offset:4096\n"
"ds_read_b128 a[76:79], v198 offset:6144\n"
"ds_read_b128 a[80:83], v198 offset:8192\n"
"ds_read_b128 a[84:87], v198 offset:10240\n"
"ds_read_b128 a[88:91], v198 offset:512\n"
"ds_read_b128 a[92:95], v198 offset:2560\n"
"ds_read_b128 a[96:99], v198 offset:4608\n"
"ds_read_b128 a[100:103], v198 offset:6656\n"
"ds_read_b128 a[104:107], v198 offset:8704\n"
"ds_read_b128 a[108:111], v198 offset:10752\n"
"s_add_u32 m0, s68, 0x0\n"
"s_mov_b32 s75, 0x80000\n"
"buffer_load_dwordx4 v200, s[44:47], s75 offen offset:0 lds\n"
"buffer_load_dwordx4 v201, s[44:47], s75 offen offset:1024 lds\n"
"buffer_load_dwordx4 v202, s[44:47], s75 offen offset:2048 lds\n"
"buffer_load_dwordx4 v203, s[44:47], s75 offen offset:3072 lds\n"
"s_waitcnt vmcnt(11) lgkmcnt(0)\n"
"s_barrier\n"
"v_mfma_f32_16x16x32_bf16 v[0:3], a[0:3], a[64:67], 0\n"
"s_add_u32 m0, s68, 0x4000\n"
"s_mov_b32 s75, 0xc0000\n"
"ds_read_b64_tr_b16 a[32:33], v206 offset:16384\n"
"v_mfma_f32_16x16x32_bf16 v[32:35], a[0:3], a[68:71], 0\n"
"ds_read_b64_tr_b16 a[34:35], v206 offset:16640\n"
"v_mfma_f32_16x16x32_bf16 v[64:67], a[0:3], a[72:75], 0\n"
"ds_read_b64_tr_b16 a[36:37], v209 offset:16384\n"
"buffer_load_dwordx4 v200, s[44:47], s75 offen offset:0 lds\n"
"v_mfma_f32_16x16x32_bf16 v[96:99], a[0:3], a[76:79], 0\n"
"ds_read_b64_tr_b16 a[38:39], v209 offset:16640\n"
"v_mfma_f32_16x16x32_bf16 v[128:131], a[0:3], a[80:83], 0\n"
"ds_read_b64_tr_b16 a[40:41], v206 offset:16896\n"
"v_mfma_f32_16x16x32_bf16 v[160:163], a[0:3], a[84:87], 0\n"
"ds_read_b64_tr_b16 a[42:43], v206 offset:17152\n"
"buffer_load_dwordx4 v201, s[44:47], s75 offen offset:1024 lds\n"
"v_mfma_f32_16x16x32_bf16 v[4:7], a[4:7], a[64:67], 0\n"
"ds_read_b64_tr_b16 a[44:45], v209 offset:16896\n"
"v_mfma_f32_16x16x32_bf16 v[36:39], a[4:7], a[68:71], 0\n"
"ds_read_b64_tr_b16 a[46:47], v209 offset:17152\n"
"v_mfma_f32_16x16x32_bf16 v[68:71], a[4:7], a[72:75], 0\n"
"ds_read_b64_tr_b16 a[48:49], v206 offset:17408\n"
"buffer_load_dwordx4 v202, s[44:47], s75 offen offset:2048 lds\n"
"v_mfma_f32_16x16x32_bf16 v[100:103], a[4:7], a[76:79], 0\n"
"ds_read_b64_tr_b16 a[50:51], v206 offset:17664\n"
"v_mfma_f32_16x16x32_bf16 v[132:135], a[4:7], a[80:83], 0\n"
"ds_read_b64_tr_b16 a[52:53], v209 offset:17408\n"
"v_mfma_f32_16x16x32_bf16 v[164:167], a[4:7], a[84:87], 0\n"
"ds_read_b64_tr_b16 a[54:55], v209 offset:17664\n"
"buffer_load_dwordx4 v203, s[44:47], s75 offen offset:3072 lds\n"
"v_mfma_f32_16x16x32_bf16 v[8:11], a[8:11], a[64:67], 0\n"
"s_add_u32 s70, s22, 0\n"
"s_add_u32 m0, s69, 0x1a000\n"
"ds_read_b64_tr_b16 a[56:57], v206 offset:17920\n"
"v_mfma_f32_16x16x32_bf16 v[40:43], a[8:11], a[68:71], 0\n"
"ds_read_b64_tr_b16 a[58:59], v206 offset:18176\n"
"v_mfma_f32_16x16x32_bf16 v[72:75], a[8:11], a[72:75], 0\n"
"ds_read_b64_tr_b16 a[60:61], v209 offset:17920\n"
"buffer_load_dwordx4 v192, s[40:43], s70 offen offset:0 lds\n"
"v_mfma_f32_16x16x32_bf16 v[104:107], a[8:11], a[76:79], 0\n"
"ds_read_b64_tr_b16 a[62:63], v209 offset:18176\n"
"v_mfma_f32_16x16x32_bf16 v[136:139], a[8:11], a[80:83], 0\n"
"v_mfma_f32_16x16x32_bf16 v[168:171], a[8:11], a[84:87], 0\n"
"buffer_load_dwordx4 v193, s[40:43], s70 offen offset:1024 lds\n"
"v_mfma_f32_16x16x32_bf16 v[12:15], a[12:15], a[64:67], 0\n"
"v_mfma_f32_16x16x32_bf16 v[44:47], a[12:15], a[68:71], 0\n"
"v_mfma_f32_16x16x32_bf16 v[76:79], a[12:15], a[72:75], 0\n"
"buffer_load_dwordx4 v194, s[40:43], s70 offen offset:2048 lds\n"
"v_mfma_f32_16x16x32_bf16 v[108:111], a[12:15], a[76:79], 0\n"
"v_mfma_f32_16x16x32_bf16 v[140:143], a[12:15], a[80:83], 0\n"
"v_mfma_f32_16x16x32_bf16 v[172:175], a[12:15], a[84:87], 0\n"
"v_mfma_f32_16x16x32_bf16 v[16:19], a[16:19], a[64:67], 0\n"
"buffer_load_dwordx4 v195, s[40:43], s70 offen offset:3072 lds\n"
"v_mfma_f32_16x16x32_bf16 v[48:51], a[16:19], a[68:71], 0\n"
"s_add_u32 m0, s69, 0x1b000\n"
"v_mfma_f32_16x16x32_bf16 v[80:83], a[16:19], a[72:75], 0\n"
"v_mfma_f32_16x16x32_bf16 v[112:115], a[16:19], a[76:79], 0\n"
"buffer_load_dwordx4 v196, s[40:43], s70 offen offset:0 lds\n"
"v_mfma_f32_16x16x32_bf16 v[144:147], a[16:19], a[80:83], 0\n"
"v_mfma_f32_16x16x32_bf16 v[176:179], a[16:19], a[84:87], 0\n"
"v_mfma_f32_16x16x32_bf16 v[20:23], a[20:23], a[64:67], 0\n"
"buffer_load_dwordx4 v197, s[40:43], s70 offen offset:1024 lds\n"
"v_mfma_f32_16x16x32_bf16 v[52:55], a[20:23], a[68:71], 0\n"
"s_add_u32 m0, s68, 0x16000\n"
"s_add_u32 s75, s25, 0x0\n"
"v_mfma_f32_16x16x32_bf16 v[84:87], a[20:23], a[72:75], 0\n"
"v_mfma_f32_16x16x32_bf16 v[116:119], a[20:23], a[76:79], 0\n"
"buffer_load_dwordx4 v200, s[44:47], s75 offen offset:0 lds\n"
"v_mfma_f32_16x16x32_bf16 v[148:151], a[20:23], a[80:83], 0\n"
"v_mfma_f32_16x16x32_bf16 v[180:183], a[20:23], a[84:87], 0\n"
"v_mfma_f32_16x16x32_bf16 v[24:27], a[24:27], a[64:67], 0\n"
"buffer_load_dwordx4 v201, s[44:47], s75 offen offset:1024 lds\n"
"v_mfma_f32_16x16x32_bf16 v[56:59], a[24:27], a[68:71], 0\n"
"v_mfma_f32_16x16x32_bf16 v[88:91], a[24:27], a[72:75], 0\n"
"v_mfma_f32_16x16x32_bf16 v[120:123], a[24:27], a[76:79], 0\n"
"buffer_load_dwordx4 v202, s[44:47], s75 offen offset:2048 lds\n"
"v_mfma_f32_16x16x32_bf16 v[152:155], a[24:27], a[80:83], 0\n"
"v_mfma_f32_16x16x32_bf16 v[184:187], a[24:27], a[84:87], 0\n"
"v_mfma_f32_16x16x32_bf16 v[28:31], a[28:31], a[64:67], 0\n"
"buffer_load_dwordx4 v203, s[44:47], s75 offen offset:3072 lds\n"
"v_mfma_f32_16x16x32_bf16 v[60:63], a[28:31], a[68:71], 0\n"
"v_mfma_f32_16x16x32_bf16 v[92:95], a[28:31], a[72:75], 0\n"
"v_mfma_f32_16x16x32_bf16 v[124:127], a[28:31], a[76:79], 0\n"
"v_mfma_f32_16x16x32_bf16 v[156:159], a[28:31], a[80:83], 0\n"
"v_mfma_f32_16x16x32_bf16 v[188:191], a[28:31], a[84:87], 0\n"
"s_waitcnt vmcnt(14) lgkmcnt(0)\n"
"s_barrier\n"
"v_mfma_f32_16x16x32_bf16 v[0:3], a[32:35], a[88:91], v[0:3]\n"
"s_add_u32 s70, s22, 128\n"
"s_add_u32 m0, s69, 0x10000\n"
"ds_read_b64_tr_b16 a[0:1], v204 offset:0\n"
"v_mfma_f32_16x16x32_bf16 v[32:35], a[32:35], a[92:95], v[32:35]\n"
"ds_read_b64_tr_b16 a[2:3], v204 offset:256\n"
"v_mfma_f32_16x16x32_bf16 v[64:67], a[32:35], a[96:99], v[64:67]\n"
"ds_read_b64_tr_b16 a[4:5], v207 offset:0\n"
"buffer_load_dwordx4 v192, s[40:43], s70 offen offset:0 lds\n"
"v_mfma_f32_16x16x32_bf16 v[96:99], a[32:35], a[100:103], v[96:99]\n"
"ds_read_b64_tr_b16 a[6:7], v207 offset:256\n"
"v_mfma_f32_16x16x32_bf16 v[128:131], a[32:35], a[104:107], v[128:131]\n"
"ds_read_b64_tr_b16 a[8:9], v204 offset:512\n"
"v_mfma_f32_16x16x32_bf16 v[160:163], a[32:35], a[108:111], v[160:163]\n"
"ds_read_b64_tr_b16 a[10:11], v204 offset:768\n"
"v_mfma_f32_16x16x32_bf16 v[4:7], a[36:39], a[88:91], v[4:7]\n"
"ds_read_b64_tr_b16 a[12:13], v207 offset:512\n"
"buffer_load_dwordx4 v193, s[40:43], s70 offen offset:1024 lds\n"
"v_mfma_f32_16x16x32_bf16 v[36:39], a[36:39], a[92:95], v[36:39]\n"
"ds_read_b64_tr_b16 a[14:15], v207 offset:768\n"
"v_mfma_f32_16x16x32_bf16 v[68:71], a[36:39], a[96:99], v[68:71]\n"
"ds_read_b64_tr_b16 a[16:17], v204 offset:1024\n"
"v_mfma_f32_16x16x32_bf16 v[100:103], a[36:39], a[100:103], v[100:103]\n"
"ds_read_b64_tr_b16 a[18:19], v204 offset:1280\n"
"v_mfma_f32_16x16x32_bf16 v[132:135], a[36:39], a[104:107], v[132:135]\n"
"ds_read_b64_tr_b16 a[20:21], v207 offset:1024\n"
"buffer_load_dwordx4 v194, s[40:43], s70 offen offset:2048 lds\n"
"v_mfma_f32_16x16x32_bf16 v[164:167], a[36:39], a[108:111], v[164:167]\n"
"ds_read_b64_tr_b16 a[22:23], v207 offset:1280\n"
"v_mfma_f32_16x16x32_bf16 v[8:11], a[40:43], a[88:91], v[8:11]\n"
"ds_read_b64_tr_b16 a[24:25], v204 offset:1536\n"
"v_mfma_f32_16x16x32_bf16 v[40:43], a[40:43], a[92:95], v[40:43]\n"
"ds_read_b64_tr_b16 a[26:27], v204 offset:1792\n"
"v_mfma_f32_16x16x32_bf16 v[72:75], a[40:43], a[96:99], v[72:75]\n"
"ds_read_b64_tr_b16 a[28:29], v207 offset:1536\n"
"buffer_load_dwordx4 v195, s[40:43], s70 offen offset:3072 lds\n"
"v_mfma_f32_16x16x32_bf16 v[104:107], a[40:43], a[100:103], v[104:107]\n"
"s_add_u32 m0, s69, 0x11000\n"
"ds_read_b64_tr_b16 a[30:31], v207 offset:1792\n"
"v_mfma_f32_16x16x32_bf16 v[136:139], a[40:43], a[104:107], v[136:139]\n"
"ds_read_b128 a[112:115], v199 offset:0\n"
"v_mfma_f32_16x16x32_bf16 v[168:171], a[40:43], a[108:111], v[168:171]\n"
"ds_read_b128 a[116:119], v199 offset:2048\n"
"v_mfma_f32_16x16x32_bf16 v[12:15], a[44:47], a[88:91], v[12:15]\n"
"ds_read_b128 a[120:123], v199 offset:4096\n"
"buffer_load_dwordx4 v196, s[40:43], s70 offen offset:0 lds\n"
"v_mfma_f32_16x16x32_bf16 v[44:47], a[44:47], a[92:95], v[44:47]\n"
"ds_read_b128 a[124:127], v199 offset:6144\n"
"v_mfma_f32_16x16x32_bf16 v[76:79], a[44:47], a[96:99], v[76:79]\n"
"ds_read_b128 a[128:131], v199 offset:8192\n"
"v_mfma_f32_16x16x32_bf16 v[108:111], a[44:47], a[100:103], v[108:111]\n"
"ds_read_b128 a[132:135], v199 offset:10240\n"
"v_mfma_f32_16x16x32_bf16 v[140:143], a[44:47], a[104:107], v[140:143]\n"
"ds_read_b128 a[136:139], v199 offset:512\n"
"buffer_load_dwordx4 v197, s[40:43], s70 offen offset:1024 lds\n"
"v_mfma_f32_16x16x32_bf16 v[172:175], a[44:47], a[108:111], v[172:175]\n"
"s_add_u32 m0, s68, 0x20000\n"
"s_add_u32 s75, s25, 0x40000\n"
"ds_read_b128 a[140:143], v199 offset:2560\n"
"v_mfma_f32_16x16x32_bf16 v[16:19], a[48:51], a[88:91], v[16:19]\n"
"ds_read_b128 a[144:147], v199 offset:4608\n"
"v_mfma_f32_16x16x32_bf16 v[48:51], a[48:51], a[92:95], v[48:51]\n"
"ds_read_b128 a[148:151], v199 offset:6656\n"
"v_mfma_f32_16x16x32_bf16 v[80:83], a[48:51], a[96:99], v[80:83]\n"
"ds_read_b128 a[152:155], v199 offset:8704\n"
"buffer_load_dwordx4 v200, s[44:47], s75 offen offset:0 lds\n"
"v_mfma_f32_16x16x32_bf16 v[112:115], a[48:51], a[100:103], v[112:115]\n"
"ds_read_b128 a[156:159], v199 offset:10752\n"
"v_mfma_f32_16x16x32_bf16 v[144:147], a[48:51], a[104:107], v[144:147]\n"
"v_mfma_f32_16x16x32_bf16 v[176:179], a[48:51], a[108:111], v[176:179]\n"
"v_mfma_f32_16x16x32_bf16 v[20:23], a[52:55], a[88:91], v[20:23]\n"
"buffer_load_dwordx4 v201, s[44:47], s75 offen offset:1024 lds\n"
"v_mfma_f32_16x16x32_bf16 v[52:55], a[52:55], a[92:95], v[52:55]\n"
"v_mfma_f32_16x16x32_bf16 v[84:87], a[52:55], a[96:99], v[84:87]\n"
"v_mfma_f32_16x16x32_bf16 v[116:119], a[52:55], a[100:103], v[116:119]\n"
"v_mfma_f32_16x16x32_bf16 v[148:151], a[52:55], a[104:107], v[148:151]\n"
"buffer_load_dwordx4 v202, s[44:47], s75 offen offset:2048 lds\n"
"v_mfma_f32_16x16x32_bf16 v[180:183], a[52:55], a[108:111], v[180:183]\n"
"v_mfma_f32_16x16x32_bf16 v[24:27], a[56:59], a[88:91], v[24:27]\n"
"v_mfma_f32_16x16x32_bf16 v[56:59], a[56:59], a[92:95], v[56:59]\n"
"v_mfma_f32_16x16x32_bf16 v[88:91], a[56:59], a[96:99], v[88:91]\n"
"buffer_load_dwordx4 v203, s[44:47], s75 offen offset:3072 lds\n"
"v_mfma_f32_16x16x32_bf16 v[120:123], a[56:59], a[100:103], v[120:123]\n"
"v_mfma_f32_16x16x32_bf16 v[152:155], a[56:59], a[104:107], v[152:155]\n"
"v_mfma_f32_16x16x32_bf16 v[184:187], a[56:59], a[108:111], v[184:187]\n"
"v_mfma_f32_16x16x32_bf16 v[28:31], a[60:63], a[88:91], v[28:31]\n"
"v_mfma_f32_16x16x32_bf16 v[60:63], a[60:63], a[92:95], v[60:63]\n"
"v_mfma_f32_16x16x32_bf16 v[92:95], a[60:63], a[96:99], v[92:95]\n"
"v_mfma_f32_16x16x32_bf16 v[124:127], a[60:63], a[100:103], v[124:127]\n"
"v_mfma_f32_16x16x32_bf16 v[156:159], a[60:63], a[104:107], v[156:159]\n"
"v_mfma_f32_16x16x32_bf16 v[188:191], a[60:63], a[108:111], v[188:191]\n"
"s_waitcnt vmcnt(14) lgkmcnt(0)\n"
"s_barrier\n"
"v_mfma_f32_16x16x32_bf16 v[0:3], a[0:3], a[112:115], v[0:3]\n"
"s_add_u32 m0, s68, 0x8000\n"
"s_add_u32 s75, s25, 0x80000\n"
"ds_read_b64_tr_b16 a[32:33], v204 offset:16384\n"
"v_mfma_f32_16x16x32_bf16 v[32:35], a[0:3], a[116:119], v[32:35]\n"
"ds_read_b64_tr_b16 a[34:35], v204 offset:16640\n"
"v_mfma_f32_16x16x32_bf16 v[64:67], a[0:3], a[120:123], v[64:67]\n"
"ds_read_b64_tr_b16 a[36:37], v207 offset:16384\n"
"buffer_load_dwordx4 v200, s[44:47], s75 offen offset:0 lds\n"
"v_mfma_f32_16x16x32_bf16 v[96:99], a[0:3], a[124:127], v[96:99]\n"
"ds_read_b64_tr_b16 a[38:39], v207 offset:16640\n"
"v_mfma_f32_16x16x32_bf16 v[128:131], a[0:3], a[128:131], v[128:131]\n"
"ds_read_b64_tr_b16 a[40:41], v204 offset:16896\n"
"v_mfma_f32_16x16x32_bf16 v[160:163], a[0:3], a[132:135], v[160:163]\n"
"ds_read_b64_tr_b16 a[42:43], v204 offset:17152\n"
"v_mfma_f32_16x16x32_bf16 v[4:7], a[4:7], a[112:115], v[4:7]\n"
"ds_read_b64_tr_b16 a[44:45], v207 offset:16896\n"
"buffer_load_dwordx4 v201, s[44:47], s75 offen offset:1024 lds\n"
"v_mfma_f32_16x16x32_bf16 v[36:39], a[4:7], a[116:119], v[36:39]\n"
"ds_read_b64_tr_b16 a[46:47], v207 offset:17152\n"
"v_mfma_f32_16x16x32_bf16 v[68:71], a[4:7], a[120:123], v[68:71]\n"
"ds_read_b64_tr_b16 a[48:49], v204 offset:17408\n"
"v_mfma_f32_16x16x32_bf16 v[100:103], a[4:7], a[124:127], v[100:103]\n"
"ds_read_b64_tr_b16 a[50:51], v204 offset:17664\n"
"v_mfma_f32_16x16x32_bf16 v[132:135], a[4:7], a[128:131], v[132:135]\n"
"ds_read_b64_tr_b16 a[52:53], v207 offset:17408\n"
"buffer_load_dwordx4 v202, s[44:47], s75 offen offset:2048 lds\n"
"v_mfma_f32_16x16x32_bf16 v[164:167], a[4:7], a[132:135], v[164:167]\n"
"ds_read_b64_tr_b16 a[54:55], v207 offset:17664\n"
"v_mfma_f32_16x16x32_bf16 v[8:11], a[8:11], a[112:115], v[8:11]\n"
"ds_read_b64_tr_b16 a[56:57], v204 offset:17920\n"
"v_mfma_f32_16x16x32_bf16 v[40:43], a[8:11], a[116:119], v[40:43]\n"
"ds_read_b64_tr_b16 a[58:59], v204 offset:18176\n"
"v_mfma_f32_16x16x32_bf16 v[72:75], a[8:11], a[120:123], v[72:75]\n"
"ds_read_b64_tr_b16 a[60:61], v207 offset:17920\n"
"buffer_load_dwordx4 v203, s[44:47], s75 offen offset:3072 lds\n"
"v_mfma_f32_16x16x32_bf16 v[104:107], a[8:11], a[124:127], v[104:107]\n"
"s_add_u32 m0, s68, 0xc000\n"
"s_add_u32 s75, s25, 0xc0000\n"
"ds_read_b64_tr_b16 a[62:63], v207 offset:18176\n"
"v_mfma_f32_16x16x32_bf16 v[136:139], a[8:11], a[128:131], v[136:139]\n"
"ds_read_b128 a[64:67], v198 offset:40960\n"
"v_mfma_f32_16x16x32_bf16 v[168:171], a[8:11], a[132:135], v[168:171]\n"
"ds_read_b128 a[68:71], v198 offset:43008\n"
"v_mfma_f32_16x16x32_bf16 v[12:15], a[12:15], a[112:115], v[12:15]\n"
"ds_read_b128 a[72:75], v198 offset:45056\n"
"buffer_load_dwordx4 v200, s[44:47], s75 offen offset:0 lds\n"
"v_mfma_f32_16x16x32_bf16 v[44:47], a[12:15], a[116:119], v[44:47]\n"
"ds_read_b128 a[76:79], v198 offset:47104\n"
"v_mfma_f32_16x16x32_bf16 v[76:79], a[12:15], a[120:123], v[76:79]\n"
"ds_read_b128 a[80:83], v198 offset:49152\n"
"v_mfma_f32_16x16x32_bf16 v[108:111], a[12:15], a[124:127], v[108:111]\n"
"ds_read_b128 a[84:87], v198 offset:51200\n"
"v_mfma_f32_16x16x32_bf16 v[140:143], a[12:15], a[128:131], v[140:143]\n"
"ds_read_b128 a[88:91], v198 offset:41472\n"
"buffer_load_dwordx4 v201, s[44:47], s75 offen offset:1024 lds\n"
"v_mfma_f32_16x16x32_bf16 v[172:175], a[12:15], a[132:135], v[172:175]\n"
"ds_read_b128 a[92:95], v198 offset:43520\n"
"v_mfma_f32_16x16x32_bf16 v[16:19], a[16:19], a[112:115], v[16:19]\n"
"ds_read_b128 a[96:99], v198 offset:45568\n"
"v_mfma_f32_16x16x32_bf16 v[48:51], a[16:19], a[116:119], v[48:51]\n"
"ds_read_b128 a[100:103], v198 offset:47616\n"
"v_mfma_f32_16x16x32_bf16 v[80:83], a[16:19], a[120:123], v[80:83]\n"
"ds_read_b128 a[104:107], v198 offset:49664\n"
"buffer_load_dwordx4 v202, s[44:47], s75 offen offset:2048 lds\n"
"v_mfma_f32_16x16x32_bf16 v[112:115], a[16:19], a[124:127], v[112:115]\n"
"ds_read_b128 a[108:111], v198 offset:51712\n"
"v_mfma_f32_16x16x32_bf16 v[144:147], a[16:19], a[128:131], v[144:147]\n"
"v_mfma_f32_16x16x32_bf16 v[176:179], a[16:19], a[132:135], v[176:179]\n"
"v_mfma_f32_16x16x32_bf16 v[20:23], a[20:23], a[112:115], v[20:23]\n"
"buffer_load_dwordx4 v203, s[44:47], s75 offen offset:3072 lds\n"
"v_mfma_f32_16x16x32_bf16 v[52:55], a[20:23], a[116:119], v[52:55]\n"
"v_mfma_f32_16x16x32_bf16 v[84:87], a[20:23], a[120:123], v[84:87]\n"
"v_mfma_f32_16x16x32_bf16 v[116:119], a[20:23], a[124:127], v[116:119]\n"
"v_mfma_f32_16x16x32_bf16 v[148:151], a[20:23], a[128:131], v[148:151]\n"
"v_mfma_f32_16x16x32_bf16 v[180:183], a[20:23], a[132:135], v[180:183]\n"
"v_mfma_f32_16x16x32_bf16 v[24:27], a[24:27], a[112:115], v[24:27]\n"
"v_mfma_f32_16x16x32_bf16 v[56:59], a[24:27], a[116:119], v[56:59]\n"
"v_mfma_f32_16x16x32_bf16 v[88:91], a[24:27], a[120:123], v[88:91]\n"
"v_mfma_f32_16x16x32_bf16 v[120:123], a[24:27], a[124:127], v[120:123]\n"
"v_mfma_f32_16x16x32_bf16 v[152:155], a[24:27], a[128:131], v[152:155]\n"
"v_mfma_f32_16x16x32_bf16 v[184:187], a[24:27], a[132:135], v[184:187]\n"
"v_mfma_f32_16x16x32_bf16 v[28:31], a[28:31], a[112:115], v[28:31]\n"
"v_mfma_f32_16x16x32_bf16 v[60:63], a[28:31], a[116:119], v[60:63]\n"
"v_mfma_f32_16x16x32_bf16 v[92:95], a[28:31], a[120:123], v[92:95]\n"
"v_mfma_f32_16x16x32_bf16 v[124:127], a[28:31], a[124:127], v[124:127]\n"
"v_mfma_f32_16x16x32_bf16 v[156:159], a[28:31], a[128:131], v[156:159]\n"
"v_mfma_f32_16x16x32_bf16 v[188:191], a[28:31], a[132:135], v[188:191]\n"
"s_waitcnt vmcnt(18) lgkmcnt(0)\n"
"s_barrier\n"
"v_mfma_f32_16x16x32_bf16 v[0:3], a[32:35], a[136:139], v[0:3]\n"
"s_add_u32 s70, s23, 0\n"
"s_add_u32 m0, s69, 0x1a000\n"
"ds_read_b64_tr_b16 a[0:1], v205 offset:24576\n"
"v_mfma_f32_16x16x32_bf16 v[32:35], a[32:35], a[140:143], v[32:35]\n"
"ds_read_b64_tr_b16 a[2:3], v205 offset:24832\n"
"v_mfma_f32_16x16x32_bf16 v[64:67], a[32:35], a[144:147], v[64:67]\n"
"ds_read_b64_tr_b16 a[4:5], v208 offset:24576\n"
"buffer_load_dwordx4 v192, s[40:43], s70 offen offset:0 lds\n"
"v_mfma_f32_16x16x32_bf16 v[96:99], a[32:35], a[148:151], v[96:99]\n"
"ds_read_b64_tr_b16 a[6:7], v208 offset:24832\n"
"v_mfma_f32_16x16x32_bf16 v[128:131], a[32:35], a[152:155], v[128:131]\n"
"ds_read_b64_tr_b16 a[8:9], v205 offset:25088\n"
"v_mfma_f32_16x16x32_bf16 v[160:163], a[32:35], a[156:159], v[160:163]\n"
"ds_read_b64_tr_b16 a[10:11], v205 offset:25344\n"
"v_mfma_f32_16x16x32_bf16 v[4:7], a[36:39], a[136:139], v[4:7]\n"
"ds_read_b64_tr_b16 a[12:13], v208 offset:25088\n"
"buffer_load_dwordx4 v193, s[40:43], s70 offen offset:1024 lds\n"
"v_mfma_f32_16x16x32_bf16 v[36:39], a[36:39], a[140:143], v[36:39]\n"
"ds_read_b64_tr_b16 a[14:15], v208 offset:25344\n"
"v_mfma_f32_16x16x32_bf16 v[68:71], a[36:39], a[144:147], v[68:71]\n"
"ds_read_b64_tr_b16 a[16:17], v205 offset:25600\n"
"v_mfma_f32_16x16x32_bf16 v[100:103], a[36:39], a[148:151], v[100:103]\n"
"ds_read_b64_tr_b16 a[18:19], v205 offset:25856\n"
"v_mfma_f32_16x16x32_bf16 v[132:135], a[36:39], a[152:155], v[132:135]\n"
"ds_read_b64_tr_b16 a[20:21], v208 offset:25600\n"
"buffer_load_dwordx4 v194, s[40:43], s70 offen offset:2048 lds\n"
"v_mfma_f32_16x16x32_bf16 v[164:167], a[36:39], a[156:159], v[164:167]\n"
"ds_read_b64_tr_b16 a[22:23], v208 offset:25856\n"
"v_mfma_f32_16x16x32_bf16 v[8:11], a[40:43], a[136:139], v[8:11]\n"
"ds_read_b64_tr_b16 a[24:25], v205 offset:26112\n"
"v_mfma_f32_16x16x32_bf16 v[40:43], a[40:43], a[140:143], v[40:43]\n"
"ds_read_b64_tr_b16 a[26:27], v205 offset:26368\n"
"v_mfma_f32_16x16x32_bf16 v[72:75], a[40:43], a[144:147], v[72:75]\n"
"ds_read_b64_tr_b16 a[28:29], v208 offset:26112\n"
"buffer_load_dwordx4 v195, s[40:43], s70 offen offset:3072 lds\n"
"v_mfma_f32_16x16x32_bf16 v[104:107], a[40:43], a[148:151], v[104:107]\n"
"s_add_u32 m0, s69, 0x1b000\n"
"ds_read_b64_tr_b16 a[30:31], v208 offset:26368\n"
"v_mfma_f32_16x16x32_bf16 v[136:139], a[40:43], a[152:155], v[136:139]\n"
"v_mfma_f32_16x16x32_bf16 v[168:171], a[40:43], a[156:159], v[168:171]\n"
"v_mfma_f32_16x16x32_bf16 v[12:15], a[44:47], a[136:139], v[12:15]\n"
"buffer_load_dwordx4 v196, s[40:43], s70 offen offset:0 lds\n"
"v_mfma_f32_16x16x32_bf16 v[44:47], a[44:47], a[140:143], v[44:47]\n"
"v_mfma_f32_16x16x32_bf16 v[76:79], a[44:47], a[144:147], v[76:79]\n"
"v_mfma_f32_16x16x32_bf16 v[108:111], a[44:47], a[148:151], v[108:111]\n"
"v_mfma_f32_16x16x32_bf16 v[140:143], a[44:47], a[152:155], v[140:143]\n"
"buffer_load_dwordx4 v197, s[40:43], s70 offen offset:1024 lds\n"
"v_mfma_f32_16x16x32_bf16 v[172:175], a[44:47], a[156:159], v[172:175]\n"
"s_add_u32 m0, s68, 0x24000\n"
"s_add_u32 s75, s26, 0x0\n"
"v_mfma_f32_16x16x32_bf16 v[16:19], a[48:51], a[136:139], v[16:19]\n"
"v_mfma_f32_16x16x32_bf16 v[48:51], a[48:51], a[140:143], v[48:51]\n"
"v_mfma_f32_16x16x32_bf16 v[80:83], a[48:51], a[144:147], v[80:83]\n"
"buffer_load_dwordx4 v200, s[44:47], s75 offen offset:0 lds\n"
"v_mfma_f32_16x16x32_bf16 v[112:115], a[48:51], a[148:151], v[112:115]\n"
"v_mfma_f32_16x16x32_bf16 v[144:147], a[48:51], a[152:155], v[144:147]\n"
"v_mfma_f32_16x16x32_bf16 v[176:179], a[48:51], a[156:159], v[176:179]\n"
"v_mfma_f32_16x16x32_bf16 v[20:23], a[52:55], a[136:139], v[20:23]\n"
"buffer_load_dwordx4 v201, s[44:47], s75 offen offset:1024 lds\n"
"v_mfma_f32_16x16x32_bf16 v[52:55], a[52:55], a[140:143], v[52:55]\n"
"v_mfma_f32_16x16x32_bf16 v[84:87], a[52:55], a[144:147], v[84:87]\n"
"v_mfma_f32_16x16x32_bf16 v[116:119], a[52:55], a[148:151], v[116:119]\n"
"v_mfma_f32_16x16x32_bf16 v[148:151], a[52:55], a[152:155], v[148:151]\n"
"buffer_load_dwordx4 v202, s[44:47], s75 offen offset:2048 lds\n"
"v_mfma_f32_16x16x32_bf16 v[180:183], a[52:55], a[156:159], v[180:183]\n"
"v_mfma_f32_16x16x32_bf16 v[24:27], a[56:59], a[136:139], v[24:27]\n"
"v_mfma_f32_16x16x32_bf16 v[56:59], a[56:59], a[140:143], v[56:59]\n"
"v_mfma_f32_16x16x32_bf16 v[88:91], a[56:59], a[144:147], v[88:91]\n"
"buffer_load_dwordx4 v203, s[44:47], s75 offen offset:3072 lds\n"
"v_mfma_f32_16x16x32_bf16 v[120:123], a[56:59], a[148:151], v[120:123]\n"
"v_mfma_f32_16x16x32_bf16 v[152:155], a[56:59], a[152:155], v[152:155]\n"
"v_mfma_f32_16x16x32_bf16 v[184:187], a[56:59], a[156:159], v[184:187]\n"
"v_mfma_f32_16x16x32_bf16 v[28:31], a[60:63], a[136:139], v[28:31]\n"
"v_mfma_f32_16x16x32_bf16 v[60:63], a[60:63], a[140:143], v[60:63]\n"
"v_mfma_f32_16x16x32_bf16 v[92:95], a[60:63], a[144:147], v[92:95]\n"
"v_mfma_f32_16x16x32_bf16 v[124:127], a[60:63], a[148:151], v[124:127]\n"
"v_mfma_f32_16x16x32_bf16 v[156:159], a[60:63], a[152:155], v[156:159]\n"
"v_mfma_f32_16x16x32_bf16 v[188:191], a[60:63], a[156:159], v[188:191]\n"
"s_waitcnt vmcnt(18) lgkmcnt(0)\n"
"s_barrier\n"
"v_mfma_f32_16x16x32_bf16 v[0:3], a[0:3], a[64:67], v[0:3]\n"
"s_add_u32 m0, s68, 0x0\n"
"s_add_u32 s75, s26, 0x40000\n"
"ds_read_b64_tr_b16 a[32:33], v206 offset:0\n"
"v_mfma_f32_16x16x32_bf16 v[32:35], a[0:3], a[68:71], v[32:35]\n"
"ds_read_b64_tr_b16 a[34:35], v206 offset:256\n"
"v_mfma_f32_16x16x32_bf16 v[64:67], a[0:3], a[72:75], v[64:67]\n"
"ds_read_b64_tr_b16 a[36:37], v209 offset:0\n"
"buffer_load_dwordx4 v200, s[44:47], s75 offen offset:0 lds\n"
"v_mfma_f32_16x16x32_bf16 v[96:99], a[0:3], a[76:79], v[96:99]\n"
"ds_read_b64_tr_b16 a[38:39], v209 offset:256\n"
"v_mfma_f32_16x16x32_bf16 v[128:131], a[0:3], a[80:83], v[128:131]\n"
"ds_read_b64_tr_b16 a[40:41], v206 offset:512\n"
"v_mfma_f32_16x16x32_bf16 v[160:163], a[0:3], a[84:87], v[160:163]\n"
"ds_read_b64_tr_b16 a[42:43], v206 offset:768\n"
"v_mfma_f32_16x16x32_bf16 v[4:7], a[4:7], a[64:67], v[4:7]\n"
"ds_read_b64_tr_b16 a[44:45], v209 offset:512\n"
"buffer_load_dwordx4 v201, s[44:47], s75 offen offset:1024 lds\n"
"v_mfma_f32_16x16x32_bf16 v[36:39], a[4:7], a[68:71], v[36:39]\n"
"ds_read_b64_tr_b16 a[46:47], v209 offset:768\n"
"v_mfma_f32_16x16x32_bf16 v[68:71], a[4:7], a[72:75], v[68:71]\n"
"ds_read_b64_tr_b16 a[48:49], v206 offset:1024\n"
"v_mfma_f32_16x16x32_bf16 v[100:103], a[4:7], a[76:79], v[100:103]\n"
"ds_read_b64_tr_b16 a[50:51], v206 offset:1280\n"
"v_mfma_f32_16x16x32_bf16 v[132:135], a[4:7], a[80:83], v[132:135]\n"
"ds_read_b64_tr_b16 a[52:53], v209 offset:1024\n"
"buffer_load_dwordx4 v202, s[44:47], s75 offen offset:2048 lds\n"
"v_mfma_f32_16x16x32_bf16 v[164:167], a[4:7], a[84:87], v[164:167]\n"
"ds_read_b64_tr_b16 a[54:55], v209 offset:1280\n"
"v_mfma_f32_16x16x32_bf16 v[8:11], a[8:11], a[64:67], v[8:11]\n"
"ds_read_b64_tr_b16 a[56:57], v206 offset:1536\n"
"v_mfma_f32_16x16x32_bf16 v[40:43], a[8:11], a[68:71], v[40:43]\n"
"ds_read_b64_tr_b16 a[58:59], v206 offset:1792\n"
"v_mfma_f32_16x16x32_bf16 v[72:75], a[8:11], a[72:75], v[72:75]\n"
"ds_read_b64_tr_b16 a[60:61], v209 offset:1536\n"
"buffer_load_dwordx4 v203, s[44:47], s75 offen offset:3072 lds\n"
"v_mfma_f32_16x16x32_bf16 v[104:107], a[8:11], a[76:79], v[104:107]\n"
"s_add_u32 m0, s68, 0x4000\n"
"s_add_u32 s75, s26, 0x80000\n"
"ds_read_b64_tr_b16 a[62:63], v209 offset:1792\n"
"v_mfma_f32_16x16x32_bf16 v[136:139], a[8:11], a[80:83], v[136:139]\n"
"ds_read_b128 a[112:115], v198 offset:0\n"
"v_mfma_f32_16x16x32_bf16 v[168:171], a[8:11], a[84:87], v[168:171]\n"
"ds_read_b128 a[116:119], v198 offset:2048\n"
"v_mfma_f32_16x16x32_bf16 v[12:15], a[12:15], a[64:67], v[12:15]\n"
"ds_read_b128 a[120:123], v198 offset:4096\n"
"buffer_load_dwordx4 v200, s[44:47], s75 offen offset:0 lds\n"
"v_mfma_f32_16x16x32_bf16 v[44:47], a[12:15], a[68:71], v[44:47]\n"
"ds_read_b128 a[124:127], v198 offset:6144\n"
"v_mfma_f32_16x16x32_bf16 v[76:79], a[12:15], a[72:75], v[76:79]\n"
"ds_read_b128 a[128:131], v198 offset:8192\n"
"v_mfma_f32_16x16x32_bf16 v[108:111], a[12:15], a[76:79], v[108:111]\n"
"ds_read_b128 a[132:135], v198 offset:10240\n"
"v_mfma_f32_16x16x32_bf16 v[140:143], a[12:15], a[80:83], v[140:143]\n"
"ds_read_b128 a[136:139], v198 offset:512\n"
"buffer_load_dwordx4 v201, s[44:47], s75 offen offset:1024 lds\n"
"v_mfma_f32_16x16x32_bf16 v[172:175], a[12:15], a[84:87], v[172:175]\n"
"ds_read_b128 a[140:143], v198 offset:2560\n"
"v_mfma_f32_16x16x32_bf16 v[16:19], a[16:19], a[64:67], v[16:19]\n"
"ds_read_b128 a[144:147], v198 offset:4608\n"
"v_mfma_f32_16x16x32_bf16 v[48:51], a[16:19], a[68:71], v[48:51]\n"
"ds_read_b128 a[148:151], v198 offset:6656\n"
"v_mfma_f32_16x16x32_bf16 v[80:83], a[16:19], a[72:75], v[80:83]\n"
"ds_read_b128 a[152:155], v198 offset:8704\n"
"buffer_load_dwordx4 v202, s[44:47], s75 offen offset:2048 lds\n"
"v_mfma_f32_16x16x32_bf16 v[112:115], a[16:19], a[76:79], v[112:115]\n"
"ds_read_b128 a[156:159], v198 offset:10752\n"
"v_mfma_f32_16x16x32_bf16 v[144:147], a[16:19], a[80:83], v[144:147]\n"
"v_mfma_f32_16x16x32_bf16 v[176:179], a[16:19], a[84:87], v[176:179]\n"
"v_mfma_f32_16x16x32_bf16 v[20:23], a[20:23], a[64:67], v[20:23]\n"
"buffer_load_dwordx4 v203, s[44:47], s75 offen offset:3072 lds\n"
"v_mfma_f32_16x16x32_bf16 v[52:55], a[20:23], a[68:71], v[52:55]\n"
"v_mfma_f32_16x16x32_bf16 v[84:87], a[20:23], a[72:75], v[84:87]\n"
"v_mfma_f32_16x16x32_bf16 v[116:119], a[20:23], a[76:79], v[116:119]\n"
"v_mfma_f32_16x16x32_bf16 v[148:151], a[20:23], a[80:83], v[148:151]\n"
"v_mfma_f32_16x16x32_bf16 v[180:183], a[20:23], a[84:87], v[180:183]\n"
"v_mfma_f32_16x16x32_bf16 v[24:27], a[24:27], a[64:67], v[24:27]\n"
"v_mfma_f32_16x16x32_bf16 v[56:59], a[24:27], a[68:71], v[56:59]\n"
"v_mfma_f32_16x16x32_bf16 v[88:91], a[24:27], a[72:75], v[88:91]\n"
"v_mfma_f32_16x16x32_bf16 v[120:123], a[24:27], a[76:79], v[120:123]\n"
"v_mfma_f32_16x16x32_bf16 v[152:155], a[24:27], a[80:83], v[152:155]\n"
"v_mfma_f32_16x16x32_bf16 v[184:187], a[24:27], a[84:87], v[184:187]\n"
"v_mfma_f32_16x16x32_bf16 v[28:31], a[28:31], a[64:67], v[28:31]\n"
"v_mfma_f32_16x16x32_bf16 v[60:63], a[28:31], a[68:71], v[60:63]\n"
"v_mfma_f32_16x16x32_bf16 v[92:95], a[28:31], a[72:75], v[92:95]\n"
"v_mfma_f32_16x16x32_bf16 v[124:127], a[28:31], a[76:79], v[124:127]\n"
"v_mfma_f32_16x16x32_bf16 v[156:159], a[28:31], a[80:83], v[156:159]\n"
"v_mfma_f32_16x16x32_bf16 v[188:191], a[28:31], a[84:87], v[188:191]\n"
"s_waitcnt vmcnt(22) lgkmcnt(0)\n"
"s_barrier\n"
"v_mfma_f32_16x16x32_bf16 v[0:3], a[32:35], a[88:91], v[0:3]\n"
"s_add_u32 s70, s23, 128\n"
"s_add_u32 m0, s69, 0x10000\n"
"ds_read_b64_tr_b16 a[0:1], v204 offset:32768\n"
"v_mfma_f32_16x16x32_bf16 v[32:35], a[32:35], a[92:95], v[32:35]\n"
"ds_read_b64_tr_b16 a[2:3], v204 offset:33024\n"
"v_mfma_f32_16x16x32_bf16 v[64:67], a[32:35], a[96:99], v[64:67]\n"
"ds_read_b64_tr_b16 a[4:5], v207 offset:32768\n"
"buffer_load_dwordx4 v192, s[40:43], s70 offen offset:0 lds\n"
"v_mfma_f32_16x16x32_bf16 v[96:99], a[32:35], a[100:103], v[96:99]\n"
"ds_read_b64_tr_b16 a[6:7], v207 offset:33024\n"
"v_mfma_f32_16x16x32_bf16 v[128:131], a[32:35], a[104:107], v[128:131]\n"
"ds_read_b64_tr_b16 a[8:9], v204 offset:33280\n"
"v_mfma_f32_16x16x32_bf16 v[160:163], a[32:35], a[108:111], v[160:163]\n"
"ds_read_b64_tr_b16 a[10:11], v204 offset:33536\n"
"v_mfma_f32_16x16x32_bf16 v[4:7], a[36:39], a[88:91], v[4:7]\n"
"ds_read_b64_tr_b16 a[12:13], v207 offset:33280\n"
"buffer_load_dwordx4 v193, s[40:43], s70 offen offset:1024 lds\n"
"v_mfma_f32_16x16x32_bf16 v[36:39], a[36:39], a[92:95], v[36:39]\n"
"ds_read_b64_tr_b16 a[14:15], v207 offset:33536\n"
"v_mfma_f32_16x16x32_bf16 v[68:71], a[36:39], a[96:99], v[68:71]\n"
"ds_read_b64_tr_b16 a[16:17], v204 offset:33792\n"
"v_mfma_f32_16x16x32_bf16 v[100:103], a[36:39], a[100:103], v[100:103]\n"
"ds_read_b64_tr_b16 a[18:19], v204 offset:34048\n"
"v_mfma_f32_16x16x32_bf16 v[132:135], a[36:39], a[104:107], v[132:135]\n"
"ds_read_b64_tr_b16 a[20:21], v207 offset:33792\n"
"buffer_load_dwordx4 v194, s[40:43], s70 offen offset:2048 lds\n"
"v_mfma_f32_16x16x32_bf16 v[164:167], a[36:39], a[108:111], v[164:167]\n"
"ds_read_b64_tr_b16 a[22:23], v207 offset:34048\n"
"v_mfma_f32_16x16x32_bf16 v[8:11], a[40:43], a[88:91], v[8:11]\n"
"ds_read_b64_tr_b16 a[24:25], v204 offset:34304\n"
"v_mfma_f32_16x16x32_bf16 v[40:43], a[40:43], a[92:95], v[40:43]\n"
"ds_read_b64_tr_b16 a[26:27], v204 offset:34560\n"
"v_mfma_f32_16x16x32_bf16 v[72:75], a[40:43], a[96:99], v[72:75]\n"
"ds_read_b64_tr_b16 a[28:29], v207 offset:34304\n"
"buffer_load_dwordx4 v195, s[40:43], s70 offen offset:3072 lds\n"
"v_mfma_f32_16x16x32_bf16 v[104:107], a[40:43], a[100:103], v[104:107]\n"
"s_add_u32 m0, s69, 0x11000\n"
"ds_read_b64_tr_b16 a[30:31], v207 offset:34560\n"
"v_mfma_f32_16x16x32_bf16 v[136:139], a[40:43], a[104:107], v[136:139]\n"
"v_mfma_f32_16x16x32_bf16 v[168:171], a[40:43], a[108:111], v[168:171]\n"
"v_mfma_f32_16x16x32_bf16 v[12:15], a[44:47], a[88:91], v[12:15]\n"
"buffer_load_dwordx4 v196, s[40:43], s70 offen offset:0 lds\n"
"v_mfma_f32_16x16x32_bf16 v[44:47], a[44:47], a[92:95], v[44:47]\n"
"v_mfma_f32_16x16x32_bf16 v[76:79], a[44:47], a[96:99], v[76:79]\n"
"v_mfma_f32_16x16x32_bf16 v[108:111], a[44:47], a[100:103], v[108:111]\n"
"v_mfma_f32_16x16x32_bf16 v[140:143], a[44:47], a[104:107], v[140:143]\n"
"buffer_load_dwordx4 v197, s[40:43], s70 offen offset:1024 lds\n"
"v_mfma_f32_16x16x32_bf16 v[172:175], a[44:47], a[108:111], v[172:175]\n"
"s_add_u32 m0, s68, 0x16000\n"
"s_add_u32 s75, s27, 0x40000\n"
"v_mfma_f32_16x16x32_bf16 v[16:19], a[48:51], a[88:91], v[16:19]\n"
"v_mfma_f32_16x16x32_bf16 v[48:51], a[48:51], a[92:95], v[48:51]\n"
"v_mfma_f32_16x16x32_bf16 v[80:83], a[48:51], a[96:99], v[80:83]\n"
"buffer_load_dwordx4 v200, s[44:47], s75 offen offset:0 lds\n"
"v_mfma_f32_16x16x32_bf16 v[112:115], a[48:51], a[100:103], v[112:115]\n"
"v_mfma_f32_16x16x32_bf16 v[144:147], a[48:51], a[104:107], v[144:147]\n"
"v_mfma_f32_16x16x32_bf16 v[176:179], a[48:51], a[108:111], v[176:179]\n"
"v_mfma_f32_16x16x32_bf16 v[20:23], a[52:55], a[88:91], v[20:23]\n"
"buffer_load_dwordx4 v201, s[44:47], s75 offen offset:1024 lds\n"
"v_mfma_f32_16x16x32_bf16 v[52:55], a[52:55], a[92:95], v[52:55]\n"
"v_mfma_f32_16x16x32_bf16 v[84:87], a[52:55], a[96:99], v[84:87]\n"
"v_mfma_f32_16x16x32_bf16 v[116:119], a[52:55], a[100:103], v[116:119]\n"
"v_mfma_f32_16x16x32_bf16 v[148:151], a[52:55], a[104:107], v[148:151]\n"
"buffer_load_dwordx4 v202, s[44:47], s75 offen offset:2048 lds\n"
"v_mfma_f32_16x16x32_bf16 v[180:183], a[52:55], a[108:111], v[180:183]\n"
"v_mfma_f32_16x16x32_bf16 v[24:27], a[56:59], a[88:91], v[24:27]\n"
"v_mfma_f32_16x16x32_bf16 v[56:59], a[56:59], a[92:95], v[56:59]\n"
"v_mfma_f32_16x16x32_bf16 v[88:91], a[56:59], a[96:99], v[88:91]\n"
"buffer_load_dwordx4 v203, s[44:47], s75 offen offset:3072 lds\n"
"v_mfma_f32_16x16x32_bf16 v[120:123], a[56:59], a[100:103], v[120:123]\n"
"v_mfma_f32_16x16x32_bf16 v[152:155], a[56:59], a[104:107], v[152:155]\n"
"v_mfma_f32_16x16x32_bf16 v[184:187], a[56:59], a[108:111], v[184:187]\n"
"v_mfma_f32_16x16x32_bf16 v[28:31], a[60:63], a[88:91], v[28:31]\n"
"v_mfma_f32_16x16x32_bf16 v[60:63], a[60:63], a[92:95], v[60:63]\n"
"v_mfma_f32_16x16x32_bf16 v[92:95], a[60:63], a[96:99], v[92:95]\n"
"v_mfma_f32_16x16x32_bf16 v[124:127], a[60:63], a[100:103], v[124:127]\n"
"v_mfma_f32_16x16x32_bf16 v[156:159], a[60:63], a[104:107], v[156:159]\n"
"v_mfma_f32_16x16x32_bf16 v[188:191], a[60:63], a[108:111], v[188:191]\n"
"s_waitcnt vmcnt(22) lgkmcnt(0)\n"
"s_barrier\n"
"v_mfma_f32_16x16x32_bf16 v[0:3], a[0:3], a[112:115], v[0:3]\n"
"s_add_u32 m0, s68, 0x8000\n"
"s_add_u32 s75, s26, 0xc0000\n"
"ds_read_b64_tr_b16 a[32:33], v204 offset:49152\n"
"v_mfma_f32_16x16x32_bf16 v[32:35], a[0:3], a[116:119], v[32:35]\n"
"ds_read_b64_tr_b16 a[34:35], v204 offset:49408\n"
"v_mfma_f32_16x16x32_bf16 v[64:67], a[0:3], a[120:123], v[64:67]\n"
"ds_read_b64_tr_b16 a[36:37], v207 offset:49152\n"
"buffer_load_dwordx4 v200, s[44:47], s75 offen offset:0 lds\n"
"v_mfma_f32_16x16x32_bf16 v[96:99], a[0:3], a[124:127], v[96:99]\n"
"ds_read_b64_tr_b16 a[38:39], v207 offset:49408\n"
"v_mfma_f32_16x16x32_bf16 v[128:131], a[0:3], a[128:131], v[128:131]\n"
"ds_read_b64_tr_b16 a[40:41], v204 offset:49664\n"
"v_mfma_f32_16x16x32_bf16 v[160:163], a[0:3], a[132:135], v[160:163]\n"
"ds_read_b64_tr_b16 a[42:43], v204 offset:49920\n"
"v_mfma_f32_16x16x32_bf16 v[4:7], a[4:7], a[112:115], v[4:7]\n"
"ds_read_b64_tr_b16 a[44:45], v207 offset:49664\n"
"buffer_load_dwordx4 v201, s[44:47], s75 offen offset:1024 lds\n"
"v_mfma_f32_16x16x32_bf16 v[36:39], a[4:7], a[116:119], v[36:39]\n"
"ds_read_b64_tr_b16 a[46:47], v207 offset:49920\n"
"v_mfma_f32_16x16x32_bf16 v[68:71], a[4:7], a[120:123], v[68:71]\n"
"ds_read_b64_tr_b16 a[48:49], v204 offset:50176\n"
"v_mfma_f32_16x16x32_bf16 v[100:103], a[4:7], a[124:127], v[100:103]\n"
"ds_read_b64_tr_b16 a[50:51], v204 offset:50432\n"
"v_mfma_f32_16x16x32_bf16 v[132:135], a[4:7], a[128:131], v[132:135]\n"
"ds_read_b64_tr_b16 a[52:53], v207 offset:50176\n"
"buffer_load_dwordx4 v202, s[44:47], s75 offen offset:2048 lds\n"
"v_mfma_f32_16x16x32_bf16 v[164:167], a[4:7], a[132:135], v[164:167]\n"
"ds_read_b64_tr_b16 a[54:55], v207 offset:50432\n"
"v_mfma_f32_16x16x32_bf16 v[8:11], a[8:11], a[112:115], v[8:11]\n"
"ds_read_b64_tr_b16 a[56:57], v204 offset:50688\n"
"v_mfma_f32_16x16x32_bf16 v[40:43], a[8:11], a[116:119], v[40:43]\n"
"ds_read_b64_tr_b16 a[58:59], v204 offset:50944\n"
"v_mfma_f32_16x16x32_bf16 v[72:75], a[8:11], a[120:123], v[72:75]\n"
"ds_read_b64_tr_b16 a[60:61], v207 offset:50688\n"
"buffer_load_dwordx4 v203, s[44:47], s75 offen offset:3072 lds\n"
"v_mfma_f32_16x16x32_bf16 v[104:107], a[8:11], a[124:127], v[104:107]\n"
"s_add_u32 m0, s68, 0x20000\n"
"s_add_u32 s75, s27, 0x80000\n"
"ds_read_b64_tr_b16 a[62:63], v207 offset:50944\n"
"v_mfma_f32_16x16x32_bf16 v[136:139], a[8:11], a[128:131], v[136:139]\n"
"ds_read_b128 a[64:67], v198 offset:40960\n"
"v_mfma_f32_16x16x32_bf16 v[168:171], a[8:11], a[132:135], v[168:171]\n"
"ds_read_b128 a[68:71], v198 offset:43008\n"
"v_mfma_f32_16x16x32_bf16 v[12:15], a[12:15], a[112:115], v[12:15]\n"
"ds_read_b128 a[72:75], v198 offset:45056\n"
"buffer_load_dwordx4 v200, s[44:47], s75 offen offset:0 lds\n"
"v_mfma_f32_16x16x32_bf16 v[44:47], a[12:15], a[116:119], v[44:47]\n"
"ds_read_b128 a[76:79], v198 offset:47104\n"
"v_mfma_f32_16x16x32_bf16 v[76:79], a[12:15], a[120:123], v[76:79]\n"
"ds_read_b128 a[80:83], v198 offset:49152\n"
"v_mfma_f32_16x16x32_bf16 v[108:111], a[12:15], a[124:127], v[108:111]\n"
"ds_read_b128 a[84:87], v198 offset:51200\n"
"v_mfma_f32_16x16x32_bf16 v[140:143], a[12:15], a[128:131], v[140:143]\n"
"ds_read_b128 a[88:91], v198 offset:41472\n"
"buffer_load_dwordx4 v201, s[44:47], s75 offen offset:1024 lds\n"
"v_mfma_f32_16x16x32_bf16 v[172:175], a[12:15], a[132:135], v[172:175]\n"
"ds_read_b128 a[92:95], v198 offset:43520\n"
"v_mfma_f32_16x16x32_bf16 v[16:19], a[16:19], a[112:115], v[16:19]\n"
"ds_read_b128 a[96:99], v198 offset:45568\n"
"v_mfma_f32_16x16x32_bf16 v[48:51], a[16:19], a[116:119], v[48:51]\n"
"ds_read_b128 a[100:103], v198 offset:47616\n"
"v_mfma_f32_16x16x32_bf16 v[80:83], a[16:19], a[120:123], v[80:83]\n"
"ds_read_b128 a[104:107], v198 offset:49664\n"
"buffer_load_dwordx4 v202, s[44:47], s75 offen offset:2048 lds\n"
"v_mfma_f32_16x16x32_bf16 v[112:115], a[16:19], a[124:127], v[112:115]\n"
"ds_read_b128 a[108:111], v198 offset:51712\n"
"v_mfma_f32_16x16x32_bf16 v[144:147], a[16:19], a[128:131], v[144:147]\n"
"v_mfma_f32_16x16x32_bf16 v[176:179], a[16:19], a[132:135], v[176:179]\n"
"v_mfma_f32_16x16x32_bf16 v[20:23], a[20:23], a[112:115], v[20:23]\n"
"buffer_load_dwordx4 v203, s[44:47], s75 offen offset:3072 lds\n"
"v_and_b32 v230, 15, v255\n"
"v_mfma_f32_16x16x32_bf16 v[52:55], a[20:23], a[116:119], v[52:55]\n"
"v_cmp_eq_u32 vcc, 0, v230\n"
"v_mfma_f32_16x16x32_bf16 v[84:87], a[20:23], a[120:123], v[84:87]\n"
"v_lshlrev_b32 v230, 2, v230\n"
"v_mfma_f32_16x16x32_bf16 v[116:119], a[20:23], a[124:127], v[116:119]\n"
"v_readfirstlane_b32 s82, v229\n"
"v_mfma_f32_16x16x32_bf16 v[148:151], a[20:23], a[128:131], v[148:151]\n"
"v_cndmask_b32 v228, 0, 4, vcc\n"
"v_mfma_f32_16x16x32_bf16 v[180:183], a[20:23], a[132:135], v[180:183]\n"
"s_add_u32 s82, s82, 4\n"
"v_mfma_f32_16x16x32_bf16 v[24:27], a[24:27], a[112:115], v[24:27]\n"
"v_add_u32 v228, v228, v229\n"
"v_mfma_f32_16x16x32_bf16 v[56:59], a[24:27], a[116:119], v[56:59]\n"
"buffer_store_dword v228, v230, s[52:55], s86 offen sc1\n"
"v_mfma_f32_16x16x32_bf16 v[88:91], a[24:27], a[120:123], v[88:91]\n"
"v_mfma_f32_16x16x32_bf16 v[120:123], a[24:27], a[124:127], v[120:123]\n"
"v_mfma_f32_16x16x32_bf16 v[152:155], a[24:27], a[128:131], v[152:155]\n"
"v_mfma_f32_16x16x32_bf16 v[184:187], a[24:27], a[132:135], v[184:187]\n"
"v_mfma_f32_16x16x32_bf16 v[28:31], a[28:31], a[112:115], v[28:31]\n"
"v_mfma_f32_16x16x32_bf16 v[60:63], a[28:31], a[116:119], v[60:63]\n"
"v_mfma_f32_16x16x32_bf16 v[92:95], a[28:31], a[120:123], v[92:95]\n"
"v_mfma_f32_16x16x32_bf16 v[124:127], a[28:31], a[124:127], v[124:127]\n"
"v_mfma_f32_16x16x32_bf16 v[156:159], a[28:31], a[128:131], v[156:159]\n"
"v_mfma_f32_16x16x32_bf16 v[188:191], a[28:31], a[132:135], v[188:191]\n"
"s_waitcnt vmcnt(27) lgkmcnt(0)\n"
"s_barrier\n"
"v_mfma_f32_16x16x32_bf16 v[0:3], a[32:35], a[136:139], v[0:3]\n"
"s_add_u32 s70, s24, 0\n"
"s_add_u32 m0, s69, 0x1a000\n"
"ds_read_b64_tr_b16 a[0:1], v206 offset:16384\n"
"v_mfma_f32_16x16x32_bf16 v[32:35], a[32:35], a[140:143], v[32:35]\n"
"ds_read_b64_tr_b16 a[2:3], v206 offset:16640\n"
"v_mfma_f32_16x16x32_bf16 v[64:67], a[32:35], a[144:147], v[64:67]\n"
"ds_read_b64_tr_b16 a[4:5], v209 offset:16384\n"
"buffer_load_dwordx4 v192, s[40:43], s70 offen offset:0 lds\n"
"v_mfma_f32_16x16x32_bf16 v[96:99], a[32:35], a[148:151], v[96:99]\n"
"ds_read_b64_tr_b16 a[6:7], v209 offset:16640\n"
"v_mfma_f32_16x16x32_bf16 v[128:131], a[32:35], a[152:155], v[128:131]\n"
"ds_read_b64_tr_b16 a[8:9], v206 offset:16896\n"
"v_mfma_f32_16x16x32_bf16 v[160:163], a[32:35], a[156:159], v[160:163]\n"
"ds_read_b64_tr_b16 a[10:11], v206 offset:17152\n"
"v_mfma_f32_16x16x32_bf16 v[4:7], a[36:39], a[136:139], v[4:7]\n"
"ds_read_b64_tr_b16 a[12:13], v209 offset:16896\n"
"buffer_load_dwordx4 v193, s[40:43], s70 offen offset:1024 lds\n"
"v_mfma_f32_16x16x32_bf16 v[36:39], a[36:39], a[140:143], v[36:39]\n"
"ds_read_b64_tr_b16 a[14:15], v209 offset:17152\n"
"v_mfma_f32_16x16x32_bf16 v[68:71], a[36:39], a[144:147], v[68:71]\n"
"ds_read_b64_tr_b16 a[16:17], v206 offset:17408\n"
"v_mfma_f32_16x16x32_bf16 v[100:103], a[36:39], a[148:151], v[100:103]\n"
"ds_read_b64_tr_b16 a[18:19], v206 offset:17664\n"
"v_mfma_f32_16x16x32_bf16 v[132:135], a[36:39], a[152:155], v[132:135]\n"
"ds_read_b64_tr_b16 a[20:21], v209 offset:17408\n"
"buffer_load_dwordx4 v194, s[40:43], s70 offen offset:2048 lds\n"
"v_mfma_f32_16x16x32_bf16 v[164:167], a[36:39], a[156:159], v[164:167]\n"
"ds_read_b64_tr_b16 a[22:23], v209 offset:17664\n"
"v_mfma_f32_16x16x32_bf16 v[8:11], a[40:43], a[136:139], v[8:11]\n"
"ds_read_b64_tr_b16 a[24:25], v206 offset:17920\n"
"v_mfma_f32_16x16x32_bf16 v[40:43], a[40:43], a[140:143], v[40:43]\n"
"ds_read_b64_tr_b16 a[26:27], v206 offset:18176\n"
"v_mfma_f32_16x16x32_bf16 v[72:75], a[40:43], a[144:147], v[72:75]\n"
"ds_read_b64_tr_b16 a[28:29], v209 offset:17920\n"
"buffer_load_dwordx4 v195, s[40:43], s70 offen offset:3072 lds\n"
"v_mfma_f32_16x16x32_bf16 v[104:107], a[40:43], a[148:151], v[104:107]\n"
"s_add_u32 m0, s69, 0x1b000\n"
"ds_read_b64_tr_b16 a[30:31], v209 offset:18176\n"
"v_mfma_f32_16x16x32_bf16 v[136:139], a[40:43], a[152:155], v[136:139]\n"
"v_mfma_f32_16x16x32_bf16 v[168:171], a[40:43], a[156:159], v[168:171]\n"
"v_mfma_f32_16x16x32_bf16 v[12:15], a[44:47], a[136:139], v[12:15]\n"
"buffer_load_dwordx4 v196, s[40:43], s70 offen offset:0 lds\n"
"v_mfma_f32_16x16x32_bf16 v[44:47], a[44:47], a[140:143], v[44:47]\n"
"v_mfma_f32_16x16x32_bf16 v[76:79], a[44:47], a[144:147], v[76:79]\n"
"v_mfma_f32_16x16x32_bf16 v[108:111], a[44:47], a[148:151], v[108:111]\n"
"v_mfma_f32_16x16x32_bf16 v[140:143], a[44:47], a[152:155], v[140:143]\n"
"buffer_load_dwordx4 v197, s[40:43], s70 offen offset:1024 lds\n"
"v_mfma_f32_16x16x32_bf16 v[172:175], a[44:47], a[156:159], v[172:175]\n"
"s_add_u32 m0, s68, 0xc000\n"
"s_add_u32 s75, s27, 0x0\n"
"v_mfma_f32_16x16x32_bf16 v[16:19], a[48:51], a[136:139], v[16:19]\n"
"v_mfma_f32_16x16x32_bf16 v[48:51], a[48:51], a[140:143], v[48:51]\n"
"v_mfma_f32_16x16x32_bf16 v[80:83], a[48:51], a[144:147], v[80:83]\n"
"buffer_load_dwordx4 v200, s[44:47], s75 offen offset:0 lds\n"
"v_mfma_f32_16x16x32_bf16 v[112:115], a[48:51], a[148:151], v[112:115]\n"
"v_mfma_f32_16x16x32_bf16 v[144:147], a[48:51], a[152:155], v[144:147]\n"
"v_mfma_f32_16x16x32_bf16 v[176:179], a[48:51], a[156:159], v[176:179]\n"
"v_mfma_f32_16x16x32_bf16 v[20:23], a[52:55], a[136:139], v[20:23]\n"
"buffer_load_dwordx4 v201, s[44:47], s75 offen offset:1024 lds\n"
"v_mfma_f32_16x16x32_bf16 v[52:55], a[52:55], a[140:143], v[52:55]\n"
"v_mfma_f32_16x16x32_bf16 v[84:87], a[52:55], a[144:147], v[84:87]\n"
"v_mfma_f32_16x16x32_bf16 v[116:119], a[52:55], a[148:151], v[116:119]\n"
"v_mfma_f32_16x16x32_bf16 v[148:151], a[52:55], a[152:155], v[148:151]\n"
"buffer_load_dwordx4 v202, s[44:47], s75 offen offset:2048 lds\n"
"v_mfma_f32_16x16x32_bf16 v[180:183], a[52:55], a[156:159], v[180:183]\n"
"v_mfma_f32_16x16x32_bf16 v[24:27], a[56:59], a[136:139], v[24:27]\n"
"v_mfma_f32_16x16x32_bf16 v[56:59], a[56:59], a[140:143], v[56:59]\n"
"v_mfma_f32_16x16x32_bf16 v[88:91], a[56:59], a[144:147], v[88:91]\n"
"buffer_load_dwordx4 v203, s[44:47], s75 offen offset:3072 lds\n"
"v_mfma_f32_16x16x32_bf16 v[120:123], a[56:59], a[148:151], v[120:123]\n"
"v_mfma_f32_16x16x32_bf16 v[152:155], a[56:59], a[152:155], v[152:155]\n"
"v_mfma_f32_16x16x32_bf16 v[184:187], a[56:59], a[156:159], v[184:187]\n"
"v_mfma_f32_16x16x32_bf16 v[28:31], a[60:63], a[136:139], v[28:31]\n"
"v_mfma_f32_16x16x32_bf16 v[60:63], a[60:63], a[140:143], v[60:63]\n"
"v_mfma_f32_16x16x32_bf16 v[92:95], a[60:63], a[144:147], v[92:95]\n"
"v_mfma_f32_16x16x32_bf16 v[124:127], a[60:63], a[148:151], v[124:127]\n"
"v_mfma_f32_16x16x32_bf16 v[156:159], a[60:63], a[152:155], v[156:159]\n"
"v_mfma_f32_16x16x32_bf16 v[188:191], a[60:63], a[156:159], v[188:191]\n"
"s_waitcnt vmcnt(23) lgkmcnt(0)\n"
"s_barrier\n"
"v_mfma_f32_16x16x32_bf16 v[0:3], a[0:3], a[64:67], v[0:3]\n"
"s_add_u32 m0, s68, 0x24000\n"
"s_add_u32 s75, s27, 0xc0000\n"
"ds_read_b64_tr_b16 a[32:33], v204 offset:0\n"
"v_mfma_f32_16x16x32_bf16 v[32:35], a[0:3], a[68:71], v[32:35]\n"
"ds_read_b64_tr_b16 a[34:35], v204 offset:256\n"
"v_mfma_f32_16x16x32_bf16 v[64:67], a[0:3], a[72:75], v[64:67]\n"
"ds_read_b64_tr_b16 a[36:37], v207 offset:0\n"
"buffer_load_dwordx4 v200, s[44:47], s75 offen offset:0 lds\n"
"v_mfma_f32_16x16x32_bf16 v[96:99], a[0:3], a[76:79], v[96:99]\n"
"ds_read_b64_tr_b16 a[38:39], v207 offset:256\n"
"v_mfma_f32_16x16x32_bf16 v[128:131], a[0:3], a[80:83], v[128:131]\n"
"ds_read_b64_tr_b16 a[40:41], v204 offset:512\n"
"v_mfma_f32_16x16x32_bf16 v[160:163], a[0:3], a[84:87], v[160:163]\n"
"ds_read_b64_tr_b16 a[42:43], v204 offset:768\n"
"v_mfma_f32_16x16x32_bf16 v[4:7], a[4:7], a[64:67], v[4:7]\n"
"ds_read_b64_tr_b16 a[44:45], v207 offset:512\n"
"buffer_load_dwordx4 v201, s[44:47], s75 offen offset:1024 lds\n"
"v_mfma_f32_16x16x32_bf16 v[36:39], a[4:7], a[68:71], v[36:39]\n"
"ds_read_b64_tr_b16 a[46:47], v207 offset:768\n"
"v_mfma_f32_16x16x32_bf16 v[68:71], a[4:7], a[72:75], v[68:71]\n"
"ds_read_b64_tr_b16 a[48:49], v204 offset:1024\n"
"v_mfma_f32_16x16x32_bf16 v[100:103], a[4:7], a[76:79], v[100:103]\n"
"ds_read_b64_tr_b16 a[50:51], v204 offset:1280\n"
"v_mfma_f32_16x16x32_bf16 v[132:135], a[4:7], a[80:83], v[132:135]\n"
"ds_read_b64_tr_b16 a[52:53], v207 offset:1024\n"
"buffer_load_dwordx4 v202, s[44:47], s75 offen offset:2048 lds\n"
"v_mfma_f32_16x16x32_bf16 v[164:167], a[4:7], a[84:87], v[164:167]\n"
"ds_read_b64_tr_b16 a[54:55], v207 offset:1280\n"
"v_mfma_f32_16x16x32_bf16 v[8:11], a[8:11], a[64:67], v[8:11]\n"
"ds_read_b64_tr_b16 a[56:57], v204 offset:1536\n"
"v_mfma_f32_16x16x32_bf16 v[40:43], a[8:11], a[68:71], v[40:43]\n"
"ds_read_b64_tr_b16 a[58:59], v204 offset:1792\n"
"v_mfma_f32_16x16x32_bf16 v[72:75], a[8:11], a[72:75], v[72:75]\n"
"ds_read_b64_tr_b16 a[60:61], v207 offset:1536\n"
"buffer_load_dwordx4 v203, s[44:47], s75 offen offset:3072 lds\n"
"v_mfma_f32_16x16x32_bf16 v[104:107], a[8:11], a[76:79], v[104:107]\n"
"ds_read_b64_tr_b16 a[62:63], v207 offset:1792\n"
"v_mfma_f32_16x16x32_bf16 v[136:139], a[8:11], a[80:83], v[136:139]\n"
"ds_read_b128 a[112:115], v198 offset:0\n"
"v_mfma_f32_16x16x32_bf16 v[168:171], a[8:11], a[84:87], v[168:171]\n"
"ds_read_b128 a[116:119], v198 offset:2048\n"
"v_mfma_f32_16x16x32_bf16 v[12:15], a[12:15], a[64:67], v[12:15]\n"
"ds_read_b128 a[120:123], v198 offset:4096\n"
"v_mfma_f32_16x16x32_bf16 v[44:47], a[12:15], a[68:71], v[44:47]\n"
"ds_read_b128 a[124:127], v198 offset:6144\n"
"v_mfma_f32_16x16x32_bf16 v[76:79], a[12:15], a[72:75], v[76:79]\n"
"ds_read_b128 a[128:131], v198 offset:8192\n"
"v_mfma_f32_16x16x32_bf16 v[108:111], a[12:15], a[76:79], v[108:111]\n"
"ds_read_b128 a[132:135], v198 offset:10240\n"
"v_mfma_f32_16x16x32_bf16 v[140:143], a[12:15], a[80:83], v[140:143]\n"
"ds_read_b128 a[136:139], v198 offset:512\n"
"v_mfma_f32_16x16x32_bf16 v[172:175], a[12:15], a[84:87], v[172:175]\n"
"ds_read_b128 a[140:143], v198 offset:2560\n"
"v_mfma_f32_16x16x32_bf16 v[16:19], a[16:19], a[64:67], v[16:19]\n"
"ds_read_b128 a[144:147], v198 offset:4608\n"
"v_mfma_f32_16x16x32_bf16 v[48:51], a[16:19], a[68:71], v[48:51]\n"
"ds_read_b128 a[148:151], v198 offset:6656\n"
"v_mfma_f32_16x16x32_bf16 v[80:83], a[16:19], a[72:75], v[80:83]\n"
"ds_read_b128 a[152:155], v198 offset:8704\n"
"v_mfma_f32_16x16x32_bf16 v[112:115], a[16:19], a[76:79], v[112:115]\n"
"ds_read_b128 a[156:159], v198 offset:10752\n"
"v_mfma_f32_16x16x32_bf16 v[144:147], a[16:19], a[80:83], v[144:147]\n"
"v_mfma_f32_16x16x32_bf16 v[176:179], a[16:19], a[84:87], v[176:179]\n"
"v_mfma_f32_16x16x32_bf16 v[20:23], a[20:23], a[64:67], v[20:23]\n"
"v_mfma_f32_16x16x32_bf16 v[52:55], a[20:23], a[68:71], v[52:55]\n"
"v_mfma_f32_16x16x32_bf16 v[84:87], a[20:23], a[72:75], v[84:87]\n"
"v_mfma_f32_16x16x32_bf16 v[116:119], a[20:23], a[76:79], v[116:119]\n"
"v_mfma_f32_16x16x32_bf16 v[148:151], a[20:23], a[80:83], v[148:151]\n"
"v_mfma_f32_16x16x32_bf16 v[180:183], a[20:23], a[84:87], v[180:183]\n"
"v_mfma_f32_16x16x32_bf16 v[24:27], a[24:27], a[64:67], v[24:27]\n"
"v_mfma_f32_16x16x32_bf16 v[56:59], a[24:27], a[68:71], v[56:59]\n"
"v_mfma_f32_16x16x32_bf16 v[88:91], a[24:27], a[72:75], v[88:91]\n"
"v_mfma_f32_16x16x32_bf16 v[120:123], a[24:27], a[76:79], v[120:123]\n"
"v_mfma_f32_16x16x32_bf16 v[152:155], a[24:27], a[80:83], v[152:155]\n"
"v_mfma_f32_16x16x32_bf16 v[184:187], a[24:27], a[84:87], v[184:187]\n"
"v_mfma_f32_16x16x32_bf16 v[28:31], a[28:31], a[64:67], v[28:31]\n"
"v_mfma_f32_16x16x32_bf16 v[60:63], a[28:31], a[68:71], v[60:63]\n"
"v_mfma_f32_16x16x32_bf16 v[92:95], a[28:31], a[72:75], v[92:95]\n"
"v_mfma_f32_16x16x32_bf16 v[124:127], a[28:31], a[76:79], v[124:127]\n"
"v_mfma_f32_16x16x32_bf16 v[156:159], a[28:31], a[80:83], v[156:159]\n"
"v_mfma_f32_16x16x32_bf16 v[188:191], a[28:31], a[84:87], v[188:191]\n"
"s_waitcnt vmcnt(27) lgkmcnt(0)\n"
"s_barrier\n"
"v_mfma_f32_16x16x32_bf16 v[0:3], a[32:35], a[88:91], v[0:3]\n"
"s_add_u32 s70, s24, 128\n"
"s_add_u32 m0, s69, 0x10000\n"
"ds_read_b64_tr_b16 a[0:1], v204 offset:16384\n"
"v_mfma_f32_16x16x32_bf16 v[32:35], a[32:35], a[92:95], v[32:35]\n"
"ds_read_b64_tr_b16 a[2:3], v204 offset:16640\n"
"v_mfma_f32_16x16x32_bf16 v[64:67], a[32:35], a[96:99], v[64:67]\n"
"ds_read_b64_tr_b16 a[4:5], v207 offset:16384\n"
"buffer_load_dwordx4 v192, s[40:43], s70 offen offset:0 lds\n"
"v_mfma_f32_16x16x32_bf16 v[96:99], a[32:35], a[100:103], v[96:99]\n"
"ds_read_b64_tr_b16 a[6:7], v207 offset:16640\n"
"v_mfma_f32_16x16x32_bf16 v[128:131], a[32:35], a[104:107], v[128:131]\n"
"ds_read_b64_tr_b16 a[8:9], v204 offset:16896\n"
"v_mfma_f32_16x16x32_bf16 v[160:163], a[32:35], a[108:111], v[160:163]\n"
"ds_read_b64_tr_b16 a[10:11], v204 offset:17152\n"
"v_mfma_f32_16x16x32_bf16 v[4:7], a[36:39], a[88:91], v[4:7]\n"
"ds_read_b64_tr_b16 a[12:13], v207 offset:16896\n"
"buffer_load_dwordx4 v193, s[40:43], s70 offen offset:1024 lds\n"
"v_mfma_f32_16x16x32_bf16 v[36:39], a[36:39], a[92:95], v[36:39]\n"
"ds_read_b64_tr_b16 a[14:15], v207 offset:17152\n"
"v_mfma_f32_16x16x32_bf16 v[68:71], a[36:39], a[96:99], v[68:71]\n"
"ds_read_b64_tr_b16 a[16:17], v204 offset:17408\n"
"v_mfma_f32_16x16x32_bf16 v[100:103], a[36:39], a[100:103], v[100:103]\n"
"ds_read_b64_tr_b16 a[18:19], v204 offset:17664\n"
"v_mfma_f32_16x16x32_bf16 v[132:135], a[36:39], a[104:107], v[132:135]\n"
"ds_read_b64_tr_b16 a[20:21], v207 offset:17408\n"
"buffer_load_dwordx4 v194, s[40:43], s70 offen offset:2048 lds\n"
"v_mfma_f32_16x16x32_bf16 v[164:167], a[36:39], a[108:111], v[164:167]\n"
"ds_read_b64_tr_b16 a[22:23], v207 offset:17664\n"
"v_mfma_f32_16x16x32_bf16 v[8:11], a[40:43], a[88:91], v[8:11]\n"
"ds_read_b64_tr_b16 a[24:25], v204 offset:17920\n"
"v_mfma_f32_16x16x32_bf16 v[40:43], a[40:43], a[92:95], v[40:43]\n"
"ds_read_b64_tr_b16 a[26:27], v204 offset:18176\n"
"v_mfma_f32_16x16x32_bf16 v[72:75], a[40:43], a[96:99], v[72:75]\n"
"ds_read_b64_tr_b16 a[28:29], v207 offset:17920\n"
"buffer_load_dwordx4 v195, s[40:43], s70 offen offset:3072 lds\n"
"v_mfma_f32_16x16x32_bf16 v[104:107], a[40:43], a[100:103], v[104:107]\n"
"s_add_u32 m0, s69, 0x11000\n"
"ds_read_b64_tr_b16 a[30:31], v207 offset:18176\n"
"v_mfma_f32_16x16x32_bf16 v[136:139], a[40:43], a[104:107], v[136:139]\n"
"v_mfma_f32_16x16x32_bf16 v[168:171], a[40:43], a[108:111], v[168:171]\n"
"v_mfma_f32_16x16x32_bf16 v[12:15], a[44:47], a[88:91], v[12:15]\n"
"buffer_load_dwordx4 v196, s[40:43], s70 offen offset:0 lds\n"
"v_mfma_f32_16x16x32_bf16 v[44:47], a[44:47], a[92:95], v[44:47]\n"
"v_mfma_f32_16x16x32_bf16 v[76:79], a[44:47], a[96:99], v[76:79]\n"
"v_mfma_f32_16x16x32_bf16 v[108:111], a[44:47], a[100:103], v[108:111]\n"
"v_mfma_f32_16x16x32_bf16 v[140:143], a[44:47], a[104:107], v[140:143]\n"
"buffer_load_dwordx4 v197, s[40:43], s70 offen offset:1024 lds\n"
"v_mfma_f32_16x16x32_bf16 v[172:175], a[44:47], a[108:111], v[172:175]\n"
"v_mfma_f32_16x16x32_bf16 v[16:19], a[48:51], a[88:91], v[16:19]\n"
"v_mfma_f32_16x16x32_bf16 v[48:51], a[48:51], a[92:95], v[48:51]\n"
"v_mfma_f32_16x16x32_bf16 v[80:83], a[48:51], a[96:99], v[80:83]\n"
"v_mfma_f32_16x16x32_bf16 v[112:115], a[48:51], a[100:103], v[112:115]\n"
"v_mfma_f32_16x16x32_bf16 v[144:147], a[48:51], a[104:107], v[144:147]\n"
"v_mfma_f32_16x16x32_bf16 v[176:179], a[48:51], a[108:111], v[176:179]\n"
"v_mfma_f32_16x16x32_bf16 v[20:23], a[52:55], a[88:91], v[20:23]\n"
"v_mfma_f32_16x16x32_bf16 v[52:55], a[52:55], a[92:95], v[52:55]\n"
"v_mfma_f32_16x16x32_bf16 v[84:87], a[52:55], a[96:99], v[84:87]\n"
"v_mfma_f32_16x16x32_bf16 v[116:119], a[52:55], a[100:103], v[116:119]\n"
"v_mfma_f32_16x16x32_bf16 v[148:151], a[52:55], a[104:107], v[148:151]\n"
"v_mfma_f32_16x16x32_bf16 v[180:183], a[52:55], a[108:111], v[180:183]\n"
"v_mfma_f32_16x16x32_bf16 v[24:27], a[56:59], a[88:91], v[24:27]\n"
"v_mfma_f32_16x16x32_bf16 v[56:59], a[56:59], a[92:95], v[56:59]\n"
"v_mfma_f32_16x16x32_bf16 v[88:91], a[56:59], a[96:99], v[88:91]\n"
"v_mfma_f32_16x16x32_bf16 v[120:123], a[56:59], a[100:103], v[120:123]\n"
"v_mfma_f32_16x16x32_bf16 v[152:155], a[56:59], a[104:107], v[152:155]\n"
"v_mfma_f32_16x16x32_bf16 v[184:187], a[56:59], a[108:111], v[184:187]\n"
"v_mfma_f32_16x16x32_bf16 v[28:31], a[60:63], a[88:91], v[28:31]\n"
"v_mfma_f32_16x16x32_bf16 v[60:63], a[60:63], a[92:95], v[60:63]\n"
"v_mfma_f32_16x16x32_bf16 v[92:95], a[60:63], a[96:99], v[92:95]\n"
"v_mfma_f32_16x16x32_bf16 v[124:127], a[60:63], a[100:103], v[124:127]\n"
"v_mfma_f32_16x16x32_bf16 v[156:159], a[60:63], a[104:107], v[156:159]\n"
"v_mfma_f32_16x16x32_bf16 v[188:191], a[60:63], a[108:111], v[188:191]\n"
"s_waitcnt vmcnt(14) lgkmcnt(0)\n"
"s_barrier\n"
"v_mfma_f32_16x16x32_bf16 v[0:3], a[0:3], a[112:115], v[0:3]\n"
"s_add_u32 m0, s59, 0x0\n"
"ds_read_b64_tr_b16 a[32:33], v204 offset:32768\n"
"v_mfma_f32_16x16x32_bf16 v[32:35], a[0:3], a[116:119], v[32:35]\n"
"ds_read_b64_tr_b16 a[34:35], v204 offset:33024\n"
"v_mfma_f32_16x16x32_bf16 v[64:67], a[0:3], a[120:123], v[64:67]\n"
"ds_read_b64_tr_b16 a[36:37], v207 offset:32768\n"
"buffer_load_dwordx4 v246, s[28:31], s56 offen offset:0 nt lds\n"
"v_mfma_f32_16x16x32_bf16 v[96:99], a[0:3], a[124:127], v[96:99]\n"
"ds_read_b64_tr_b16 a[38:39], v207 offset:33024\n"
"v_mfma_f32_16x16x32_bf16 v[128:131], a[0:3], a[128:131], v[128:131]\n"
"s_add_u32 s58, s59, 0x20000\n"
"s_lshl_b32 s71, s67, 16\n"
"ds_read_b64_tr_b16 a[40:41], v204 offset:33280\n"
"v_mfma_f32_16x16x32_bf16 v[160:163], a[0:3], a[132:135], v[160:163]\n"
"s_add_u32 s40, s88, s71\n"
"s_addc_u32 s41, s89, 0\n"
"ds_read_b64_tr_b16 a[42:43], v204 offset:33536\n"
"v_mfma_f32_16x16x32_bf16 v[4:7], a[4:7], a[112:115], v[4:7]\n"
"s_and_b32 s41, s41, 0xffff\n"
"s_mov_b32 s42, 0x50000\n"
"ds_read_b64_tr_b16 a[44:45], v207 offset:33280\n"
"v_mfma_f32_16x16x32_bf16 v[36:39], a[4:7], a[116:119], v[36:39]\n"
"s_lshl_b32 s68, s67, 13\n"
"s_add_u32 s68, s68, s66\n"
"ds_read_b64_tr_b16 a[46:47], v207 offset:33536\n"
"v_mfma_f32_16x16x32_bf16 v[68:71], a[4:7], a[120:123], v[68:71]\n"
"s_mov_b32 s26, s76\n"
"s_mov_b32 s27, s76\n"
"ds_read_b64_tr_b16 a[48:49], v204 offset:33792\n"
"v_mfma_f32_16x16x32_bf16 v[100:103], a[4:7], a[124:127], v[100:103]\n"
"s_and_b32 s71, s85, 7\n"
"s_lshl_b32 s71, s71, 13\n"
"ds_read_b64_tr_b16 a[50:51], v204 offset:34048\n"
"v_mfma_f32_16x16x32_bf16 v[132:135], a[4:7], a[128:131], v[132:135]\n"
"s_add_u32 m0, s59, 0x0\n"
"s_add_u32 s71, s71, s96\n"
"ds_read_b64_tr_b16 a[52:53], v207 offset:33792\n"
"v_mfma_f32_16x16x32_bf16 v[164:167], a[4:7], a[132:135], v[164:167]\n"
"s_mov_b32 s70, 0\n"
"ds_read_b64_tr_b16 a[54:55], v207 offset:34048\n"
"v_mfma_f32_16x16x32_bf16 v[8:11], a[8:11], a[112:115], v[8:11]\n"
"ds_read_b64_tr_b16 a[56:57], v204 offset:34304\n"
"v_lshl_add_u32 v250, v255, 7, s71\n"
"v_mfma_f32_16x16x32_bf16 v[40:43], a[8:11], a[116:119], v[40:43]\n"
"ds_read_b64_tr_b16 a[58:59], v204 offset:34560\n"
"v_and_b32 v251, 3, v255\n"
"v_mfma_f32_16x16x32_bf16 v[72:75], a[8:11], a[120:123], v[72:75]\n"
"ds_read_b64_tr_b16 a[60:61], v207 offset:34304\n"
"buffer_load_dwordx4 v247, s[28:31], s56 offen offset:1024 nt lds\n"
"v_mfma_f32_16x16x32_bf16 v[104:107], a[8:11], a[124:127], v[104:107]\n"
"ds_read_b64_tr_b16 a[62:63], v207 offset:34560\n"
"v_cmp_eq_u32 vcc, 0, v251\n"
"v_mfma_f32_16x16x32_bf16 v[136:139], a[8:11], a[128:131], v[136:139]\n"
"ds_read_b128 a[64:67], v198 offset:40960\n"
"v_mov_b32 v251, 0x3f803f80\n"
"v_mfma_f32_16x16x32_bf16 v[168:171], a[8:11], a[132:135], v[168:171]\n"
"s_nop 0\n"
"ds_read_b128 a[68:71], v198 offset:43008\n"
"v_mfma_f32_16x16x32_bf16 v[12:15], a[12:15], a[112:115], v[12:15]\n"
"ds_read_b128 a[72:75], v198 offset:45056\n"
"v_cndmask_b32 v251, 0, v251, vcc\n"
"v_mfma_f32_16x16x32_bf16 v[44:47], a[12:15], a[116:119], v[44:47]\n"
"ds_read_b128 a[76:79], v198 offset:47104\n"
"v_mfma_f32_16x16x32_bf16 v[76:79], a[12:15], a[120:123], v[76:79]\n"
"ds_read_b128 a[80:83], v198 offset:49152\n"
"v_mfma_f32_16x16x32_bf16 v[108:111], a[12:15], a[124:127], v[108:111]\n"
"ds_read_b128 a[84:87], v198 offset:51200\n"
"v_mfma_f32_16x16x32_bf16 v[140:143], a[12:15], a[128:131], v[140:143]\n"
"s_add_u32 m0, s59, 0x0\n"
"ds_read_b128 a[88:91], v198 offset:41472\n"
"v_mfma_f32_16x16x32_bf16 v[172:175], a[12:15], a[132:135], v[172:175]\n"
"ds_read_b128 a[92:95], v198 offset:43520\n"
"v_mfma_f32_16x16x32_bf16 v[16:19], a[16:19], a[112:115], v[16:19]\n"
"ds_read_b128 a[96:99], v198 offset:45568\n"
"v_mfma_f32_16x16x32_bf16 v[48:51], a[16:19], a[116:119], v[48:51]\n"
"ds_read_b128 a[100:103], v198 offset:47616\n"
"v_mfma_f32_16x16x32_bf16 v[80:83], a[16:19], a[120:123], v[80:83]\n"
"ds_read_b128 a[104:107], v198 offset:49664\n"
"buffer_load_dwordx4 v248, s[28:31], s56 offen offset:2048 nt lds\n"
"v_mfma_f32_16x16x32_bf16 v[112:115], a[16:19], a[124:127], v[112:115]\n"
"ds_read_b128 a[108:111], v198 offset:51712\n"
"v_mfma_f32_16x16x32_bf16 v[144:147], a[16:19], a[128:131], v[144:147]\n"
"v_mfma_f32_16x16x32_bf16 v[176:179], a[16:19], a[132:135], v[176:179]\n"
"v_mfma_f32_16x16x32_bf16 v[20:23], a[20:23], a[112:115], v[20:23]\n"
"v_mfma_f32_16x16x32_bf16 v[52:55], a[20:23], a[116:119], v[52:55]\n"
"v_mfma_f32_16x16x32_bf16 v[84:87], a[20:23], a[120:123], v[84:87]\n"
"v_mfma_f32_16x16x32_bf16 v[116:119], a[20:23], a[124:127], v[116:119]\n"
"v_mfma_f32_16x16x32_bf16 v[148:151], a[20:23], a[128:131], v[148:151]\n"
"s_add_u32 m0, s59, 0x0\n"
"v_mfma_f32_16x16x32_bf16 v[180:183], a[20:23], a[132:135], v[180:183]\n"
"v_mfma_f32_16x16x32_bf16 v[24:27], a[24:27], a[112:115], v[24:27]\n"
"v_mfma_f32_16x16x32_bf16 v[56:59], a[24:27], a[116:119], v[56:59]\n"
"v_mfma_f32_16x16x32_bf16 v[88:91], a[24:27], a[120:123], v[88:91]\n"
"buffer_load_dwordx4 v249, s[28:31], s56 offen offset:3072 nt lds\n"
"v_mfma_f32_16x16x32_bf16 v[120:123], a[24:27], a[124:127], v[120:123]\n"
"v_mfma_f32_16x16x32_bf16 v[152:155], a[24:27], a[128:131], v[152:155]\n"
"v_mfma_f32_16x16x32_bf16 v[184:187], a[24:27], a[132:135], v[184:187]\n"
"v_mfma_f32_16x16x32_bf16 v[28:31], a[28:31], a[112:115], v[28:31]\n"
"v_mfma_f32_16x16x32_bf16 v[60:63], a[28:31], a[116:119], v[60:63]\n"
"v_mfma_f32_16x16x32_bf16 v[92:95], a[28:31], a[120:123], v[92:95]\n"
"v_mfma_f32_16x16x32_bf16 v[124:127], a[28:31], a[124:127], v[124:127]\n"
"v_mfma_f32_16x16x32_bf16 v[156:159], a[28:31], a[128:131], v[156:159]\n"
"v_mfma_f32_16x16x32_bf16 v[188:191], a[28:31], a[132:135], v[188:191]\n"
"s_waitcnt vmcnt(14) lgkmcnt(0)\n"
"s_barrier\n"
"v_mfma_f32_16x16x32_bf16 v[0:3], a[32:35], a[136:139], v[0:3]\n"
"s_add_u32 m0, s59, 0x1000\n"
"s_add_u32 s33, s56, 4096\n"
"ds_read_b64_tr_b16 a[0:1], v204 offset:49152\n"
"v_mfma_f32_16x16x32_bf16 v[32:35], a[32:35], a[140:143], v[32:35]\n"
"s_add_u32 s56, s56, 16384\n"
"s_and_b32 s56, s56, 0x3ffff\n"
"ds_read_b64_tr_b16 a[2:3], v204 offset:49408\n"
"v_mfma_f32_16x16x32_bf16 v[64:67], a[32:35], a[144:147], v[64:67]\n"
"ds_read_b64_tr_b16 a[4:5], v207 offset:49152\n"
"buffer_load_dwordx4 v246, s[28:31], s33 offen offset:0 nt lds\n"
"v_mfma_f32_16x16x32_bf16 v[96:99], a[32:35], a[148:151], v[96:99]\n"
"ds_read_b64_tr_b16 a[6:7], v207 offset:49408\n"
"v_mfma_f32_16x16x32_bf16 v[128:131], a[32:35], a[152:155], v[128:131]\n"
"ds_read_b64_tr_b16 a[8:9], v204 offset:49664\n"
"v_mfma_f32_16x16x32_bf16 v[160:163], a[32:35], a[156:159], v[160:163]\n"
"ds_read_b64_tr_b16 a[10:11], v204 offset:49920\n"
"v_mfma_f32_16x16x32_bf16 v[4:7], a[36:39], a[136:139], v[4:7]\n"
"ds_read_b64_tr_b16 a[12:13], v207 offset:49664\n"
"v_mfma_f32_16x16x32_bf16 v[36:39], a[36:39], a[140:143], v[36:39]\n"
"ds_read_b64_tr_b16 a[14:15], v207 offset:49920\n"
"v_mfma_f32_16x16x32_bf16 v[68:71], a[36:39], a[144:147], v[68:71]\n"
"ds_read_b64_tr_b16 a[16:17], v204 offset:50176\n"
"v_mfma_f32_16x16x32_bf16 v[100:103], a[36:39], a[148:151], v[100:103]\n"
"ds_read_b64_tr_b16 a[18:19], v204 offset:50432\n"
"v_mfma_f32_16x16x32_bf16 v[132:135], a[36:39], a[152:155], v[132:135]\n"
"s_add_u32 m0, s59, 0x1000\n"
"ds_read_b64_tr_b16 a[20:21], v207 offset:50176\n"
"v_mfma_f32_16x16x32_bf16 v[164:167], a[36:39], a[156:159], v[164:167]\n"
"ds_read_b64_tr_b16 a[22:23], v207 offset:50432\n"
"v_mfma_f32_16x16x32_bf16 v[8:11], a[40:43], a[136:139], v[8:11]\n"
"ds_read_b64_tr_b16 a[24:25], v204 offset:50688\n"
"v_mfma_f32_16x16x32_bf16 v[40:43], a[40:43], a[140:143], v[40:43]\n"
"ds_read_b64_tr_b16 a[26:27], v204 offset:50944\n"
"v_mfma_f32_16x16x32_bf16 v[72:75], a[40:43], a[144:147], v[72:75]\n"
"ds_read_b64_tr_b16 a[28:29], v207 offset:50688\n"
"buffer_load_dwordx4 v247, s[28:31], s33 offen offset:1024 nt lds\n"
"v_mfma_f32_16x16x32_bf16 v[104:107], a[40:43], a[148:151], v[104:107]\n"
"ds_read_b64_tr_b16 a[30:31], v207 offset:50944\n"
"v_mfma_f32_16x16x32_bf16 v[136:139], a[40:43], a[152:155], v[136:139]\n"
"ds_read_b64_tr_b16 a[160:161], v205 offset:24576\n"
"v_mfma_f32_16x16x32_bf16 v[168:171], a[40:43], a[156:159], v[168:171]\n"
"ds_read_b64_tr_b16 a[162:163], v205 offset:24832\n"
"v_mfma_f32_16x16x32_bf16 v[12:15], a[44:47], a[136:139], v[12:15]\n"
"ds_read_b64_tr_b16 a[164:165], v208 offset:24576\n"
"v_mfma_f32_16x16x32_bf16 v[44:47], a[44:47], a[140:143], v[44:47]\n"
"ds_read_b64_tr_b16 a[166:167], v208 offset:24832\n"
"v_mfma_f32_16x16x32_bf16 v[76:79], a[44:47], a[144:147], v[76:79]\n"
"ds_read_b64_tr_b16 a[168:169], v205 offset:25088\n"
"v_mfma_f32_16x16x32_bf16 v[108:111], a[44:47], a[148:151], v[108:111]\n"
"ds_read_b64_tr_b16 a[170:171], v205 offset:25344\n"
"v_mfma_f32_16x16x32_bf16 v[140:143], a[44:47], a[152:155], v[140:143]\n"
"s_add_u32 m0, s59, 0x1000\n"
"ds_read_b64_tr_b16 a[172:173], v208 offset:25088\n"
"v_mfma_f32_16x16x32_bf16 v[172:175], a[44:47], a[156:159], v[172:175]\n"
"ds_read_b64_tr_b16 a[174:175], v208 offset:25344\n"
"v_mfma_f32_16x16x32_bf16 v[16:19], a[48:51], a[136:139], v[16:19]\n"
"ds_read_b64_tr_b16 a[176:177], v205 offset:25600\n"
"v_mfma_f32_16x16x32_bf16 v[48:51], a[48:51], a[140:143], v[48:51]\n"
"ds_read_b64_tr_b16 a[178:179], v205 offset:25856\n"
"v_mfma_f32_16x16x32_bf16 v[80:83], a[48:51], a[144:147], v[80:83]\n"
"ds_read_b64_tr_b16 a[180:181], v208 offset:25600\n"
"buffer_load_dwordx4 v248, s[28:31], s33 offen offset:2048 nt lds\n"
"v_mfma_f32_16x16x32_bf16 v[112:115], a[48:51], a[148:151], v[112:115]\n"
"ds_read_b64_tr_b16 a[182:183], v208 offset:25856\n"
"v_mfma_f32_16x16x32_bf16 v[144:147], a[48:51], a[152:155], v[144:147]\n"
"ds_read_b64_tr_b16 a[184:185], v205 offset:26112\n"
"v_mfma_f32_16x16x32_bf16 v[176:179], a[48:51], a[156:159], v[176:179]\n"
"ds_read_b64_tr_b16 a[186:187], v205 offset:26368\n"
"v_mfma_f32_16x16x32_bf16 v[20:23], a[52:55], a[136:139], v[20:23]\n"
"ds_read_b64_tr_b16 a[188:189], v208 offset:26112\n"
"v_mfma_f32_16x16x32_bf16 v[52:55], a[52:55], a[140:143], v[52:55]\n"
"ds_read_b64_tr_b16 a[190:191], v208 offset:26368\n"
"v_mfma_f32_16x16x32_bf16 v[84:87], a[52:55], a[144:147], v[84:87]\n"
"v_mfma_f32_16x16x32_bf16 v[116:119], a[52:55], a[148:151], v[116:119]\n"
"v_mfma_f32_16x16x32_bf16 v[148:151], a[52:55], a[152:155], v[148:151]\n"
"s_add_u32 m0, s59, 0x1000\n"
"buffer_load_dword v254, v245, s[52:55], 0 offen sc1\n"
"v_mfma_f32_16x16x32_bf16 v[180:183], a[52:55], a[156:159], v[180:183]\n"
"v_mfma_f32_16x16x32_bf16 v[24:27], a[56:59], a[136:139], v[24:27]\n"
"v_mfma_f32_16x16x32_bf16 v[56:59], a[56:59], a[140:143], v[56:59]\n"
"v_mfma_f32_16x16x32_bf16 v[88:91], a[56:59], a[144:147], v[88:91]\n"
"buffer_load_dwordx4 v249, s[28:31], s33 offen offset:3072 nt lds\n"
"v_mfma_f32_16x16x32_bf16 v[120:123], a[56:59], a[148:151], v[120:123]\n"
"v_mfma_f32_16x16x32_bf16 v[152:155], a[56:59], a[152:155], v[152:155]\n"
"v_mfma_f32_16x16x32_bf16 v[184:187], a[56:59], a[156:159], v[184:187]\n"
"v_mfma_f32_16x16x32_bf16 v[28:31], a[60:63], a[136:139], v[28:31]\n"
"v_mfma_f32_16x16x32_bf16 v[60:63], a[60:63], a[140:143], v[60:63]\n"
"v_mfma_f32_16x16x32_bf16 v[92:95], a[60:63], a[144:147], v[92:95]\n"
"v_mfma_f32_16x16x32_bf16 v[124:127], a[60:63], a[148:151], v[124:127]\n"
"v_mfma_f32_16x16x32_bf16 v[156:159], a[60:63], a[152:155], v[156:159]\n"
"v_mfma_f32_16x16x32_bf16 v[188:191], a[60:63], a[156:159], v[188:191]\n"
"s_waitcnt vmcnt(9) lgkmcnt(0)\n"
"s_barrier\n"
"v_mfma_f32_16x16x32_bf16 v[0:3], a[0:3], a[64:67], v[0:3]\n"
"ds_read_b64_tr_b16 a[224:225], v206 offset:0\n"
"ds_read_b64_tr_b16 a[226:227], v206 offset:256\n"
"v_mfma_f32_16x16x32_bf16 v[4:7], a[4:7], a[64:67], v[4:7]\n"
"ds_read_b64_tr_b16 a[228:229], v209 offset:0\n"
"ds_read_b64_tr_b16 a[230:231], v209 offset:256\n"
"v_mfma_f32_16x16x32_bf16 v[8:11], a[8:11], a[64:67], v[8:11]\n"
"ds_read_b64_tr_b16 a[232:233], v206 offset:512\n"
"ds_read_b64_tr_b16 a[234:235], v206 offset:768\n"
"v_mfma_f32_16x16x32_bf16 v[12:15], a[12:15], a[64:67], v[12:15]\n"
"ds_read_b64_tr_b16 a[236:237], v209 offset:512\n"
"ds_read_b64_tr_b16 a[238:239], v209 offset:768\n"
"v_mfma_f32_16x16x32_bf16 v[16:19], a[16:19], a[64:67], v[16:19]\n"
"ds_read_b64_tr_b16 a[240:241], v206 offset:1024\n"
"ds_read_b64_tr_b16 a[242:243], v206 offset:1280\n"
"v_mfma_f32_16x16x32_bf16 v[20:23], a[20:23], a[64:67], v[20:23]\n"
"s_add_u32 m0, s59, 0x8000\n"
"ds_read_b64_tr_b16 a[244:245], v209 offset:1024\n"
"buffer_load_dwordx4 v246, s[36:39], s57 offen offset:0 nt lds\n"
"v_mfma_f32_16x16x32_bf16 v[24:27], a[24:27], a[64:67], v[24:27]\n"
"ds_read_b64_tr_b16 a[246:247], v209 offset:1280\n"
"ds_read_b64_tr_b16 a[248:249], v206 offset:1536\n"
"v_mfma_f32_16x16x32_bf16 v[28:31], a[28:31], a[64:67], v[28:31]\n"
"ds_read_b64_tr_b16 a[250:251], v206 offset:1792\n"
"ds_read_b64_tr_b16 a[252:253], v209 offset:1536\n"
"v_mfma_f32_16x16x32_bf16 v[0:3], a[160:163], a[88:91], v[0:3]\n"
"ds_read_b64_tr_b16 a[254:255], v209 offset:1792\n"
"ds_read_b128 a[112:115], v198 offset:0\n"
"v_mfma_f32_16x16x32_bf16 v[4:7], a[164:167], a[88:91], v[4:7]\n"
"ds_read_b128 a[136:139], v198 offset:512\n"
"ds_read_b64_tr_b16 a[32:33], v206 offset:16384\n"
"v_mfma_f32_16x16x32_bf16 v[8:11], a[168:171], a[88:91], v[8:11]\n"
"ds_read_b64_tr_b16 a[34:35], v206 offset:16640\n"
"ds_read_b64_tr_b16 a[36:37], v209 offset:16384\n"
"v_mfma_f32_16x16x32_bf16 v[12:15], a[172:175], a[88:91], v[12:15]\n"
"ds_read_b64_tr_b16 a[38:39], v209 offset:16640\n"
"ds_read_b64_tr_b16 a[40:41], v206 offset:16896\n"
"v_mfma_f32_16x16x32_bf16 v[16:19], a[176:179], a[88:91], v[16:19]\n"
"ds_read_b64_tr_b16 a[42:43], v206 offset:17152\n"
"ds_read_b64_tr_b16 a[44:45], v209 offset:16896\n"
"v_mfma_f32_16x16x32_bf16 v[20:23], a[180:183], a[88:91], v[20:23]\n"
"ds_read_b64_tr_b16 a[46:47], v209 offset:17152\n"
"ds_read_b64_tr_b16 a[48:49], v206 offset:17408\n"
"v_mfma_f32_16x16x32_bf16 v[24:27], a[184:187], a[88:91], v[24:27]\n"
"ds_read_b64_tr_b16 a[50:51], v206 offset:17664\n"
"ds_read_b64_tr_b16 a[52:53], v209 offset:17408\n"
"v_mfma_f32_16x16x32_bf16 v[28:31], a[188:191], a[88:91], v[28:31]\n"
"s_add_u32 m0, s59, 0x8000\n"
"ds_read_b64_tr_b16 a[54:55], v209 offset:17664\n"
"buffer_load_dwordx4 v247, s[36:39], s57 offen offset:1024 nt lds\n"
"v_mfma_f32_16x16x32_bf16 v[32:35], a[0:3], a[68:71], v[32:35]\n"
"ds_read_b64_tr_b16 a[56:57], v206 offset:17920\n"
"ds_read_b64_tr_b16 a[58:59], v206 offset:18176\n"
"v_mfma_f32_16x16x32_bf16 v[36:39], a[4:7], a[68:71], v[36:39]\n"
"ds_read_b64_tr_b16 a[60:61], v209 offset:17920\n"
"ds_read_b64_tr_b16 a[62:63], v209 offset:18176\n"
"v_mfma_f32_16x16x32_bf16 v[40:43], a[8:11], a[68:71], v[40:43]\n"
"ds_read_b128 a[116:119], v198 offset:2048\n"
"ds_read_b128 a[140:143], v198 offset:2560\n"
"v_mfma_f32_16x16x32_bf16 v[44:47], a[12:15], a[68:71], v[44:47]\n"
"ds_read_b128 a[120:123], v198 offset:4096\n"
"ds_read_b128 a[144:147], v198 offset:4608\n"
"v_mfma_f32_16x16x32_bf16 v[48:51], a[16:19], a[68:71], v[48:51]\n"
"ds_read_b128 a[124:127], v198 offset:6144\n"
"ds_read_b128 a[148:151], v198 offset:6656\n"
"v_mfma_f32_16x16x32_bf16 v[52:55], a[20:23], a[68:71], v[52:55]\n"
"ds_read_b128 a[128:131], v198 offset:8192\n"
"ds_read_b128 a[152:155], v198 offset:8704\n"
"v_mfma_f32_16x16x32_bf16 v[56:59], a[24:27], a[68:71], v[56:59]\n"
"ds_read_b128 a[132:135], v198 offset:10240\n"
"ds_read_b128 a[156:159], v198 offset:10752\n"
"v_mfma_f32_16x16x32_bf16 v[60:63], a[28:31], a[68:71], v[60:63]\n"
"v_mfma_f32_16x16x32_bf16 v[32:35], a[160:163], a[92:95], v[32:35]\n"
"v_mfma_f32_16x16x32_bf16 v[36:39], a[164:167], a[92:95], v[36:39]\n"
"v_mfma_f32_16x16x32_bf16 v[40:43], a[168:171], a[92:95], v[40:43]\n"
"v_mfma_f32_16x16x32_bf16 v[44:47], a[172:175], a[92:95], v[44:47]\n"
"v_mfma_f32_16x16x32_bf16 v[48:51], a[176:179], a[92:95], v[48:51]\n"
"v_mfma_f32_16x16x32_bf16 v[52:55], a[180:183], a[92:95], v[52:55]\n"
"v_mfma_f32_16x16x32_bf16 v[56:59], a[184:187], a[92:95], v[56:59]\n"
"v_mfma_f32_16x16x32_bf16 v[60:63], a[188:191], a[92:95], v[60:63]\n"
"s_waitcnt lgkmcnt(10)\n"
"v_mfma_f32_16x16x32_bf16 v[0:3], a[224:227], a[112:115], v[0:3]\n"
"v_mfma_f32_16x16x32_bf16 v[4:7], a[228:231], a[112:115], v[4:7]\n"
"v_mfma_f32_16x16x32_bf16 v[8:11], a[232:235], a[112:115], v[8:11]\n"
"v_mfma_f32_16x16x32_bf16 v[12:15], a[236:239], a[112:115], v[12:15]\n"
"v_mfma_f32_16x16x32_bf16 v[16:19], a[240:243], a[112:115], v[16:19]\n"
"v_mfma_f32_16x16x32_bf16 v[20:23], a[244:247], a[112:115], v[20:23]\n"
"v_mfma_f32_16x16x32_bf16 v[24:27], a[248:251], a[112:115], v[24:27]\n"
"v_mfma_f32_16x16x32_bf16 v[28:31], a[252:255], a[112:115], v[28:31]\n"
"v_mfma_f32_16x16x32_bf16 v[0:3], a[32:35], a[136:139], v[0:3]\n"
"v_mfma_f32_16x16x32_bf16 v[4:7], a[36:39], a[136:139], v[4:7]\n"
"v_mfma_f32_16x16x32_bf16 v[8:11], a[40:43], a[136:139], v[8:11]\n"
"v_mfma_f32_16x16x32_bf16 v[12:15], a[44:47], a[136:139], v[12:15]\n"
"v_mfma_f32_16x16x32_bf16 v[16:19], a[48:51], a[136:139], v[16:19]\n"
"v_mfma_f32_16x16x32_bf16 v[20:23], a[52:55], a[136:139], v[20:23]\n"
"v_mfma_f32_16x16x32_bf16 v[24:27], a[56:59], a[136:139], v[24:27]\n"
"v_mfma_f32_16x16x32_bf16 v[28:31], a[60:63], a[136:139], v[28:31]\n"
"s_waitcnt lgkmcnt(0)\n"
"v_mul_f32 v0, s76, v0\n"
"v_mul_f32 v1, s76, v1\n"
"v_mfma_f32_16x16x32_bf16 v[32:35], a[224:227], a[116:119], v[32:35]\n"
"v_mul_f32 v2, s76, v2\n"
"s_add_u32 m0, s59, 0x8000\n"
"v_mfma_f32_16x16x32_bf16 v[36:39], a[228:231], a[116:119], v[36:39]\n"
"v_mul_f32 v3, s76, v3\n"
"v_mfma_f32_16x16x32_bf16 v[40:43], a[232:235], a[116:119], v[40:43]\n"
"buffer_load_dwordx4 v248, s[36:39], s57 offen offset:2048 nt lds\n"
"v_mul_f32 v4, s76, v4\n"
"v_mfma_f32_16x16x32_bf16 v[44:47], a[236:239], a[116:119], v[44:47]\n"
"v_mul_f32 v5, s76, v5\n"
"v_mul_f32 v6, s76, v6\n"
"v_mfma_f32_16x16x32_bf16 v[48:51], a[240:243], a[116:119], v[48:51]\n"
"v_mul_f32 v7, s76, v7\n"
"v_cvt_pk_bf16_f32 v0, v0, v1\n"
"v_mfma_f32_16x16x32_bf16 v[52:55], a[244:247], a[116:119], v[52:55]\n"
"v_cvt_pk_bf16_f32 v1, v2, v3\n"
"v_cvt_pk_bf16_f32 v2, v4, v5\n"
"v_mfma_f32_16x16x32_bf16 v[56:59], a[248:251], a[116:119], v[56:59]\n"
"v_cvt_pk_bf16_f32 v3, v6, v7\n"
"v_mul_f32 v8, s76, v8\n"
"v_mfma_f32_16x16x32_bf16 v[60:63], a[252:255], a[116:119], v[60:63]\n"
"v_mul_f32 v9, s76, v9\n"
"v_mul_f32 v10, s76, v10\n"
"v_mfma_f32_16x16x32_bf16 v[32:35], a[32:35], a[140:143], v[32:35]\n"
"v_mul_f32 v11, s76, v11\n"
"v_mul_f32 v12, s76, v12\n"
"v_mfma_f32_16x16x32_bf16 v[36:39], a[36:39], a[140:143], v[36:39]\n"
"v_mul_f32 v13, s76, v13\n"
"v_mul_f32 v14, s76, v14\n"
"v_mfma_f32_16x16x32_bf16 v[40:43], a[40:43], a[140:143], v[40:43]\n"
"v_mul_f32 v15, s76, v15\n"
"v_permlane16_swap_b32 v0, v2\n"
"v_mfma_f32_16x16x32_bf16 v[44:47], a[44:47], a[140:143], v[44:47]\n"
"v_permlane16_swap_b32 v1, v3\n"
"s_add_u32 m0, s59, 0x8000\n"
"v_mfma_f32_16x16x32_bf16 v[48:51], a[48:51], a[140:143], v[48:51]\n"
"v_cvt_pk_bf16_f32 v4, v8, v9\n"
"v_mfma_f32_16x16x32_bf16 v[52:55], a[52:55], a[140:143], v[52:55]\n"
"buffer_load_dwordx4 v249, s[36:39], s57 offen offset:3072 nt lds\n"
"v_cvt_pk_bf16_f32 v5, v10, v11\n"
"v_mfma_f32_16x16x32_bf16 v[56:59], a[56:59], a[140:143], v[56:59]\n"
"v_cvt_pk_bf16_f32 v6, v12, v13\n"
"v_cvt_pk_bf16_f32 v7, v14, v15\n"
"v_mfma_f32_16x16x32_bf16 v[60:63], a[60:63], a[140:143], v[60:63]\n"
"v_mul_f32 v16, s76, v16\n"
"v_mul_f32 v17, s76, v17\n"
"v_mfma_f32_16x16x32_bf16 v[64:67], a[0:3], a[72:75], v[64:67]\n"
"v_mul_f32 v18, s76, v18\n"
"v_mul_f32 v19, s76, v19\n"
"v_mfma_f32_16x16x32_bf16 v[68:71], a[4:7], a[72:75], v[68:71]\n"
"v_mul_f32 v20, s76, v20\n"
"v_mul_f32 v21, s76, v21\n"
"v_mfma_f32_16x16x32_bf16 v[72:75], a[8:11], a[72:75], v[72:75]\n"
"v_mul_f32 v22, s76, v22\n"
"v_mul_f32 v23, s76, v23\n"
"v_mfma_f32_16x16x32_bf16 v[76:79], a[12:15], a[72:75], v[76:79]\n"
"v_permlane16_swap_b32 v4, v6\n"
"v_permlane16_swap_b32 v5, v7\n"
"v_mfma_f32_16x16x32_bf16 v[80:83], a[16:19], a[72:75], v[80:83]\n"
"v_cvt_pk_bf16_f32 v8, v16, v17\n"
"v_cvt_pk_bf16_f32 v9, v18, v19\n"
"v_mfma_f32_16x16x32_bf16 v[84:87], a[20:23], a[72:75], v[84:87]\n"
"v_cvt_pk_bf16_f32 v10, v20, v21\n"
"v_cvt_pk_bf16_f32 v11, v22, v23\n"
"v_mfma_f32_16x16x32_bf16 v[88:91], a[24:27], a[72:75], v[88:91]\n"
"s_add_u32 m0, s59, 0x9000\n"
"s_add_u32 s33, s57, 4096\n"
"s_add_u32 s57, s57, 16384\n"
"v_mfma_f32_16x16x32_bf16 v[92:95], a[28:31], a[72:75], v[92:95]\n"
"s_and_b32 s57, s57, 0x3ffff\n"
"v_mul_f32 v24, s76, v24\n"
"v_mfma_f32_16x16x32_bf16 v[64:67], a[160:163], a[96:99], v[64:67]\n"
"buffer_load_dwordx4 v246, s[36:39], s33 offen offset:0 nt lds\n"
"v_mul_f32 v25, s76, v25\n"
"v_mfma_f32_16x16x32_bf16 v[68:71], a[164:167], a[96:99], v[68:71]\n"
"v_mul_f32 v26, s76, v26\n"
"v_mul_f32 v27, s76, v27\n"
"v_mfma_f32_16x16x32_bf16 v[72:75], a[168:171], a[96:99], v[72:75]\n"
"v_mul_f32 v28, s76, v28\n"
"v_mul_f32 v29, s76, v29\n"
"v_mfma_f32_16x16x32_bf16 v[76:79], a[172:175], a[96:99], v[76:79]\n"
"v_mul_f32 v30, s76, v30\n"
"v_mul_f32 v31, s76, v31\n"
"v_mfma_f32_16x16x32_bf16 v[80:83], a[176:179], a[96:99], v[80:83]\n"
"v_permlane16_swap_b32 v8, v10\n"
"v_permlane16_swap_b32 v9, v11\n"
"v_mfma_f32_16x16x32_bf16 v[84:87], a[180:183], a[96:99], v[84:87]\n"
"v_cvt_pk_bf16_f32 v12, v24, v25\n"
"v_cvt_pk_bf16_f32 v13, v26, v27\n"
"v_mfma_f32_16x16x32_bf16 v[88:91], a[184:187], a[96:99], v[88:91]\n"
"v_cvt_pk_bf16_f32 v14, v28, v29\n"
"v_cvt_pk_bf16_f32 v15, v30, v31\n"
"v_mfma_f32_16x16x32_bf16 v[92:95], a[188:191], a[96:99], v[92:95]\n"
"s_nop 1\n"
"v_permlane16_swap_b32 v12, v14\n"
"v_mfma_f32_16x16x32_bf16 v[64:67], a[224:227], a[120:123], v[64:67]\n"
"v_permlane16_swap_b32 v13, v15\n"
"v_mul_f32 v32, s76, v32\n"
"v_mfma_f32_16x16x32_bf16 v[68:71], a[228:231], a[120:123], v[68:71]\n"
"v_mul_f32 v33, s76, v33\n"
"s_add_u32 m0, s59, 0x9000\n"
"v_mfma_f32_16x16x32_bf16 v[72:75], a[232:235], a[120:123], v[72:75]\n"
"v_mul_f32 v34, s76, v34\n"
"v_mfma_f32_16x16x32_bf16 v[76:79], a[236:239], a[120:123], v[76:79]\n"
"buffer_load_dwordx4 v247, s[36:39], s33 offen offset:1024 nt lds\n"
"v_mul_f32 v35, s76, v35\n"
"v_mfma_f32_16x16x32_bf16 v[80:83], a[240:243], a[120:123], v[80:83]\n"
"v_mul_f32 v36, s76, v36\n"
"v_mul_f32 v37, s76, v37\n"
"v_mfma_f32_16x16x32_bf16 v[84:87], a[244:247], a[120:123], v[84:87]\n"
"v_mul_f32 v38, s76, v38\n"
"v_mul_f32 v39, s76, v39\n"
"v_mfma_f32_16x16x32_bf16 v[88:91], a[248:251], a[120:123], v[88:91]\n"
"v_cvt_pk_bf16_f32 v16, v32, v33\n"
"v_cvt_pk_bf16_f32 v17, v34, v35\n"
"v_mfma_f32_16x16x32_bf16 v[92:95], a[252:255], a[120:123], v[92:95]\n"
"v_cvt_pk_bf16_f32 v18, v36, v37\n"
"v_cvt_pk_bf16_f32 v19, v38, v39\n"
"v_mfma_f32_16x16x32_bf16 v[64:67], a[32:35], a[144:147], v[64:67]\n"
"v_mul_f32 v40, s76, v40\n"
"v_mul_f32 v41, s76, v41\n"
"v_mfma_f32_16x16x32_bf16 v[68:71], a[36:39], a[144:147], v[68:71]\n"
"v_mul_f32 v42, s76, v42\n"
"v_mul_f32 v43, s76, v43\n"
"v_mfma_f32_16x16x32_bf16 v[72:75], a[40:43], a[144:147], v[72:75]\n"
"v_mul_f32 v44, s76, v44\n"
"v_mul_f32 v45, s76, v45\n"
"v_mfma_f32_16x16x32_bf16 v[76:79], a[44:47], a[144:147], v[76:79]\n"
"v_mul_f32 v46, s76, v46\n"
"v_mul_f32 v47, s76, v47\n"
"v_mfma_f32_16x16x32_bf16 v[80:83], a[48:51], a[144:147], v[80:83]\n"
"s_add_u32 m0, s59, 0x9000\n"
"v_permlane16_swap_b32 v16, v18\n"
"v_mfma_f32_16x16x32_bf16 v[84:87], a[52:55], a[144:147], v[84:87]\n"
"buffer_load_dwordx4 v248, s[36:39], s33 offen offset:2048 nt lds\n"
"v_permlane16_swap_b32 v17, v19\n"
"v_mfma_f32_16x16x32_bf16 v[88:91], a[56:59], a[144:147], v[88:91]\n"
"v_cvt_pk_bf16_f32 v20, v40, v41\n"
"v_cvt_pk_bf16_f32 v21, v42, v43\n"
"v_mfma_f32_16x16x32_bf16 v[92:95], a[60:63], a[144:147], v[92:95]\n"
"v_cvt_pk_bf16_f32 v22, v44, v45\n"
"v_cvt_pk_bf16_f32 v23, v46, v47\n"
"v_mfma_f32_16x16x32_bf16 v[96:99], a[0:3], a[76:79], v[96:99]\n"
"v_mul_f32 v48, s76, v48\n"
"v_mul_f32 v49, s76, v49\n"
"v_mfma_f32_16x16x32_bf16 v[100:103], a[4:7], a[76:79], v[100:103]\n"
"v_mul_f32 v50, s76, v50\n"
"v_mul_f32 v51, s76, v51\n"
"v_mfma_f32_16x16x32_bf16 v[104:107], a[8:11], a[76:79], v[104:107]\n"
"v_mul_f32 v52, s76, v52\n"
"v_mul_f32 v53, s76, v53\n"
"v_mfma_f32_16x16x32_bf16 v[108:111], a[12:15], a[76:79], v[108:111]\n"
"v_mul_f32 v54, s76, v54\n"
"v_mul_f32 v55, s76, v55\n"
"v_mfma_f32_16x16x32_bf16 v[112:115], a[16:19], a[76:79], v[112:115]\n"
"v_permlane16_swap_b32 v20, v22\n"
"v_permlane16_swap_b32 v21, v23\n"
"v_mfma_f32_16x16x32_bf16 v[116:119], a[20:23], a[76:79], v[116:119]\n"
"v_cvt_pk_bf16_f32 v24, v48, v49\n"
"v_cvt_pk_bf16_f32 v25, v50, v51\n"
"v_mfma_f32_16x16x32_bf16 v[120:123], a[24:27], a[76:79], v[120:123]\n"
"v_cvt_pk_bf16_f32 v26, v52, v53\n"
"s_add_u32 m0, s59, 0x9000\n"
"v_mfma_f32_16x16x32_bf16 v[124:127], a[28:31], a[76:79], v[124:127]\n"
"v_cvt_pk_bf16_f32 v27, v54, v55\n"
"v_mfma_f32_16x16x32_bf16 v[96:99], a[160:163], a[100:103], v[96:99]\n"
"buffer_load_dwordx4 v249, s[36:39], s33 offen offset:3072 nt lds\n"
"v_mul_f32 v56, s76, v56\n"
"v_mfma_f32_16x16x32_bf16 v[100:103], a[164:167], a[100:103], v[100:103]\n"
"v_mul_f32 v57, s76, v57\n"
"v_mul_f32 v58, s76, v58\n"
"v_mfma_f32_16x16x32_bf16 v[104:107], a[168:171], a[100:103], v[104:107]\n"
"v_mul_f32 v59, s76, v59\n"
"v_mul_f32 v60, s76, v60\n"
"v_mfma_f32_16x16x32_bf16 v[108:111], a[172:175], a[100:103], v[108:111]\n"
"v_mul_f32 v61, s76, v61\n"
"v_mul_f32 v62, s76, v62\n"
"v_mfma_f32_16x16x32_bf16 v[112:115], a[176:179], a[100:103], v[112:115]\n"
"v_mul_f32 v63, s76, v63\n"
"v_permlane16_swap_b32 v24, v26\n"
"v_mfma_f32_16x16x32_bf16 v[116:119], a[180:183], a[100:103], v[116:119]\n"
"v_permlane16_swap_b32 v25, v27\n"
"v_cvt_pk_bf16_f32 v28, v56, v57\n"
"v_mfma_f32_16x16x32_bf16 v[120:123], a[184:187], a[100:103], v[120:123]\n"
"v_cvt_pk_bf16_f32 v29, v58, v59\n"
"v_cvt_pk_bf16_f32 v30, v60, v61\n"
"v_mfma_f32_16x16x32_bf16 v[124:127], a[188:191], a[100:103], v[124:127]\n"
"v_cvt_pk_bf16_f32 v31, v62, v63\n"
"s_nop 1\n"
"v_mfma_f32_16x16x32_bf16 v[96:99], a[224:227], a[124:127], v[96:99]\n"
"v_permlane16_swap_b32 v28, v30\n"
"v_permlane16_swap_b32 v29, v31\n"
"v_mfma_f32_16x16x32_bf16 v[100:103], a[228:231], a[124:127], v[100:103]\n"
"s_waitcnt vmcnt(8)\n"
"s_barrier\n"
"v_mfma_f32_16x16x32_bf16 v[104:107], a[232:235], a[124:127], v[104:107]\n"
"ds_read_b128 a[192:195], v240 offset:0\n"
"ds_read_b128 a[196:199], v240 offset:512\n"
"v_mfma_f32_16x16x32_bf16 v[108:111], a[236:239], a[124:127], v[108:111]\n"
"ds_read_b128 a[200:203], v240 offset:1024\n"
"ds_read_b128 a[204:207], v240 offset:1536\n"
"v_mfma_f32_16x16x32_bf16 v[112:115], a[240:243], a[124:127], v[112:115]\n"
"ds_read_b128 a[208:211], v240 offset:256\n"
"ds_read_b128 a[212:215], v240 offset:768\n"
"v_mfma_f32_16x16x32_bf16 v[116:119], a[244:247], a[124:127], v[116:119]\n"
"ds_read_b128 a[216:219], v240 offset:1280\n"
"ds_read_b128 a[220:223], v240 offset:1792\n"
"v_mfma_f32_16x16x32_bf16 v[120:123], a[248:251], a[124:127], v[120:123]\n"
"v_mul_f32 v64, s76, v64\n"
"v_mul_f32 v65, s76, v65\n"
"v_mfma_f32_16x16x32_bf16 v[124:127], a[252:255], a[124:127], v[124:127]\n"
"v_mul_f32 v66, s76, v66\n"
"v_mul_f32 v67, s76, v67\n"
"v_mfma_f32_16x16x32_bf16 v[96:99], a[32:35], a[148:151], v[96:99]\n"
"v_mul_f32 v68, s76, v68\n"
"v_mul_f32 v69, s76, v69\n"
"v_mfma_f32_16x16x32_bf16 v[100:103], a[36:39], a[148:151], v[100:103]\n"
"v_mul_f32 v70, s76, v70\n"
"v_mul_f32 v71, s76, v71\n"
"v_mfma_f32_16x16x32_bf16 v[104:107], a[40:43], a[148:151], v[104:107]\n"
"v_cvt_pk_bf16_f32 v32, v64, v65\n"
"v_cvt_pk_bf16_f32 v33, v66, v67\n"
"v_mfma_f32_16x16x32_bf16 v[108:111], a[44:47], a[148:151], v[108:111]\n"
"v_cvt_pk_bf16_f32 v34, v68, v69\n"
"v_cvt_pk_bf16_f32 v35, v70, v71\n"
"v_mfma_f32_16x16x32_bf16 v[112:115], a[48:51], a[148:151], v[112:115]\n"
"v_mul_f32 v72, s76, v72\n"
"v_mul_f32 v73, s76, v73\n"
"v_mfma_f32_16x16x32_bf16 v[116:119], a[52:55], a[148:151], v[116:119]\n"
"v_mul_f32 v74, s76, v74\n"
"v_mul_f32 v75, s76, v75\n"
"v_mfma_f32_16x16x32_bf16 v[120:123], a[56:59], a[148:151], v[120:123]\n"
"v_mul_f32 v76, s76, v76\n"
"v_mul_f32 v77, s76, v77\n"
"v_mfma_f32_16x16x32_bf16 v[124:127], a[60:63], a[148:151], v[124:127]\n"
"v_mul_f32 v78, s76, v78\n"
"v_mul_f32 v79, s76, v79\n"
"v_mfma_f32_16x16x32_bf16 v[128:131], a[0:3], a[80:83], v[128:131]\n"
"v_permlane16_swap_b32 v32, v34\n"
"v_permlane16_swap_b32 v33, v35\n"
"v_mfma_f32_16x16x32_bf16 v[132:135], a[4:7], a[80:83], v[132:135]\n"
"s_add_u32 m0, s59, 0x10000\n"
"v_cvt_pk_bf16_f32 v36, v72, v73\n"
"v_mfma_f32_16x16x32_bf16 v[136:139], a[8:11], a[80:83], v[136:139]\n"
"buffer_load_dwordx4 v246, s[28:31], s56 offen offset:0 nt lds\n"
"v_cvt_pk_bf16_f32 v37, v74, v75\n"
"v_mfma_f32_16x16x32_bf16 v[140:143], a[12:15], a[80:83], v[140:143]\n"
"v_cvt_pk_bf16_f32 v38, v76, v77\n"
"v_cvt_pk_bf16_f32 v39, v78, v79\n"
"v_mfma_f32_16x16x32_bf16 v[144:147], a[16:19], a[80:83], v[144:147]\n"
"v_mul_f32 v80, s76, v80\n"
"v_mul_f32 v81, s76, v81\n"
"v_mfma_f32_16x16x32_bf16 v[148:151], a[20:23], a[80:83], v[148:151]\n"
"v_mul_f32 v82, s76, v82\n"
"v_mul_f32 v83, s76, v83\n"
"v_mfma_f32_16x16x32_bf16 v[152:155], a[24:27], a[80:83], v[152:155]\n"
"v_mul_f32 v84, s76, v84\n"
"v_mul_f32 v85, s76, v85\n"
"v_mfma_f32_16x16x32_bf16 v[156:159], a[28:31], a[80:83], v[156:159]\n"
"v_mul_f32 v86, s76, v86\n"
"v_mul_f32 v87, s76, v87\n"
"v_mfma_f32_16x16x32_bf16 v[128:131], a[160:163], a[104:107], v[128:131]\n"
"v_permlane16_swap_b32 v36, v38\n"
"v_permlane16_swap_b32 v37, v39\n"
"v_mfma_f32_16x16x32_bf16 v[132:135], a[164:167], a[104:107], v[132:135]\n"
"v_cvt_pk_bf16_f32 v40, v80, v81\n"
"v_cvt_pk_bf16_f32 v41, v82, v83\n"
"v_mfma_f32_16x16x32_bf16 v[136:139], a[168:171], a[104:107], v[136:139]\n"
"v_cvt_pk_bf16_f32 v42, v84, v85\n"
"v_cvt_pk_bf16_f32 v43, v86, v87\n"
"v_mfma_f32_16x16x32_bf16 v[140:143], a[172:175], a[104:107], v[140:143]\n"
"v_mul_f32 v88, s76, v88\n"
"v_mul_f32 v89, s76, v89\n"
"v_mfma_f32_16x16x32_bf16 v[144:147], a[176:179], a[104:107], v[144:147]\n"
"v_mul_f32 v90, s76, v90\n"
"v_mul_f32 v91, s76, v91\n"
"v_mfma_f32_16x16x32_bf16 v[148:151], a[180:183], a[104:107], v[148:151]\n"
"v_mul_f32 v92, s76, v92\n"
"v_mul_f32 v93, s76, v93\n"
"v_mfma_f32_16x16x32_bf16 v[152:155], a[184:187], a[104:107], v[152:155]\n"
"v_mul_f32 v94, s76, v94\n"
"v_mul_f32 v95, s76, v95\n"
"v_mfma_f32_16x16x32_bf16 v[156:159], a[188:191], a[104:107], v[156:159]\n"
"s_add_u32 m0, s59, 0x10000\n"
"v_permlane16_swap_b32 v40, v42\n"
"v_mfma_f32_16x16x32_bf16 v[128:131], a[224:227], a[128:131], v[128:131]\n"
"buffer_load_dwordx4 v247, s[28:31], s56 offen offset:1024 nt lds\n"
"v_permlane16_swap_b32 v41, v43\n"
"v_mfma_f32_16x16x32_bf16 v[132:135], a[228:231], a[128:131], v[132:135]\n"
"v_cvt_pk_bf16_f32 v44, v88, v89\n"
"v_cvt_pk_bf16_f32 v45, v90, v91\n"
"v_mfma_f32_16x16x32_bf16 v[136:139], a[232:235], a[128:131], v[136:139]\n"
"v_cvt_pk_bf16_f32 v46, v92, v93\n"
"v_cvt_pk_bf16_f32 v47, v94, v95\n"
"v_mfma_f32_16x16x32_bf16 v[140:143], a[236:239], a[128:131], v[140:143]\n"
"s_nop 1\n"
"v_permlane16_swap_b32 v44, v46\n"
"v_mfma_f32_16x16x32_bf16 v[144:147], a[240:243], a[128:131], v[144:147]\n"
"v_permlane16_swap_b32 v45, v47\n"
"s_waitcnt lgkmcnt(0)\n"
"v_mfma_f32_16x16x32_bf16 v[148:151], a[244:247], a[128:131], v[148:151]\n"
"v_mul_f32 v96, s76, v96\n"
"v_mul_f32 v97, s76, v97\n"
"v_mfma_f32_16x16x32_bf16 v[152:155], a[248:251], a[128:131], v[152:155]\n"
"v_mul_f32 v98, s76, v98\n"
"v_mul_f32 v99, s76, v99\n"
"v_mfma_f32_16x16x32_bf16 v[156:159], a[252:255], a[128:131], v[156:159]\n"
"v_mul_f32 v100, s76, v100\n"
"v_mul_f32 v101, s76, v101\n"
"v_mfma_f32_16x16x32_bf16 v[128:131], a[32:35], a[152:155], v[128:131]\n"
"v_mul_f32 v102, s76, v102\n"
"v_mul_f32 v103, s76, v103\n"
"v_mfma_f32_16x16x32_bf16 v[132:135], a[36:39], a[152:155], v[132:135]\n"
"v_cvt_pk_bf16_f32 v48, v96, v97\n"
"v_cvt_pk_bf16_f32 v49, v98, v99\n"
"v_mfma_f32_16x16x32_bf16 v[136:139], a[40:43], a[152:155], v[136:139]\n"
"v_cvt_pk_bf16_f32 v50, v100, v101\n"
"v_cvt_pk_bf16_f32 v51, v102, v103\n"
"v_mfma_f32_16x16x32_bf16 v[140:143], a[44:47], a[152:155], v[140:143]\n"
"v_mul_f32 v104, s76, v104\n"
"v_mul_f32 v105, s76, v105\n"
"v_mfma_f32_16x16x32_bf16 v[144:147], a[48:51], a[152:155], v[144:147]\n"
"v_mul_f32 v106, s76, v106\n"
"v_mul_f32 v107, s76, v107\n"
"v_mfma_f32_16x16x32_bf16 v[148:151], a[52:55], a[152:155], v[148:151]\n"
"s_add_u32 m0, s59, 0x10000\n"
"v_mul_f32 v108, s76, v108\n"
"v_mfma_f32_16x16x32_bf16 v[152:155], a[56:59], a[152:155], v[152:155]\n"
"buffer_load_dwordx4 v248, s[28:31], s56 offen offset:2048 nt lds\n"
"v_mul_f32 v109, s76, v109\n"
"v_mfma_f32_16x16x32_bf16 v[156:159], a[60:63], a[152:155], v[156:159]\n"
"v_mul_f32 v110, s76, v110\n"
"v_mul_f32 v111, s76, v111\n"
"v_mfma_f32_16x16x32_bf16 v[160:163], a[0:3], a[84:87], v[160:163]\n"
"v_permlane16_swap_b32 v48, v50\n"
"v_permlane16_swap_b32 v49, v51\n"
"v_mfma_f32_16x16x32_bf16 v[164:167], a[4:7], a[84:87], v[164:167]\n"
"v_cvt_pk_bf16_f32 v52, v104, v105\n"
"v_cvt_pk_bf16_f32 v53, v106, v107\n"
"v_mfma_f32_16x16x32_bf16 v[168:171], a[8:11], a[84:87], v[168:171]\n"
"v_cvt_pk_bf16_f32 v54, v108, v109\n"
"v_cvt_pk_bf16_f32 v55, v110, v111\n"
"v_mfma_f32_16x16x32_bf16 v[172:175], a[12:15], a[84:87], v[172:175]\n"
"v_mul_f32 v112, s76, v112\n"
"v_mul_f32 v113, s76, v113\n"
"v_mfma_f32_16x16x32_bf16 v[176:179], a[16:19], a[84:87], v[176:179]\n"
"v_mul_f32 v114, s76, v114\n"
"v_mul_f32 v115, s76, v115\n"
"v_mfma_f32_16x16x32_bf16 v[180:183], a[20:23], a[84:87], v[180:183]\n"
"v_mul_f32 v116, s76, v116\n"
"v_mul_f32 v117, s76, v117\n"
"v_mfma_f32_16x16x32_bf16 v[184:187], a[24:27], a[84:87], v[184:187]\n"
"v_mul_f32 v118, s76, v118\n"
"v_mul_f32 v119, s76, v119\n"
"v_mfma_f32_16x16x32_bf16 v[188:191], a[28:31], a[84:87], v[188:191]\n"
"v_permlane16_swap_b32 v52, v54\n"
"v_permlane16_swap_b32 v53, v55\n"
"v_mfma_f32_16x16x32_bf16 v[160:163], a[160:163], a[108:111], v[160:163]\n"
"v_cvt_pk_bf16_f32 v56, v112, v113\n"
"v_cvt_pk_bf16_f32 v57, v114, v115\n"
"v_mfma_f32_16x16x32_bf16 v[164:167], a[164:167], a[108:111], v[164:167]\n"
"v_cvt_pk_bf16_f32 v58, v116, v117\n"
"v_cvt_pk_bf16_f32 v59, v118, v119\n"
"v_mfma_f32_16x16x32_bf16 v[168:171], a[168:171], a[108:111], v[168:171]\n"
"v_mul_f32 v120, s76, v120\n"
"s_add_u32 m0, s59, 0x10000\n"
"v_mfma_f32_16x16x32_bf16 v[172:175], a[172:175], a[108:111], v[172:175]\n"
"v_mul_f32 v121, s76, v121\n"
"v_mfma_f32_16x16x32_bf16 v[176:179], a[176:179], a[108:111], v[176:179]\n"
"buffer_load_dwordx4 v249, s[28:31], s56 offen offset:3072 nt lds\n"
"v_mul_f32 v122, s76, v122\n"
"v_mfma_f32_16x16x32_bf16 v[180:183], a[180:183], a[108:111], v[180:183]\n"
"v_mul_f32 v123, s76, v123\n"
"v_mul_f32 v124, s76, v124\n"
"v_mfma_f32_16x16x32_bf16 v[184:187], a[184:187], a[108:111], v[184:187]\n"
"v_mul_f32 v125, s76, v125\n"
"v_mul_f32 v126, s76, v126\n"
"v_mfma_f32_16x16x32_bf16 v[188:191], a[188:191], a[108:111], v[188:191]\n"
"v_mul_f32 v127, s76, v127\n"
"v_permlane16_swap_b32 v56, v58\n"
"v_mfma_f32_16x16x32_bf16 v[160:163], a[224:227], a[132:135], v[160:163]\n"
"v_permlane16_swap_b32 v57, v59\n"
"v_cvt_pk_bf16_f32 v60, v120, v121\n"
"v_mfma_f32_16x16x32_bf16 v[164:167], a[228:231], a[132:135], v[164:167]\n"
"v_cvt_pk_bf16_f32 v61, v122, v123\n"
"v_cvt_pk_bf16_f32 v62, v124, v125\n"
"v_mfma_f32_16x16x32_bf16 v[168:171], a[232:235], a[132:135], v[168:171]\n"
"v_cvt_pk_bf16_f32 v63, v126, v127\n"
"s_nop 1\n"
"v_mfma_f32_16x16x32_bf16 v[172:175], a[236:239], a[132:135], v[172:175]\n"
"v_permlane16_swap_b32 v60, v62\n"
"v_permlane16_swap_b32 v61, v63\n"
"v_mfma_f32_16x16x32_bf16 v[176:179], a[240:243], a[132:135], v[176:179]\n"
"v_mul_f32 v128, s76, v128\n"
"v_mul_f32 v129, s76, v129\n"
"v_mfma_f32_16x16x32_bf16 v[180:183], a[244:247], a[132:135], v[180:183]\n"
"v_mul_f32 v130, s76, v130\n"
"v_mul_f32 v131, s76, v131\n"
"v_mfma_f32_16x16x32_bf16 v[184:187], a[248:251], a[132:135], v[184:187]\n"
"v_mul_f32 v132, s76, v132\n"
"v_mul_f32 v133, s76, v133\n"
"v_mfma_f32_16x16x32_bf16 v[188:191], a[252:255], a[132:135], v[188:191]\n"
"v_mul_f32 v134, s76, v134\n"
"v_mul_f32 v135, s76, v135\n"
"v_mfma_f32_16x16x32_bf16 v[160:163], a[32:35], a[156:159], v[160:163]\n"
"v_cvt_pk_bf16_f32 v64, v128, v129\n"
"s_add_u32 m0, s59, 0x11000\n"
"v_mfma_f32_16x16x32_bf16 v[164:167], a[36:39], a[156:159], v[164:167]\n"
"s_add_u32 s33, s56, 4096\n"
"s_add_u32 s56, s56, 16384\n"
"s_and_b32 s56, s56, 0x3ffff\n"
"v_mfma_f32_16x16x32_bf16 v[168:171], a[40:43], a[156:159], v[168:171]\n"
"v_cvt_pk_bf16_f32 v65, v130, v131\n"
"v_mfma_f32_16x16x32_bf16 v[172:175], a[44:47], a[156:159], v[172:175]\n"
"buffer_load_dwordx4 v246, s[28:31], s33 offen offset:0 nt lds\n"
"v_cvt_pk_bf16_f32 v66, v132, v133\n"
"v_mfma_f32_16x16x32_bf16 v[176:179], a[48:51], a[156:159], v[176:179]\n"
"v_cvt_pk_bf16_f32 v67, v134, v135\n"
"v_mul_f32 v136, s76, v136\n"
"v_mfma_f32_16x16x32_bf16 v[180:183], a[52:55], a[156:159], v[180:183]\n"
"v_mul_f32 v137, s76, v137\n"
"v_mul_f32 v138, s76, v138\n"
"v_mfma_f32_16x16x32_bf16 v[184:187], a[56:59], a[156:159], v[184:187]\n"
"v_mul_f32 v139, s76, v139\n"
"v_mul_f32 v140, s76, v140\n"
"v_mfma_f32_16x16x32_bf16 v[188:191], a[60:63], a[156:159], v[188:191]\n"
"v_mul_f32 v141, s76, v141\n"
"v_mul_f32 v142, s76, v142\n"
"v_mfma_f32_16x16x32_bf16 v[192:195], a[192:195], v[0:3], 0\n"
"v_mul_f32 v143, s76, v143\n"
"v_permlane16_swap_b32 v64, v66\n"
"v_mfma_f32_16x16x32_bf16 v[192:195], a[196:199], v[4:7], v[192:195]\n"
"v_permlane16_swap_b32 v65, v67\n"
"v_cvt_pk_bf16_f32 v68, v136, v137\n"
"v_mfma_f32_16x16x32_bf16 v[192:195], a[200:203], v[8:11], v[192:195]\n"
"v_cvt_pk_bf16_f32 v69, v138, v139\n"
"v_cvt_pk_bf16_f32 v70, v140, v141\n"
"v_mfma_f32_16x16x32_bf16 v[192:195], a[204:207], v[12:15], v[192:195]\n"
"v_cvt_pk_bf16_f32 v71, v142, v143\n"
"v_mul_f32 v144, s76, v144\n"
"v_mfma_f32_16x16x32_bf16 v[196:199], a[208:211], v[0:3], 0\n"
"v_mul_f32 v145, s76, v145\n"
"v_mul_f32 v146, s76, v146\n"
"v_mfma_f32_16x16x32_bf16 v[196:199], a[212:215], v[4:7], v[196:199]\n"
"v_mul_f32 v147, s76, v147\n"
"v_mul_f32 v148, s76, v148\n"
"v_mfma_f32_16x16x32_bf16 v[196:199], a[216:219], v[8:11], v[196:199]\n"
"v_mul_f32 v149, s76, v149\n"
"v_mul_f32 v150, s76, v150\n"
"v_mfma_f32_16x16x32_bf16 v[196:199], a[220:223], v[12:15], v[196:199]\n"
"v_mul_f32 v151, s76, v151\n"
"s_add_u32 m0, s59, 0x11000\n"
"v_mfma_f32_16x16x32_bf16 v[200:203], a[192:195], v[16:19], 0\n"
"v_permlane16_swap_b32 v68, v70\n"
"v_mfma_f32_16x16x32_bf16 v[200:203], a[196:199], v[20:23], v[200:203]\n"
"buffer_load_dwordx4 v247, s[28:31], s33 offen offset:1024 nt lds\n"
"v_permlane16_swap_b32 v69, v71\n"
"v_mfma_f32_16x16x32_bf16 v[200:203], a[200:203], v[24:27], v[200:203]\n"
"v_cvt_pk_bf16_f32 v72, v144, v145\n"
"v_cvt_pk_bf16_f32 v73, v146, v147\n"
"v_mfma_f32_16x16x32_bf16 v[200:203], a[204:207], v[28:31], v[200:203]\n"
"v_cvt_pk_bf16_f32 v74, v148, v149\n"
"v_cvt_pk_bf16_f32 v75, v150, v151\n"
"v_mfma_f32_16x16x32_bf16 v[204:207], a[208:211], v[16:19], 0\n"
"v_mul_f32 v152, s76, v152\n"
"v_mul_f32 v153, s76, v153\n"
"v_mfma_f32_16x16x32_bf16 v[204:207], a[212:215], v[20:23], v[204:207]\n"
"v_mul_f32 v154, s76, v154\n"
"v_mul_f32 v155, s76, v155\n"
"v_mfma_f32_16x16x32_bf16 v[204:207], a[216:219], v[24:27], v[204:207]\n"
"v_mul_f32 v156, s76, v156\n"
"v_mul_f32 v157, s76, v157\n"
"v_mfma_f32_16x16x32_bf16 v[204:207], a[220:223], v[28:31], v[204:207]\n"
"v_mul_f32 v158, s76, v158\n"
"v_mul_f32 v159, s76, v159\n"
"v_mfma_f32_16x16x32_bf16 v[208:211], a[192:195], v[32:35], 0\n"
"v_permlane16_swap_b32 v72, v74\n"
"v_permlane16_swap_b32 v73, v75\n"
"v_mfma_f32_16x16x32_bf16 v[208:211], a[196:199], v[36:39], v[208:211]\n"
"v_cvt_pk_bf16_f32 v76, v152, v153\n"
"v_cvt_pk_bf16_f32 v77, v154, v155\n"
"v_mfma_f32_16x16x32_bf16 v[208:211], a[200:203], v[40:43], v[208:211]\n"
"v_cvt_pk_bf16_f32 v78, v156, v157\n"
"v_cvt_pk_bf16_f32 v79, v158, v159\n"
"v_mfma_f32_16x16x32_bf16 v[208:211], a[204:207], v[44:47], v[208:211]\n"
"s_nop 1\n"
"v_permlane16_swap_b32 v76, v78\n"
"v_mfma_f32_16x16x32_bf16 v[212:215], a[208:211], v[32:35], 0\n"
"v_permlane16_swap_b32 v77, v79\n"
"v_mul_f32 v160, s76, v160\n"
"v_mfma_f32_16x16x32_bf16 v[212:215], a[212:215], v[36:39], v[212:215]\n"
"v_mul_f32 v161, s76, v161\n"
"s_add_u32 m0, s59, 0x11000\n"
"v_mfma_f32_16x16x32_bf16 v[212:215], a[216:219], v[40:43], v[212:215]\n"
"v_mul_f32 v162, s76, v162\n"
"v_mfma_f32_16x16x32_bf16 v[212:215], a[220:223], v[44:47], v[212:215]\n"
"buffer_load_dwordx4 v248, s[28:31], s33 offen offset:2048 nt lds\n"
"v_mul_f32 v163, s76, v163\n"
"v_mfma_f32_16x16x32_bf16 v[216:219], a[192:195], v[48:51], 0\n"
"v_mul_f32 v164, s76, v164\n"
"v_mul_f32 v165, s76, v165\n"
"v_mfma_f32_16x16x32_bf16 v[216:219], a[196:199], v[52:55], v[216:219]\n"
"v_mul_f32 v166, s76, v166\n"
"v_mul_f32 v167, s76, v167\n"
"v_mfma_f32_16x16x32_bf16 v[216:219], a[200:203], v[56:59], v[216:219]\n"
"v_cvt_pk_bf16_f32 v80, v160, v161\n"
"v_cvt_pk_bf16_f32 v81, v162, v163\n"
"v_mfma_f32_16x16x32_bf16 v[216:219], a[204:207], v[60:63], v[216:219]\n"
"v_cvt_pk_bf16_f32 v82, v164, v165\n"
"v_cvt_pk_bf16_f32 v83, v166, v167\n"
"v_mfma_f32_16x16x32_bf16 v[220:223], a[208:211], v[48:51], 0\n"
"v_mul_f32 v168, s76, v168\n"
"v_mul_f32 v169, s76, v169\n"
"v_mfma_f32_16x16x32_bf16 v[220:223], a[212:215], v[52:55], v[220:223]\n"
"v_mul_f32 v170, s76, v170\n"
"v_mul_f32 v171, s76, v171\n"
"v_mfma_f32_16x16x32_bf16 v[220:223], a[216:219], v[56:59], v[220:223]\n"
"v_mul_f32 v172, s76, v172\n"
"v_mul_f32 v173, s76, v173\n"
"v_mfma_f32_16x16x32_bf16 v[220:223], a[220:223], v[60:63], v[220:223]\n"
"v_mul_f32 v174, s76, v174\n"
"v_mul_f32 v175, s76, v175\n"
"v_mfma_f32_16x16x32_bf16 v[224:227], a[192:195], v[64:67], 0\n"
"v_permlane16_swap_b32 v80, v82\n"
"v_permlane16_swap_b32 v81, v83\n"
"v_mfma_f32_16x16x32_bf16 v[224:227], a[196:199], v[68:71], v[224:227]\n"
"v_cvt_pk_bf16_f32 v84, v168, v169\n"
"v_cvt_pk_bf16_f32 v85, v170, v171\n"
"v_mfma_f32_16x16x32_bf16 v[224:227], a[200:203], v[72:75], v[224:227]\n"
"v_cvt_pk_bf16_f32 v86, v172, v173\n"
"v_cvt_pk_bf16_f32 v87, v174, v175\n"
"v_mfma_f32_16x16x32_bf16 v[224:227], a[204:207], v[76:79], v[224:227]\n"
"v_mul_f32 v176, s76, v176\n"
"s_add_u32 m0, s59, 0x11000\n"
"v_mfma_f32_16x16x32_bf16 v[228:231], a[208:211], v[64:67], 0\n"
"v_mul_f32 v177, s76, v177\n"
"v_mfma_f32_16x16x32_bf16 v[228:231], a[212:215], v[68:71], v[228:231]\n"
"buffer_load_dwordx4 v249, s[28:31], s33 offen offset:3072 nt lds\n"
"v_mul_f32 v178, s76, v178\n"
"v_mfma_f32_16x16x32_bf16 v[228:231], a[216:219], v[72:75], v[228:231]\n"
"v_mul_f32 v179, s76, v179\n"
"v_mul_f32 v180, s76, v180\n"
"v_mfma_f32_16x16x32_bf16 v[228:231], a[220:223], v[76:79], v[228:231]\n"
"v_mul_f32 v181, s76, v181\n"
"v_mul_f32 v182, s76, v182\n"
"v_mfma_f32_16x16x32_bf16 v[232:235], a[192:195], v[80:83], 0\n"
"v_mul_f32 v183, s76, v183\n"
"v_permlane16_swap_b32 v84, v86\n"
"v_permlane16_swap_b32 v85, v87\n"
"v_cvt_pk_bf16_f32 v88, v176, v177\n"
"v_cvt_pk_bf16_f32 v89, v178, v179\n"
"v_mfma_f32_16x16x32_bf16 v[232:235], a[196:199], v[84:87], v[232:235]\n"
"v_cvt_pk_bf16_f32 v90, v180, v181\n"
"v_cvt_pk_bf16_f32 v91, v182, v183\n"
"v_mul_f32 v184, s76, v184\n"
"v_mul_f32 v185, s76, v185\n"
"v_mul_f32 v186, s76, v186\n"
"v_mul_f32 v187, s76, v187\n"
"v_mul_f32 v188, s76, v188\n"
"v_mul_f32 v189, s76, v189\n"
"v_mul_f32 v190, s76, v190\n"
"v_mul_f32 v191, s76, v191\n"
"v_permlane16_swap_b32 v88, v90\n"
"v_permlane16_swap_b32 v89, v91\n"
"v_cvt_pk_bf16_f32 v92, v184, v185\n"
"v_cvt_pk_bf16_f32 v93, v186, v187\n"
"v_mfma_f32_16x16x32_bf16 v[232:235], a[200:203], v[88:91], v[232:235]\n"
"s_add_u32 m0, s59, 0x18000\n"
"v_cvt_pk_bf16_f32 v94, v188, v189\n"
"buffer_load_dwordx4 v246, s[36:39], s57 offen offset:0 nt lds\n"
"v_cvt_pk_bf16_f32 v95, v190, v191\n"
"s_nop 1\n"
"v_permlane16_swap_b32 v92, v94\n"
"v_permlane16_swap_b32 v93, v95\n"
"v_max3_f32 v176, v192, v193, v194\n"
"v_max3_f32 v178, v208, v209, v210\n"
"v_mfma_f32_16x16x32_bf16 v[232:235], a[204:207], v[92:95], v[232:235]\n"
"v_max3_f32 v177, v200, v201, v202\n"
"v_max3_f32 v179, v216, v217, v218\n"
"v_mfma_f32_16x16x32_bf16 v[236:239], a[208:211], v[80:83], 0\n"
"v_max3_f32 v176, v176, v195, v196\n"
"v_max3_f32 v178, v178, v211, v212\n"
"v_mfma_f32_16x16x32_bf16 v[236:239], a[212:215], v[84:87], v[236:239]\n"
"v_max3_f32 v177, v177, v203, v204\n"
"v_max3_f32 v179, v179, v219, v220\n"
"v_mfma_f32_16x16x32_bf16 v[236:239], a[216:219], v[88:91], v[236:239]\n"
"v_max3_f32 v176, v176, v197, v198\n"
"v_max3_f32 v178, v178, v213, v214\n"
"v_mfma_f32_16x16x32_bf16 v[236:239], a[220:223], v[92:95], v[236:239]\n"
"v_max3_f32 v177, v177, v205, v206\n"
"v_max3_f32 v179, v179, v221, v222\n"
"v_max_f32 v176, v176, v199\n"
"v_max_f32 v178, v178, v215\n"
"v_max_f32 v177, v177, v207\n"
"v_max_f32 v179, v179, v223\n"
"s_nop 0\n"
"v_permlane16_swap_b32 v176, v177\n"
"v_permlane16_swap_b32 v178, v179\n"
"v_max_f32 v176, v176, v177\n"
"v_max_f32 v178, v178, v179\n"
"s_add_u32 m0, s59, 0x18000\n"
"v_mov_b32 v177, v176\n"
"buffer_load_dwordx4 v247, s[36:39], s57 offen offset:1024 nt lds\n"
"v_mov_b32 v179, v178\n"
"s_nop 0\n"
"v_permlane32_swap_b32 v176, v177\n"
"v_permlane32_swap_b32 v178, v179\n"
"v_max_f32 v176, v176, v177\n"
"v_max_f32 v178, v178, v179\n"
"v_mov_b32 v177, v176\n"
"v_mov_b32 v179, v178\n"
"s_nop 0\n"
"v_permlane16_swap_b32 v176, v177\n"
"v_permlane16_swap_b32 v178, v179\n"
"v_sub_f32 v96, 0xc1a00000, v176\n"
"v_sub_f32 v104, 0xc1a00000, v178\n"
"v_sub_f32 v97, 0xc1a00000, v176\n"
"v_sub_f32 v105, 0xc1a00000, v178\n"
"v_sub_f32 v98, 0xc1a00000, v176\n"
"v_sub_f32 v106, 0xc1a00000, v178\n"
"v_sub_f32 v99, 0xc1a00000, v176\n"
"v_sub_f32 v107, 0xc1a00000, v178\n"
"v_sub_f32 v100, 0xc1a00000, v177\n"
"v_sub_f32 v108, 0xc1a00000, v179\n"
"v_sub_f32 v101, 0xc1a00000, v177\n"
"v_sub_f32 v109, 0xc1a00000, v179\n"
"v_sub_f32 v102, 0xc1a00000, v177\n"
"s_add_u32 m0, s59, 0x18000\n"
"v_sub_f32 v110, 0xc1a00000, v179\n"
"buffer_load_dwordx4 v248, s[36:39], s57 offen offset:2048 nt lds\n"
"v_sub_f32 v103, 0xc1a00000, v177\n"
"v_sub_f32 v111, 0xc1a00000, v179\n"
"v_pk_add_f32 v[192:193], v[192:193], v[96:97]\n"
"v_pk_add_f32 v[208:209], v[208:209], v[104:105]\n"
"v_pk_add_f32 v[194:195], v[194:195], v[96:97]\n"
"v_pk_add_f32 v[210:211], v[210:211], v[104:105]\n"
"v_pk_add_f32 v[196:197], v[196:197], v[96:97]\n"
"v_pk_add_f32 v[212:213], v[212:213], v[104:105]\n"
"v_pk_add_f32 v[198:199], v[198:199], v[96:97]\n"
"v_pk_add_f32 v[214:215], v[214:215], v[104:105]\n"
"v_pk_add_f32 v[200:201], v[200:201], v[100:101]\n"
"v_pk_add_f32 v[216:217], v[216:217], v[108:109]\n"
"v_pk_add_f32 v[202:203], v[202:203], v[100:101]\n"
"v_pk_add_f32 v[218:219], v[218:219], v[108:109]\n"
"v_pk_add_f32 v[204:205], v[204:205], v[100:101]\n"
"v_pk_add_f32 v[220:221], v[220:221], v[108:109]\n"
"v_pk_add_f32 v[206:207], v[206:207], v[100:101]\n"
"v_pk_add_f32 v[222:223], v[222:223], v[108:109]\n"
"v_mov_b32 v168, v251\n"
"v_mov_b32 v169, v251\n"
"v_mov_b32 v170, v251\n"
"v_mov_b32 v171, v251\n"
"s_waitcnt vmcnt(11)\n"
"s_barrier\n"
"ds_read_b64_tr_b16 a[224:225], v241 offset:0\n"
"s_add_u32 m0, s59, 0x18000\n"
"ds_read_b64_tr_b16 a[226:227], v241 offset:256\n"
"buffer_load_dwordx4 v249, s[36:39], s57 offen offset:3072 nt lds\n"
"ds_read_b64_tr_b16 a[228:229], v242 offset:0\n"
"ds_read_b64_tr_b16 a[230:231], v242 offset:256\n"
"ds_read_b64_tr_b16 a[232:233], v241 offset:512\n"
"ds_read_b64_tr_b16 a[234:235], v241 offset:768\n"
"ds_read_b64_tr_b16 a[236:237], v242 offset:512\n"
"ds_read_b64_tr_b16 a[238:239], v242 offset:768\n"
"ds_read_b64_tr_b16 a[240:241], v241 offset:1024\n"
"ds_read_b64_tr_b16 a[242:243], v241 offset:1280\n"
"ds_read_b64_tr_b16 a[244:245], v242 offset:1024\n"
"ds_read_b64_tr_b16 a[246:247], v242 offset:1280\n"
"ds_read_b64_tr_b16 a[248:249], v241 offset:1536\n"
"ds_read_b64_tr_b16 a[250:251], v241 offset:1792\n"
"ds_read_b64_tr_b16 a[252:253], v242 offset:1536\n"
"ds_read_b64_tr_b16 a[254:255], v242 offset:1792\n"
"ds_read_b128 a[192:195], v240 offset:8192\n"
"ds_read_b128 a[196:199], v240 offset:8704\n"
"ds_read_b128 a[200:203], v240 offset:9216\n"
"ds_read_b128 a[204:207], v240 offset:9728\n"
"ds_read_b128 a[208:211], v240 offset:8448\n"
"ds_read_b128 a[212:215], v240 offset:8960\n"
"ds_read_b128 a[216:219], v240 offset:9472\n"
"ds_read_b128 a[220:223], v240 offset:9984\n"
"v_exp_f32 v172, v192\n"
"v_exp_f32 v173, v193\n"
"s_add_u32 m0, s59, 0x19000\n"
"s_add_u32 s33, s57, 4096\n"
"s_add_u32 s57, s57, 16384\n"
"s_and_b32 s57, s57, 0x3ffff\n"
"v_exp_f32 v174, v194\n"
"buffer_load_dwordx4 v246, s[36:39], s33 offen offset:0 nt lds\n"
"v_exp_f32 v175, v195\n"
"v_cvt_pk_bf16_f32 v120, v172, v173\n"
"v_exp_f32 v172, v196\n"
"v_exp_f32 v173, v197\n"
"v_cvt_pk_bf16_f32 v121, v174, v175\n"
"v_exp_f32 v174, v198\n"
"v_exp_f32 v175, v199\n"
"v_cvt_pk_bf16_f32 v122, v172, v173\n"
"v_exp_f32 v172, v200\n"
"v_exp_f32 v173, v201\n"
"v_cvt_pk_bf16_f32 v123, v174, v175\n"
"v_exp_f32 v174, v202\n"
"v_exp_f32 v175, v203\n"
"v_cvt_pk_bf16_f32 v124, v172, v173\n"
"v_exp_f32 v172, v204\n"
"v_exp_f32 v173, v205\n"
"v_cvt_pk_bf16_f32 v125, v174, v175\n"
"v_exp_f32 v174, v206\n"
"v_exp_f32 v175, v207\n"
"v_cvt_pk_bf16_f32 v126, v172, v173\n"
"v_cvt_pk_bf16_f32 v127, v174, v175\n"
"s_waitcnt lgkmcnt(8)\n"
"v_mfma_f32_16x16x32_bf16 a[0:3], a[224:227], v[120:123], 0\n"
"v_max3_f32 v176, v224, v225, v226\n"
"v_exp_f32 v172, v208\n"
"v_mfma_f32_16x16x32_bf16 a[32:35], a[224:227], v[124:127], 0\n"
"v_max3_f32 v177, v232, v233, v234\n"
"s_add_u32 m0, s59, 0x19000\n"
"v_mfma_f32_16x16x32_bf16 a[4:7], a[228:231], v[120:123], 0\n"
"v_exp_f32 v173, v209\n"
"v_mfma_f32_16x16x32_bf16 a[36:39], a[228:231], v[124:127], 0\n"
"buffer_load_dwordx4 v247, s[36:39], s33 offen offset:1024 nt lds\n"
"v_max3_f32 v176, v176, v227, v228\n"
"v_mfma_f32_16x16x32_bf16 a[8:11], a[232:235], v[120:123], 0\n"
"v_exp_f32 v174, v210\n"
"v_mfma_f32_16x16x32_bf16 a[40:43], a[232:235], v[124:127], 0\n"
"v_max3_f32 v177, v177, v235, v236\n"
"v_exp_f32 v175, v211\n"
"v_mfma_f32_16x16x32_bf16 a[12:15], a[236:239], v[120:123], 0\n"
"v_max3_f32 v176, v176, v229, v230\n"
"v_cvt_pk_bf16_f32 v128, v172, v173\n"
"v_mfma_f32_16x16x32_bf16 a[44:47], a[236:239], v[124:127], 0\n"
"v_max3_f32 v177, v177, v237, v238\n"
"v_exp_f32 v172, v212\n"
"v_mfma_f32_16x16x32_bf16 a[16:19], a[240:243], v[120:123], 0\n"
"v_max_f32 v176, v176, v231\n"
"v_exp_f32 v173, v213\n"
"v_mfma_f32_16x16x32_bf16 a[48:51], a[240:243], v[124:127], 0\n"
"v_max_f32 v177, v177, v239\n"
"v_cvt_pk_bf16_f32 v129, v174, v175\n"
"v_mfma_f32_16x16x32_bf16 a[20:23], a[244:247], v[120:123], 0\n"
"v_exp_f32 v174, v214\n"
"v_mfma_f32_16x16x32_bf16 a[52:55], a[244:247], v[124:127], 0\n"
"v_exp_f32 v175, v215\n"
"v_mfma_f32_16x16x32_bf16 a[24:27], a[248:251], v[120:123], 0\n"
"v_permlane16_swap_b32 v176, v177\n"
"v_cvt_pk_bf16_f32 v130, v172, v173\n"
"v_mfma_f32_16x16x32_bf16 a[56:59], a[248:251], v[124:127], 0\n"
"v_max_f32 v176, v176, v177\n"
"v_exp_f32 v172, v216\n"
"v_mfma_f32_16x16x32_bf16 a[28:31], a[252:255], v[120:123], 0\n"
"v_mov_b32 v177, v176\n"
"v_exp_f32 v173, v217\n"
"v_mfma_f32_16x16x32_bf16 a[60:63], a[252:255], v[124:127], 0\n"
"v_cvt_pk_bf16_f32 v131, v174, v175\n"
"v_exp_f32 v174, v218\n"
"v_mfma_f32_16x16x32_bf16 v[144:147], v[168:171], v[120:123], 0\n"
"v_permlane32_swap_b32 v176, v177\n"
"v_exp_f32 v175, v219\n"
"v_mfma_f32_16x16x32_bf16 v[148:151], v[168:171], v[124:127], 0\n"
"s_add_u32 m0, s59, 0x19000\n"
"v_max_f32 v176, v176, v177\n"
"buffer_load_dwordx4 v248, s[36:39], s33 offen offset:2048 nt lds\n"
"v_cvt_pk_bf16_f32 v132, v172, v173\n"
"v_mov_b32 v177, v176\n"
"v_exp_f32 v172, v220\n"
"v_exp_f32 v173, v221\n"
"v_cvt_pk_bf16_f32 v133, v174, v175\n"
"v_permlane16_swap_b32 v176, v177\n"
"v_exp_f32 v174, v222\n"
"v_sub_f32 v112, 0xc1a00000, v176\n"
"v_exp_f32 v175, v223\n"
"v_cvt_pk_bf16_f32 v134, v172, v173\n"
"v_cvt_pk_bf16_f32 v135, v174, v175\n"
"v_sub_f32 v113, 0xc1a00000, v176\n"
"v_sub_f32 v114, 0xc1a00000, v176\n"
"v_sub_f32 v115, 0xc1a00000, v176\n"
"v_sub_f32 v116, 0xc1a00000, v177\n"
"v_sub_f32 v117, 0xc1a00000, v177\n"
"v_sub_f32 v118, 0xc1a00000, v177\n"
"v_sub_f32 v119, 0xc1a00000, v177\n"
"v_pk_add_f32 v[224:225], v[224:225], v[112:113]\n"
"v_pk_add_f32 v[226:227], v[226:227], v[112:113]\n"
"v_pk_add_f32 v[228:229], v[228:229], v[112:113]\n"
"v_pk_add_f32 v[230:231], v[230:231], v[112:113]\n"
"v_pk_add_f32 v[232:233], v[232:233], v[116:117]\n"
"v_pk_add_f32 v[234:235], v[234:235], v[116:117]\n"
"s_add_u32 m0, s59, 0x19000\n"
"v_pk_add_f32 v[236:237], v[236:237], v[116:117]\n"
"buffer_load_dwordx4 v249, s[36:39], s33 offen offset:3072 nt lds\n"
"v_pk_add_f32 v[238:239], v[238:239], v[116:117]\n"
"v_exp_f32 v172, v224\n"
"v_exp_f32 v173, v225\n"
"v_exp_f32 v174, v226\n"
"v_exp_f32 v175, v227\n"
"v_cvt_pk_bf16_f32 v136, v172, v173\n"
"v_exp_f32 v172, v228\n"
"v_exp_f32 v173, v229\n"
"v_cvt_pk_bf16_f32 v137, v174, v175\n"
"v_exp_f32 v174, v230\n"
"v_cvt_pk_bf16_f32 v138, v172, v173\n"
"v_or3_b32 v184, v136, v137, v138\n"
"v_exp_f32 v175, v231\n"
"v_exp_f32 v172, v232\n"
"v_exp_f32 v173, v233\n"
"v_cvt_pk_bf16_f32 v139, v174, v175\n"
"v_exp_f32 v174, v234\n"
"v_cvt_pk_bf16_f32 v140, v172, v173\n"
"v_or3_b32 v184, v184, v139, v140\n"
"v_exp_f32 v175, v235\n"
"v_exp_f32 v172, v236\n"
"v_exp_f32 v173, v237\n"
"s_nop 0\n"
"v_cvt_pk_bf16_f32 v141, v174, v175\n"
"s_memrealtime s[24:25]\n"
"s_waitcnt lgkmcnt(0)\n"
"s_add_u32 s87, s24, 50\n"
"s_mov_b32 s61, 11\n"
"v_mfma_f32_16x16x32_bf16 v[192:195], a[192:195], v[0:3], v[96:99]\n"
"v_cvt_pk_bf16_f32 v142, v172, v173\n"
"v_mfma_f32_16x16x32_bf16 v[192:195], a[196:199], v[4:7], v[192:195]\n"
"v_exp_f32 v174, v238\n"
"v_mfma_f32_16x16x32_bf16 v[192:195], a[200:203], v[8:11], v[192:195]\n"
"s_mov_b32 m0, s58\n"
"s_add_u32 s71, s56, 4096\n"
"v_or3_b32 v184, v184, v141, v142\n"
"v_mfma_f32_16x16x32_bf16 v[192:195], a[204:207], v[12:15], v[192:195]\n"
"v_exp_f32 v175, v239\n"
"v_mfma_f32_16x16x32_bf16 v[196:199], a[208:211], v[0:3], v[96:99]\n"
"v_cvt_pk_bf16_f32 v143, v174, v175\n"
"v_bitop3_b32 v184, v184, v143, s60 bitop3:0xa8\n"
"v_mfma_f32_16x16x32_bf16 v[196:199], a[212:215], v[4:7], v[196:199]\n"
"v_cmp_ne_u32 vcc, 0, v184\n"
"v_mfma_f32_16x16x32_bf16 v[196:199], a[216:219], v[8:11], v[196:199]\n"
"buffer_load_dwordx4 v246, s[28:31], s56 offen offset:0 nt lds\n"
"v_mfma_f32_16x16x32_bf16 v[196:199], a[220:223], v[12:15], v[196:199]\n"
"s_cbranch_vccnz .Lk1_tr1\n"
".Lk1_ct1:\n"
"v_exp_f32 v172, v192\n"
"v_mfma_f32_16x16x32_bf16 v[200:203], a[192:195], v[16:19], v[100:103]\n"
"s_cmp_lt_u32 s77, 0x18000\n"
"s_cselect_b32 s75, s79, s80\n"
"s_add_u32 s77, s77, s75\n"
"v_mfma_f32_16x16x32_bf16 v[200:203], a[196:199], v[20:23], v[200:203]\n"
"v_exp_f32 v173, v193\n"
"v_mfma_f32_16x16x32_bf16 v[200:203], a[200:203], v[24:27], v[200:203]\n"
"v_cvt_pk_bf16_f32 v120, v172, v173\n"
"v_add_u32 v240, s75, v240\n"
"v_mfma_f32_16x16x32_bf16 v[200:203], a[204:207], v[28:31], v[200:203]\n"
"v_exp_f32 v174, v194\n"
"v_mfma_f32_16x16x32_bf16 v[204:207], a[208:211], v[16:19], v[100:103]\n"
"buffer_load_dwordx4 v247, s[28:31], s56 offen offset:1024 nt lds\n"
"v_mfma_f32_16x16x32_bf16 v[204:207], a[212:215], v[20:23], v[204:207]\n"
"v_exp_f32 v175, v195\n"
"v_mfma_f32_16x16x32_bf16 v[204:207], a[216:219], v[24:27], v[204:207]\n"
"v_cvt_pk_bf16_f32 v121, v174, v175\n"
"v_mfma_f32_16x16x32_bf16 v[204:207], a[220:223], v[28:31], v[204:207]\n"
"v_exp_f32 v172, v196\n"
"v_mfma_f32_16x16x32_bf16 a[64:67], a[224:227], v[128:131], 0\n"
"buffer_load_dwordx4 v248, s[28:31], s56 offen offset:2048 nt lds\n"
"v_mfma_f32_16x16x32_bf16 a[96:99], a[224:227], v[132:135], 0\n"
"v_exp_f32 v173, v197\n"
"v_mfma_f32_16x16x32_bf16 a[68:71], a[228:231], v[128:131], 0\n"
"v_cvt_pk_bf16_f32 v122, v172, v173\n"
"v_mfma_f32_16x16x32_bf16 a[100:103], a[228:231], v[132:135], 0\n"
"v_exp_f32 v174, v198\n"
"v_mfma_f32_16x16x32_bf16 a[72:75], a[232:235], v[128:131], 0\n"
"v_or3_b32 v184, v120, v121, v122\n"
"v_mfma_f32_16x16x32_bf16 a[104:107], a[232:235], v[132:135], 0\n"
"v_exp_f32 v175, v199\n"
"v_mfma_f32_16x16x32_bf16 a[76:79], a[236:239], v[128:131], 0\n"
"buffer_load_dwordx4 v249, s[28:31], s56 offen offset:3072 nt lds\n"
"v_cvt_pk_bf16_f32 v123, v174, v175\n"
"v_mfma_f32_16x16x32_bf16 a[108:111], a[236:239], v[132:135], 0\n"
"s_add_u32 m0, s58, 4096\n"
"v_exp_f32 v172, v200\n"
"v_mfma_f32_16x16x32_bf16 a[80:83], a[240:243], v[128:131], 0\n"
"s_add_u32 s56, s56, 16384\n"
"s_and_b32 s56, s56, 0x3ffff\n"
"v_mfma_f32_16x16x32_bf16 a[112:115], a[240:243], v[132:135], 0\n"
"v_exp_f32 v173, v201\n"
"v_mfma_f32_16x16x32_bf16 a[84:87], a[244:247], v[128:131], 0\n"
"buffer_load_dwordx4 v246, s[28:31], s71 offen offset:0 nt lds\n"
"v_cvt_pk_bf16_f32 v124, v172, v173\n"
"v_mfma_f32_16x16x32_bf16 a[116:119], a[244:247], v[132:135], 0\n"
"v_exp_f32 v174, v202\n"
"v_mfma_f32_16x16x32_bf16 a[88:91], a[248:251], v[128:131], 0\n"
"v_or3_b32 v184, v184, v123, v124\n"
"v_mfma_f32_16x16x32_bf16 a[120:123], a[248:251], v[132:135], 0\n"
"v_exp_f32 v175, v203\n"
"v_mfma_f32_16x16x32_bf16 a[92:95], a[252:255], v[128:131], 0\n"
"v_cvt_pk_bf16_f32 v125, v174, v175\n"
"v_mfma_f32_16x16x32_bf16 a[124:127], a[252:255], v[132:135], 0\n"
"v_exp_f32 v172, v204\n"
"v_mfma_f32_16x16x32_bf16 v[152:155], v[168:171], v[128:131], 0\n"
"buffer_load_dwordx4 v247, s[28:31], s71 offen offset:1024 nt lds\n"
"v_mfma_f32_16x16x32_bf16 v[156:159], v[168:171], v[132:135], 0\n"
"v_exp_f32 v173, v205\n"
"v_mfma_f32_16x16x32_bf16 v[208:211], a[192:195], v[32:35], v[104:107]\n"
"v_cvt_pk_bf16_f32 v126, v172, v173\n"
"v_mfma_f32_16x16x32_bf16 v[208:211], a[196:199], v[36:39], v[208:211]\n"
"s_memrealtime s[22:23]\n"
"v_exp_f32 v174, v206\n"
"v_mfma_f32_16x16x32_bf16 v[208:211], a[200:203], v[40:43], v[208:211]\n"
"buffer_load_dwordx4 v248, s[28:31], s71 offen offset:2048 nt lds\n"
"v_or3_b32 v184, v184, v125, v126\n"
"v_mfma_f32_16x16x32_bf16 v[208:211], a[204:207], v[44:47], v[208:211]\n"
"v_exp_f32 v175, v207\n"
"v_mfma_f32_16x16x32_bf16 v[212:215], a[208:211], v[32:35], v[104:107]\n"
"v_cvt_pk_bf16_f32 v127, v174, v175\n"
"v_bitop3_b32 v184, v184, v127, s60 bitop3:0xa8\n"
"v_mfma_f32_16x16x32_bf16 v[212:215], a[212:215], v[36:39], v[212:215]\n"
"v_cmp_ne_u32 vcc, 0, v184\n"
"v_mfma_f32_16x16x32_bf16 v[212:215], a[216:219], v[40:43], v[212:215]\n"
"v_mfma_f32_16x16x32_bf16 v[212:215], a[220:223], v[44:47], v[212:215]\n"
"s_cbranch_vccnz .Lk1_tr2\n"
".Lk1_ct2:\n"
"v_exp_f32 v172, v208\n"
"v_mfma_f32_16x16x32_bf16 v[216:219], a[192:195], v[48:51], v[108:111]\n"
"buffer_load_dwordx4 v249, s[28:31], s71 offen offset:3072 nt lds\n"
"v_mfma_f32_16x16x32_bf16 v[216:219], a[196:199], v[52:55], v[216:219]\n"
"s_add_u32 s72, s58, 0x8000\n"
"v_exp_f32 v173, v209\n"
"v_mfma_f32_16x16x32_bf16 v[216:219], a[200:203], v[56:59], v[216:219]\n"
"s_waitcnt lgkmcnt(0)\n"
"v_cvt_pk_bf16_f32 v128, v172, v173\n"
"v_mfma_f32_16x16x32_bf16 v[216:219], a[204:207], v[60:63], v[216:219]\n"
"s_sub_u32 s25, s22, s24\n"
"v_exp_f32 v174, v210\n"
"v_mfma_f32_16x16x32_bf16 v[220:223], a[208:211], v[48:51], v[108:111]\n"
"s_cmp_lt_i32 s25, 0\n"
"s_cbranch_scc1 .Lk1_tr0\n"
".Lk1_ct0:\n"
"v_mfma_f32_16x16x32_bf16 v[220:223], a[212:215], v[52:55], v[220:223]\n"
"v_exp_f32 v175, v211\n"
"v_mfma_f32_16x16x32_bf16 v[220:223], a[216:219], v[56:59], v[220:223]\n"
"s_waitcnt vmcnt(16)\n"
"v_cvt_pk_bf16_f32 v129, v174, v175\n"
"v_mfma_f32_16x16x32_bf16 v[220:223], a[220:223], v[60:63], v[220:223]\n"
"s_barrier\n"
"v_exp_f32 v172, v212\n"
"v_mfma_f32_16x16x32_bf16 a[128:131], a[224:227], v[136:139], 0\n"
"s_add_u32 s24, s22, 118\n"
"s_add_u32 s87, s87, 124\n"
"s_max_u32 s24, s24, s87\n"
"v_mfma_f32_16x16x32_bf16 a[160:163], a[224:227], v[140:143], 0\n"
"v_exp_f32 v173, v213\n"
"v_mfma_f32_16x16x32_bf16 a[132:135], a[228:231], v[136:139], 0\n"
"ds_read_b64_tr_b16 a[224:225], v241 offset:8192\n"
"v_cvt_pk_bf16_f32 v130, v172, v173\n"
"v_mfma_f32_16x16x32_bf16 a[164:167], a[228:231], v[140:143], 0\n"
"v_exp_f32 v174, v214\n"
"v_mfma_f32_16x16x32_bf16 a[136:139], a[232:235], v[136:139], 0\n"
"ds_read_b64_tr_b16 a[226:227], v241 offset:8448\n"
"v_or3_b32 v184, v128, v129, v130\n"
"v_mfma_f32_16x16x32_bf16 a[168:171], a[232:235], v[140:143], 0\n"
"v_exp_f32 v175, v215\n"
"v_mfma_f32_16x16x32_bf16 a[140:143], a[236:239], v[136:139], 0\n"
"ds_read_b64_tr_b16 a[228:229], v242 offset:8192\n"
"v_cvt_pk_bf16_f32 v131, v174, v175\n"
"v_mfma_f32_16x16x32_bf16 a[172:175], a[236:239], v[140:143], 0\n"
"v_exp_f32 v172, v216\n"
"v_mfma_f32_16x16x32_bf16 a[144:147], a[240:243], v[136:139], 0\n"
"ds_read_b64_tr_b16 a[230:231], v242 offset:8448\n"
"ds_read_b64_tr_b16 a[232:233], v241 offset:8704\n"
"v_mfma_f32_16x16x32_bf16 a[176:179], a[240:243], v[140:143], 0\n"
"v_exp_f32 v173, v217\n"
"v_mfma_f32_16x16x32_bf16 a[148:151], a[244:247], v[136:139], 0\n"
"ds_read_b64_tr_b16 a[234:235], v241 offset:8960\n"
"v_cvt_pk_bf16_f32 v132, v172, v173\n"
"v_mfma_f32_16x16x32_bf16 a[180:183], a[244:247], v[140:143], 0\n"
"v_exp_f32 v174, v218\n"
"v_mfma_f32_16x16x32_bf16 a[152:155], a[248:251], v[136:139], 0\n"
"ds_read_b64_tr_b16 a[236:237], v242 offset:8704\n"
"v_or3_b32 v184, v184, v131, v132\n"
"v_mfma_f32_16x16x32_bf16 a[184:187], a[248:251], v[140:143], 0\n"
"v_exp_f32 v175, v219\n"
"v_mfma_f32_16x16x32_bf16 a[156:159], a[252:255], v[136:139], 0\n"
"ds_read_b64_tr_b16 a[238:239], v242 offset:8960\n"
"v_cvt_pk_bf16_f32 v133, v174, v175\n"
"v_mfma_f32_16x16x32_bf16 a[188:191], a[252:255], v[140:143], 0\n"
"v_exp_f32 v172, v220\n"
"v_mfma_f32_16x16x32_bf16 v[160:163], v[168:171], v[136:139], 0\n"
"ds_read_b64_tr_b16 a[240:241], v241 offset:9216\n"
"ds_read_b64_tr_b16 a[242:243], v241 offset:9472\n"
"v_mfma_f32_16x16x32_bf16 v[164:167], v[168:171], v[140:143], 0\n"
"v_exp_f32 v173, v221\n"
"v_mfma_f32_16x16x32_bf16 v[224:227], a[192:195], v[64:67], v[112:115]\n"
"ds_read_b64_tr_b16 a[244:245], v242 offset:9216\n"
"v_cvt_pk_bf16_f32 v134, v172, v173\n"
"v_mfma_f32_16x16x32_bf16 v[224:227], a[196:199], v[68:71], v[224:227]\n"
"v_exp_f32 v174, v222\n"
"v_mfma_f32_16x16x32_bf16 v[224:227], a[200:203], v[72:75], v[224:227]\n"
"ds_read_b64_tr_b16 a[246:247], v242 offset:9472\n"
"v_or3_b32 v184, v184, v133, v134\n"
"v_mfma_f32_16x16x32_bf16 v[224:227], a[204:207], v[76:79], v[224:227]\n"
"v_exp_f32 v175, v223\n"
"v_mfma_f32_16x16x32_bf16 v[228:231], a[208:211], v[64:67], v[112:115]\n"
"v_cvt_pk_bf16_f32 v135, v174, v175\n"
"v_bitop3_b32 v184, v184, v135, s60 bitop3:0xa8\n"
"v_mfma_f32_16x16x32_bf16 v[228:231], a[212:215], v[68:71], v[228:231]\n"
"ds_read_b64_tr_b16 a[248:249], v241 offset:9728\n"
"v_cmp_ne_u32 vcc, 0, v184\n"
"v_mfma_f32_16x16x32_bf16 v[228:231], a[216:219], v[72:75], v[228:231]\n"
"ds_read_b64_tr_b16 a[250:251], v241 offset:9984\n"
"ds_read_b64_tr_b16 a[252:253], v242 offset:9728\n"
"v_mfma_f32_16x16x32_bf16 v[228:231], a[220:223], v[76:79], v[228:231]\n"
"s_cbranch_vccnz .Lk1_tr3\n"
".Lk1_ct3:\n"
"v_exp_f32 v172, v224\n"
"v_mfma_f32_16x16x32_bf16 v[232:235], a[192:195], v[80:83], v[116:119]\n"
"ds_read_b64_tr_b16 a[254:255], v242 offset:9984\n"
"ds_read_b128 a[192:195], v240 offset:0\n"
"v_mfma_f32_16x16x32_bf16 v[232:235], a[196:199], v[84:87], v[232:235]\n"
"v_exp_f32 v173, v225\n"
"v_mfma_f32_16x16x32_bf16 v[232:235], a[200:203], v[88:91], v[232:235]\n"
"ds_read_b128 a[196:199], v240 offset:512\n"
"v_cvt_pk_bf16_f32 v136, v172, v173\n"
"v_mfma_f32_16x16x32_bf16 v[232:235], a[204:207], v[92:95], v[232:235]\n"
"v_exp_f32 v174, v226\n"
"v_mfma_f32_16x16x32_bf16 v[236:239], a[208:211], v[80:83], v[116:119]\n"
"ds_read_b128 a[200:203], v240 offset:1024\n"
"ds_read_b128 a[204:207], v240 offset:1536\n"
"v_mfma_f32_16x16x32_bf16 v[236:239], a[212:215], v[84:87], v[236:239]\n"
"v_exp_f32 v175, v227\n"
"v_mfma_f32_16x16x32_bf16 v[236:239], a[216:219], v[88:91], v[236:239]\n"
"ds_read_b128 a[208:211], v240 offset:256\n"
"v_cvt_pk_bf16_f32 v137, v174, v175\n"
"v_mfma_f32_16x16x32_bf16 v[236:239], a[220:223], v[92:95], v[236:239]\n"
"s_waitcnt lgkmcnt(13)\n"
"v_exp_f32 v172, v228\n"
"v_mfma_f32_16x16x32_bf16 a[0:3], a[224:227], v[120:123], a[0:3]\n"
"ds_read_b128 a[212:215], v240 offset:768\n"
"ds_read_b128 a[216:219], v240 offset:1280\n"
"v_mfma_f32_16x16x32_bf16 a[32:35], a[224:227], v[124:127], a[32:35]\n"
"v_exp_f32 v173, v229\n"
"v_mfma_f32_16x16x32_bf16 a[4:7], a[228:231], v[120:123], a[4:7]\n"
"ds_read_b128 a[220:223], v240 offset:1792\n"
"v_cvt_pk_bf16_f32 v138, v172, v173\n"
"v_mfma_f32_16x16x32_bf16 a[36:39], a[228:231], v[124:127], a[36:39]\n"
"v_exp_f32 v174, v230\n"
"v_mfma_f32_16x16x32_bf16 a[8:11], a[232:235], v[120:123], a[8:11]\n"
"s_cmp_lt_u32 s72, s81\n"
"s_cselect_b32 s58, s72, s59\n"
"s_mov_b64 exec, 0x37f\n"
"buffer_store_dword v254, v253, s[52:55], 0 offen offset:8 sc1\n"
"s_mov_b64 exec, -1\n"
"v_or3_b32 v184, v136, v137, v138\n"
"v_mfma_f32_16x16x32_bf16 a[40:43], a[232:235], v[124:127], a[40:43]\n"
"v_exp_f32 v175, v231\n"
"v_mfma_f32_16x16x32_bf16 a[12:15], a[236:239], v[120:123], a[12:15]\n"
"s_cmp_lt_u32 s78, 0x18000\n"
"s_cselect_b32 s75, s79, s80\n"
"v_cvt_pk_bf16_f32 v139, v174, v175\n"
"v_mfma_f32_16x16x32_bf16 a[44:47], a[236:239], v[124:127], a[44:47]\n"
"s_waitcnt lgkmcnt(8)\n"
"v_exp_f32 v172, v232\n"
"v_mfma_f32_16x16x32_bf16 a[16:19], a[240:243], v[120:123], a[16:19]\n"
"s_add_u32 s78, s78, s75\n"
"v_mfma_f32_16x16x32_bf16 a[48:51], a[240:243], v[124:127], a[48:51]\n"
"v_exp_f32 v173, v233\n"
"v_mfma_f32_16x16x32_bf16 a[20:23], a[244:247], v[120:123], a[20:23]\n"
"v_cvt_pk_bf16_f32 v140, v172, v173\n"
"v_add_u32 v241, s75, v241\n"
"v_mfma_f32_16x16x32_bf16 a[52:55], a[244:247], v[124:127], a[52:55]\n"
"v_exp_f32 v174, v234\n"
"v_mfma_f32_16x16x32_bf16 a[24:27], a[248:251], v[120:123], a[24:27]\n"
"v_or3_b32 v184, v184, v139, v140\n"
"v_add_u32 v242, s75, v242\n"
"v_mfma_f32_16x16x32_bf16 a[56:59], a[248:251], v[124:127], a[56:59]\n"
"v_exp_f32 v175, v235\n"
"v_mfma_f32_16x16x32_bf16 a[28:31], a[252:255], v[120:123], a[28:31]\n"
"v_cvt_pk_bf16_f32 v141, v174, v175\n"
"v_mfma_f32_16x16x32_bf16 a[60:63], a[252:255], v[124:127], a[60:63]\n"
"v_exp_f32 v172, v236\n"
"v_mfma_f32_16x16x32_bf16 v[144:147], v[168:171], v[120:123], v[144:147]\n"
"v_mfma_f32_16x16x32_bf16 v[148:151], v[168:171], v[124:127], v[148:151]\n"
"s_waitcnt lgkmcnt(4)\n"
"v_exp_f32 v173, v237\n"
"v_mfma_f32_16x16x32_bf16 v[192:195], a[192:195], v[0:3], v[96:99]\n"
"v_cvt_pk_bf16_f32 v142, v172, v173\n"
"v_lshrrev_b32 v189, 3, v255\n"
"v_mfma_f32_16x16x32_bf16 v[192:195], a[196:199], v[4:7], v[192:195]\n"
"v_exp_f32 v174, v238\n"
"v_mfma_f32_16x16x32_bf16 v[192:195], a[200:203], v[8:11], v[192:195]\n"
"s_mov_b32 m0, s58\n"
"s_add_u32 s71, s57, 4096\n"
"v_or3_b32 v184, v184, v141, v142\n"
"v_mfma_f32_16x16x32_bf16 v[192:195], a[204:207], v[12:15], v[192:195]\n"
"s_waitcnt lgkmcnt(0)\n"
"v_exp_f32 v175, v239\n"
"v_mfma_f32_16x16x32_bf16 v[196:199], a[208:211], v[0:3], v[96:99]\n"
"v_cvt_pk_bf16_f32 v143, v174, v175\n"
"v_bitop3_b32 v184, v184, v143, s60 bitop3:0xa8\n"
"v_mfma_f32_16x16x32_bf16 v[196:199], a[212:215], v[4:7], v[196:199]\n"
"v_cmp_ne_u32 vcc, 0, v184\n"
"v_and_b32 v188, 6, v189\n"
"v_mfma_f32_16x16x32_bf16 v[196:199], a[216:219], v[8:11], v[196:199]\n"
"buffer_load_dwordx4 v246, s[36:39], s57 offen offset:0 nt lds\n"
"v_xor_b32 v188, v188, v244\n"
"v_mfma_f32_16x16x32_bf16 v[196:199], a[220:223], v[12:15], v[196:199]\n"
"s_cbranch_vccnz .Lk1_tr5\n"
".Lk1_ct5:\n"
"v_exp_f32 v172, v192\n"
"v_mfma_f32_16x16x32_bf16 v[200:203], a[192:195], v[16:19], v[100:103]\n"
"v_lshlrev_b32 v188, 4, v188\n"
"v_lshl_add_u32 v188, v189, 10, v188\n"
"v_mfma_f32_16x16x32_bf16 v[200:203], a[196:199], v[20:23], v[200:203]\n"
"v_exp_f32 v173, v193\n"
"v_mfma_f32_16x16x32_bf16 v[200:203], a[200:203], v[24:27], v[200:203]\n"
"v_cvt_pk_bf16_f32 v120, v172, v173\n"
"v_add_u32 v189, 0x1c00, v188\n"
"v_mfma_f32_16x16x32_bf16 v[200:203], a[204:207], v[28:31], v[200:203]\n"
"v_exp_f32 v174, v194\n"
"v_mfma_f32_16x16x32_bf16 v[204:207], a[208:211], v[16:19], v[100:103]\n"
"buffer_load_dwordx4 v247, s[36:39], s57 offen offset:1024 nt lds\n"
"v_and_b32 v185, 15, v255\n"
"v_mfma_f32_16x16x32_bf16 v[204:207], a[212:215], v[20:23], v[204:207]\n"
"v_exp_f32 v175, v195\n"
"v_mfma_f32_16x16x32_bf16 v[204:207], a[216:219], v[24:27], v[204:207]\n"
"v_cvt_pk_bf16_f32 v121, v174, v175\n"
"v_lshlrev_b32 v185, 2, v185\n"
"v_mfma_f32_16x16x32_bf16 v[204:207], a[220:223], v[28:31], v[204:207]\n"
"v_exp_f32 v172, v196\n"
"v_mfma_f32_16x16x32_bf16 a[64:67], a[224:227], v[128:131], a[64:67]\n"
"buffer_load_dwordx4 v248, s[36:39], s57 offen offset:2048 nt lds\n"
"v_mfma_f32_16x16x32_bf16 a[96:99], a[224:227], v[132:135], a[96:99]\n"
"v_exp_f32 v173, v197\n"
"v_mfma_f32_16x16x32_bf16 a[68:71], a[228:231], v[128:131], a[68:71]\n"
"v_cvt_pk_bf16_f32 v122, v172, v173\n"
"v_mfma_f32_16x16x32_bf16 a[100:103], a[228:231], v[132:135], a[100:103]\n"
"v_exp_f32 v174, v198\n"
"v_mfma_f32_16x16x32_bf16 a[72:75], a[232:235], v[128:131], a[72:75]\n"
"v_or3_b32 v184, v120, v121, v122\n"
"v_mfma_f32_16x16x32_bf16 a[104:107], a[232:235], v[132:135], a[104:107]\n"
"v_exp_f32 v175, v199\n"
"v_mfma_f32_16x16x32_bf16 a[76:79], a[236:239], v[128:131], a[76:79]\n"
"buffer_load_dwordx4 v249, s[36:39], s57 offen offset:3072 nt lds\n"
"v_cvt_pk_bf16_f32 v123, v174, v175\n"
"v_mfma_f32_16x16x32_bf16 a[108:111], a[236:239], v[132:135], a[108:111]\n"
"s_add_u32 m0, s58, 4096\n"
"v_exp_f32 v172, v200\n"
"v_mfma_f32_16x16x32_bf16 a[80:83], a[240:243], v[128:131], a[80:83]\n"
"s_add_u32 s57, s57, 16384\n"
"s_and_b32 s57, s57, 0x3ffff\n"
"v_mfma_f32_16x16x32_bf16 a[112:115], a[240:243], v[132:135], a[112:115]\n"
"v_exp_f32 v173, v201\n"
"v_mfma_f32_16x16x32_bf16 a[84:87], a[244:247], v[128:131], a[84:87]\n"
"buffer_load_dwordx4 v246, s[36:39], s71 offen offset:0 nt lds\n"
"v_cvt_pk_bf16_f32 v124, v172, v173\n"
"v_mfma_f32_16x16x32_bf16 a[116:119], a[244:247], v[132:135], a[116:119]\n"
"v_exp_f32 v174, v202\n"
"v_mfma_f32_16x16x32_bf16 a[88:91], a[248:251], v[128:131], a[88:91]\n"
"v_or3_b32 v184, v184, v123, v124\n"
"v_mfma_f32_16x16x32_bf16 a[120:123], a[248:251], v[132:135], a[120:123]\n"
"v_exp_f32 v175, v203\n"
"v_mfma_f32_16x16x32_bf16 a[92:95], a[252:255], v[128:131], a[92:95]\n"
"v_cvt_pk_bf16_f32 v125, v174, v175\n"
"v_mfma_f32_16x16x32_bf16 a[124:127], a[252:255], v[132:135], a[124:127]\n"
"v_exp_f32 v172, v204\n"
"v_mfma_f32_16x16x32_bf16 v[152:155], v[168:171], v[128:131], v[152:155]\n"
"buffer_load_dwordx4 v247, s[36:39], s71 offen offset:1024 nt lds\n"
"v_mfma_f32_16x16x32_bf16 v[156:159], v[168:171], v[132:135], v[156:159]\n"
"v_exp_f32 v173, v205\n"
"v_mfma_f32_16x16x32_bf16 v[208:211], a[192:195], v[32:35], v[104:107]\n"
"v_cvt_pk_bf16_f32 v126, v172, v173\n"
"v_mfma_f32_16x16x32_bf16 v[208:211], a[196:199], v[36:39], v[208:211]\n"
"s_memrealtime s[22:23]\n"
"v_exp_f32 v174, v206\n"
"v_mfma_f32_16x16x32_bf16 v[208:211], a[200:203], v[40:43], v[208:211]\n"
"buffer_load_dwordx4 v248, s[36:39], s71 offen offset:2048 nt lds\n"
"v_or3_b32 v184, v184, v125, v126\n"
"v_mfma_f32_16x16x32_bf16 v[208:211], a[204:207], v[44:47], v[208:211]\n"
"v_exp_f32 v175, v207\n"
"v_mfma_f32_16x16x32_bf16 v[212:215], a[208:211], v[32:35], v[104:107]\n"
"v_cvt_pk_bf16_f32 v127, v174, v175\n"
"v_bitop3_b32 v184, v184, v127, s60 bitop3:0xa8\n"
"v_mfma_f32_16x16x32_bf16 v[212:215], a[212:215], v[36:39], v[212:215]\n"
"v_cmp_ne_u32 vcc, 0, v184\n"
"v_mfma_f32_16x16x32_bf16 v[212:215], a[216:219], v[40:43], v[212:215]\n"
"v_mfma_f32_16x16x32_bf16 v[212:215], a[220:223], v[44:47], v[212:215]\n"
"s_cbranch_vccnz .Lk1_tr6\n"
".Lk1_ct6:\n"
"v_exp_f32 v172, v208\n"
"v_mfma_f32_16x16x32_bf16 v[216:219], a[192:195], v[48:51], v[108:111]\n"
"buffer_load_dwordx4 v249, s[36:39], s71 offen offset:3072 nt lds\n"
"v_mfma_f32_16x16x32_bf16 v[216:219], a[196:199], v[52:55], v[216:219]\n"
"s_add_u32 s72, s58, 0x8000\n"
"v_exp_f32 v173, v209\n"
"v_mfma_f32_16x16x32_bf16 v[216:219], a[200:203], v[56:59], v[216:219]\n"
"s_waitcnt lgkmcnt(0)\n"
"v_cvt_pk_bf16_f32 v128, v172, v173\n"
"v_mfma_f32_16x16x32_bf16 v[216:219], a[204:207], v[60:63], v[216:219]\n"
"s_sub_u32 s25, s22, s24\n"
"v_exp_f32 v174, v210\n"
"v_mfma_f32_16x16x32_bf16 v[220:223], a[208:211], v[48:51], v[108:111]\n"
"s_cmp_lt_i32 s25, 0\n"
"s_cbranch_scc1 .Lk1_tr4\n"
".Lk1_ct4:\n"
"v_mfma_f32_16x16x32_bf16 v[220:223], a[212:215], v[52:55], v[220:223]\n"
"v_exp_f32 v175, v211\n"
"v_mfma_f32_16x16x32_bf16 v[220:223], a[216:219], v[56:59], v[220:223]\n"
"s_waitcnt vmcnt(16)\n"
"v_cvt_pk_bf16_f32 v129, v174, v175\n"
"v_mfma_f32_16x16x32_bf16 v[220:223], a[220:223], v[60:63], v[220:223]\n"
"s_barrier\n"
"v_exp_f32 v172, v212\n"
"v_mfma_f32_16x16x32_bf16 a[128:131], a[224:227], v[136:139], a[128:131]\n"
"s_add_u32 s24, s22, 118\n"
"s_add_u32 s87, s87, 124\n"
"s_max_u32 s24, s24, s87\n"
"v_mfma_f32_16x16x32_bf16 a[160:163], a[224:227], v[140:143], a[160:163]\n"
"v_exp_f32 v173, v213\n"
"v_mfma_f32_16x16x32_bf16 a[132:135], a[228:231], v[136:139], a[132:135]\n"
"ds_read_b64_tr_b16 a[224:225], v241 offset:0\n"
"v_cvt_pk_bf16_f32 v130, v172, v173\n"
"v_mfma_f32_16x16x32_bf16 a[164:167], a[228:231], v[140:143], a[164:167]\n"
"v_exp_f32 v174, v214\n"
"v_mfma_f32_16x16x32_bf16 a[136:139], a[232:235], v[136:139], a[136:139]\n"
"ds_read_b64_tr_b16 a[226:227], v241 offset:256\n"
"v_or3_b32 v184, v128, v129, v130\n"
"v_mfma_f32_16x16x32_bf16 a[168:171], a[232:235], v[140:143], a[168:171]\n"
"v_exp_f32 v175, v215\n"
"v_mfma_f32_16x16x32_bf16 a[140:143], a[236:239], v[136:139], a[140:143]\n"
"ds_read_b64_tr_b16 a[228:229], v242 offset:0\n"
"v_cvt_pk_bf16_f32 v131, v174, v175\n"
"v_mfma_f32_16x16x32_bf16 a[172:175], a[236:239], v[140:143], a[172:175]\n"
"v_exp_f32 v172, v216\n"
"v_mfma_f32_16x16x32_bf16 a[144:147], a[240:243], v[136:139], a[144:147]\n"
"ds_read_b64_tr_b16 a[230:231], v242 offset:256\n"
"ds_read_b64_tr_b16 a[232:233], v241 offset:512\n"
"v_mfma_f32_16x16x32_bf16 a[176:179], a[240:243], v[140:143], a[176:179]\n"
"v_exp_f32 v173, v217\n"
"v_mfma_f32_16x16x32_bf16 a[148:151], a[244:247], v[136:139], a[148:151]\n"
"ds_read_b64_tr_b16 a[234:235], v241 offset:768\n"
"v_cvt_pk_bf16_f32 v132, v172, v173\n"
"v_mfma_f32_16x16x32_bf16 a[180:183], a[244:247], v[140:143], a[180:183]\n"
"v_exp_f32 v174, v218\n"
"v_mfma_f32_16x16x32_bf16 a[152:155], a[248:251], v[136:139], a[152:155]\n"
"ds_read_b64_tr_b16 a[236:237], v242 offset:512\n"
"v_or3_b32 v184, v184, v131, v132\n"
"v_mfma_f32_16x16x32_bf16 a[184:187], a[248:251], v[140:143], a[184:187]\n"
"v_exp_f32 v175, v219\n"
"v_mfma_f32_16x16x32_bf16 a[156:159], a[252:255], v[136:139], a[156:159]\n"
"ds_read_b64_tr_b16 a[238:239], v242 offset:768\n"
"v_cvt_pk_bf16_f32 v133, v174, v175\n"
"v_mfma_f32_16x16x32_bf16 a[188:191], a[252:255], v[140:143], a[188:191]\n"
"v_exp_f32 v172, v220\n"
"v_mfma_f32_16x16x32_bf16 v[160:163], v[168:171], v[136:139], v[160:163]\n"
"ds_read_b64_tr_b16 a[240:241], v241 offset:1024\n"
"ds_read_b64_tr_b16 a[242:243], v241 offset:1280\n"
"v_mfma_f32_16x16x32_bf16 v[164:167], v[168:171], v[140:143], v[164:167]\n"
"v_exp_f32 v173, v221\n"
"v_mfma_f32_16x16x32_bf16 v[224:227], a[192:195], v[64:67], v[112:115]\n"
"ds_read_b64_tr_b16 a[244:245], v242 offset:1024\n"
"v_cvt_pk_bf16_f32 v134, v172, v173\n"
"v_mfma_f32_16x16x32_bf16 v[224:227], a[196:199], v[68:71], v[224:227]\n"
"v_exp_f32 v174, v222\n"
"v_mfma_f32_16x16x32_bf16 v[224:227], a[200:203], v[72:75], v[224:227]\n"
"ds_read_b64_tr_b16 a[246:247], v242 offset:1280\n"
"v_or3_b32 v184, v184, v133, v134\n"
"v_mfma_f32_16x16x32_bf16 v[224:227], a[204:207], v[76:79], v[224:227]\n"
"v_exp_f32 v175, v223\n"
"v_mfma_f32_16x16x32_bf16 v[228:231], a[208:211], v[64:67], v[112:115]\n"
"v_cvt_pk_bf16_f32 v135, v174, v175\n"
"v_bitop3_b32 v184, v184, v135, s60 bitop3:0xa8\n"
"v_mfma_f32_16x16x32_bf16 v[228:231], a[212:215], v[68:71], v[228:231]\n"
"ds_read_b64_tr_b16 a[248:249], v241 offset:1536\n"
"v_cmp_ne_u32 vcc, 0, v184\n"
"v_mfma_f32_16x16x32_bf16 v[228:231], a[216:219], v[72:75], v[228:231]\n"
"ds_read_b64_tr_b16 a[250:251], v241 offset:1792\n"
"ds_read_b64_tr_b16 a[252:253], v242 offset:1536\n"
"v_mfma_f32_16x16x32_bf16 v[228:231], a[220:223], v[76:79], v[228:231]\n"
"s_cbranch_vccnz .Lk1_tr7\n"
".Lk1_ct7:\n"
"v_exp_f32 v172, v224\n"
"v_mfma_f32_16x16x32_bf16 v[232:235], a[192:195], v[80:83], v[116:119]\n"
"ds_read_b64_tr_b16 a[254:255], v242 offset:1792\n"
"ds_read_b128 a[192:195], v240 offset:8192\n"
"v_mfma_f32_16x16x32_bf16 v[232:235], a[196:199], v[84:87], v[232:235]\n"
"v_exp_f32 v173, v225\n"
"v_mfma_f32_16x16x32_bf16 v[232:235], a[200:203], v[88:91], v[232:235]\n"
"ds_read_b128 a[196:199], v240 offset:8704\n"
"v_cvt_pk_bf16_f32 v136, v172, v173\n"
"v_mfma_f32_16x16x32_bf16 v[232:235], a[204:207], v[92:95], v[232:235]\n"
"v_exp_f32 v174, v226\n"
"v_mfma_f32_16x16x32_bf16 v[236:239], a[208:211], v[80:83], v[116:119]\n"
"ds_read_b128 a[200:203], v240 offset:9216\n"
"ds_read_b128 a[204:207], v240 offset:9728\n"
"v_mfma_f32_16x16x32_bf16 v[236:239], a[212:215], v[84:87], v[236:239]\n"
"v_exp_f32 v175, v227\n"
"v_mfma_f32_16x16x32_bf16 v[236:239], a[216:219], v[88:91], v[236:239]\n"
"ds_read_b128 a[208:211], v240 offset:8448\n"
"v_cvt_pk_bf16_f32 v137, v174, v175\n"
"v_mfma_f32_16x16x32_bf16 v[236:239], a[220:223], v[92:95], v[236:239]\n"
"s_waitcnt lgkmcnt(13)\n"
"v_exp_f32 v172, v228\n"
"v_mfma_f32_16x16x32_bf16 a[0:3], a[224:227], v[120:123], a[0:3]\n"
"ds_read_b128 a[212:215], v240 offset:8960\n"
"ds_read_b128 a[216:219], v240 offset:9472\n"
"v_mfma_f32_16x16x32_bf16 a[32:35], a[224:227], v[124:127], a[32:35]\n"
"v_exp_f32 v173, v229\n"
"v_mfma_f32_16x16x32_bf16 a[4:7], a[228:231], v[120:123], a[4:7]\n"
"ds_read_b128 a[220:223], v240 offset:9984\n"
"v_cvt_pk_bf16_f32 v138, v172, v173\n"
"v_mfma_f32_16x16x32_bf16 a[36:39], a[228:231], v[124:127], a[36:39]\n"
"v_exp_f32 v174, v230\n"
"v_mfma_f32_16x16x32_bf16 a[8:11], a[232:235], v[120:123], a[8:11]\n"
"s_cmp_lt_u32 s72, s81\n"
"s_cselect_b32 s58, s72, s59\n"
"v_or3_b32 v184, v136, v137, v138\n"
"v_mfma_f32_16x16x32_bf16 a[40:43], a[232:235], v[124:127], a[40:43]\n"
"v_exp_f32 v175, v231\n"
"v_mfma_f32_16x16x32_bf16 a[12:15], a[236:239], v[120:123], a[12:15]\n"
"v_cvt_pk_bf16_f32 v139, v174, v175\n"
"v_mfma_f32_16x16x32_bf16 a[44:47], a[236:239], v[124:127], a[44:47]\n"
"s_waitcnt lgkmcnt(8)\n"
"v_exp_f32 v172, v232\n"
"v_mfma_f32_16x16x32_bf16 a[16:19], a[240:243], v[120:123], a[16:19]\n"
"v_mfma_f32_16x16x32_bf16 a[48:51], a[240:243], v[124:127], a[48:51]\n"
"v_exp_f32 v173, v233\n"
"v_mfma_f32_16x16x32_bf16 a[20:23], a[244:247], v[120:123], a[20:23]\n"
"v_cvt_pk_bf16_f32 v140, v172, v173\n"
"v_mfma_f32_16x16x32_bf16 a[52:55], a[244:247], v[124:127], a[52:55]\n"
"v_exp_f32 v174, v234\n"
"v_mfma_f32_16x16x32_bf16 a[24:27], a[248:251], v[120:123], a[24:27]\n"
"v_or3_b32 v184, v184, v139, v140\n"
"v_mfma_f32_16x16x32_bf16 a[56:59], a[248:251], v[124:127], a[56:59]\n"
"v_exp_f32 v175, v235\n"
"v_mfma_f32_16x16x32_bf16 a[28:31], a[252:255], v[120:123], a[28:31]\n"
"v_cvt_pk_bf16_f32 v141, v174, v175\n"
"v_mfma_f32_16x16x32_bf16 a[60:63], a[252:255], v[124:127], a[60:63]\n"
"v_exp_f32 v172, v236\n"
"v_mfma_f32_16x16x32_bf16 v[144:147], v[168:171], v[120:123], v[144:147]\n"
".Lk1_loop:\n"
"v_mfma_f32_16x16x32_bf16 v[148:151], v[168:171], v[124:127], v[148:151]\n"
"s_waitcnt lgkmcnt(4)\n"
"v_exp_f32 v173, v237\n"
"v_mfma_f32_16x16x32_bf16 v[192:195], a[192:195], v[0:3], v[96:99]\n"
"v_cvt_pk_bf16_f32 v142, v172, v173\n"
"v_mfma_f32_16x16x32_bf16 v[192:195], a[196:199], v[4:7], v[192:195]\n"
"v_exp_f32 v174, v238\n"
"v_mfma_f32_16x16x32_bf16 v[192:195], a[200:203], v[8:11], v[192:195]\n"
"s_mov_b32 m0, s58\n"
"s_add_u32 s71, s56, 4096\n"
"v_or3_b32 v184, v184, v141, v142\n"
"v_mfma_f32_16x16x32_bf16 v[192:195], a[204:207], v[12:15], v[192:195]\n"
"s_waitcnt lgkmcnt(0)\n"
"v_exp_f32 v175, v239\n"
"v_mfma_f32_16x16x32_bf16 v[196:199], a[208:211], v[0:3], v[96:99]\n"
"v_cvt_pk_bf16_f32 v143, v174, v175\n"
"v_bitop3_b32 v184, v184, v143, s60 bitop3:0xa8\n"
"v_mfma_f32_16x16x32_bf16 v[196:199], a[212:215], v[4:7], v[196:199]\n"
"v_cmp_ne_u32 vcc, 0, v184\n"
"v_mfma_f32_16x16x32_bf16 v[196:199], a[216:219], v[8:11], v[196:199]\n"
"buffer_load_dwordx4 v246, s[28:31], s56 offen offset:0 nt lds\n"
"v_mfma_f32_16x16x32_bf16 v[196:199], a[220:223], v[12:15], v[196:199]\n"
"s_cbranch_vccnz .Lk1_tr9\n"
".Lk1_ct9:\n"
"v_exp_f32 v172, v192\n"
"v_mfma_f32_16x16x32_bf16 v[200:203], a[192:195], v[16:19], v[100:103]\n"
"s_cmp_lt_u32 s77, 0x18000\n"
"s_cselect_b32 s75, s79, s80\n"
"s_add_u32 s77, s77, s75\n"
"v_mfma_f32_16x16x32_bf16 v[200:203], a[196:199], v[20:23], v[200:203]\n"
"v_exp_f32 v173, v193\n"
"v_mfma_f32_16x16x32_bf16 v[200:203], a[200:203], v[24:27], v[200:203]\n"
"v_cvt_pk_bf16_f32 v120, v172, v173\n"
"v_add_u32 v240, s75, v240\n"
"v_mfma_f32_16x16x32_bf16 v[200:203], a[204:207], v[28:31], v[200:203]\n"
"v_exp_f32 v174, v194\n"
"v_mfma_f32_16x16x32_bf16 v[204:207], a[208:211], v[16:19], v[100:103]\n"
"buffer_load_dwordx4 v247, s[28:31], s56 offen offset:1024 nt lds\n"
"v_mfma_f32_16x16x32_bf16 v[204:207], a[212:215], v[20:23], v[204:207]\n"
"v_exp_f32 v175, v195\n"
"v_mfma_f32_16x16x32_bf16 v[204:207], a[216:219], v[24:27], v[204:207]\n"
"v_cvt_pk_bf16_f32 v121, v174, v175\n"
"v_mfma_f32_16x16x32_bf16 v[204:207], a[220:223], v[28:31], v[204:207]\n"
"v_exp_f32 v172, v196\n"
"v_mfma_f32_16x16x32_bf16 a[64:67], a[224:227], v[128:131], a[64:67]\n"
"buffer_load_dwordx4 v248, s[28:31], s56 offen offset:2048 nt lds\n"
"v_mfma_f32_16x16x32_bf16 a[96:99], a[224:227], v[132:135], a[96:99]\n"
"v_exp_f32 v173, v197\n"
"v_mfma_f32_16x16x32_bf16 a[68:71], a[228:231], v[128:131], a[68:71]\n"
"v_cvt_pk_bf16_f32 v122, v172, v173\n"
"v_mfma_f32_16x16x32_bf16 a[100:103], a[228:231], v[132:135], a[100:103]\n"
"v_exp_f32 v174, v198\n"
"v_mfma_f32_16x16x32_bf16 a[72:75], a[232:235], v[128:131], a[72:75]\n"
"v_or3_b32 v184, v120, v121, v122\n"
"v_mfma_f32_16x16x32_bf16 a[104:107], a[232:235], v[132:135], a[104:107]\n"
"v_exp_f32 v175, v199\n"
"v_mfma_f32_16x16x32_bf16 a[76:79], a[236:239], v[128:131], a[76:79]\n"
"buffer_load_dwordx4 v249, s[28:31], s56 offen offset:3072 nt lds\n"
"v_cvt_pk_bf16_f32 v123, v174, v175\n"
"v_mfma_f32_16x16x32_bf16 a[108:111], a[236:239], v[132:135], a[108:111]\n"
"s_add_u32 m0, s58, 4096\n"
"v_exp_f32 v172, v200\n"
"v_mfma_f32_16x16x32_bf16 a[80:83], a[240:243], v[128:131], a[80:83]\n"
"s_add_u32 s56, s56, 16384\n"
"s_and_b32 s56, s56, 0x3ffff\n"
"v_mfma_f32_16x16x32_bf16 a[112:115], a[240:243], v[132:135], a[112:115]\n"
"v_exp_f32 v173, v201\n"
"v_mfma_f32_16x16x32_bf16 a[84:87], a[244:247], v[128:131], a[84:87]\n"
"buffer_load_dwordx4 v246, s[28:31], s71 offen offset:0 nt lds\n"
"v_cvt_pk_bf16_f32 v124, v172, v173\n"
"v_mfma_f32_16x16x32_bf16 a[116:119], a[244:247], v[132:135], a[116:119]\n"
"v_exp_f32 v174, v202\n"
"v_mfma_f32_16x16x32_bf16 a[88:91], a[248:251], v[128:131], a[88:91]\n"
"v_or3_b32 v184, v184, v123, v124\n"
"v_mfma_f32_16x16x32_bf16 a[120:123], a[248:251], v[132:135], a[120:123]\n"
"v_exp_f32 v175, v203\n"
"v_mfma_f32_16x16x32_bf16 a[92:95], a[252:255], v[128:131], a[92:95]\n"
"v_cvt_pk_bf16_f32 v125, v174, v175\n"
"v_mfma_f32_16x16x32_bf16 a[124:127], a[252:255], v[132:135], a[124:127]\n"
"v_exp_f32 v172, v204\n"
"v_mfma_f32_16x16x32_bf16 v[152:155], v[168:171], v[128:131], v[152:155]\n"
"buffer_load_dwordx4 v247, s[28:31], s71 offen offset:1024 nt lds\n"
"v_mfma_f32_16x16x32_bf16 v[156:159], v[168:171], v[132:135], v[156:159]\n"
"v_exp_f32 v173, v205\n"
"v_mfma_f32_16x16x32_bf16 v[208:211], a[192:195], v[32:35], v[104:107]\n"
"v_cvt_pk_bf16_f32 v126, v172, v173\n"
"v_mfma_f32_16x16x32_bf16 v[208:211], a[196:199], v[36:39], v[208:211]\n"
"s_memrealtime s[22:23]\n"
"v_exp_f32 v174, v206\n"
"v_mfma_f32_16x16x32_bf16 v[208:211], a[200:203], v[40:43], v[208:211]\n"
"buffer_load_dwordx4 v248, s[28:31], s71 offen offset:2048 nt lds\n"
"v_or3_b32 v184, v184, v125, v126\n"
"v_mfma_f32_16x16x32_bf16 v[208:211], a[204:207], v[44:47], v[208:211]\n"
"v_exp_f32 v175, v207\n"
"v_mfma_f32_16x16x32_bf16 v[212:215], a[208:211], v[32:35], v[104:107]\n"
"v_cvt_pk_bf16_f32 v127, v174, v175\n"
"v_bitop3_b32 v184, v184, v127, s60 bitop3:0xa8\n"
"v_mfma_f32_16x16x32_bf16 v[212:215], a[212:215], v[36:39], v[212:215]\n"
"v_cmp_ne_u32 vcc, 0, v184\n"
"v_mfma_f32_16x16x32_bf16 v[212:215], a[216:219], v[40:43], v[212:215]\n"
"v_mfma_f32_16x16x32_bf16 v[212:215], a[220:223], v[44:47], v[212:215]\n"
"s_cbranch_vccnz .Lk1_tr10\n"
".Lk1_ct10:\n"
"v_exp_f32 v172, v208\n"
"v_mfma_f32_16x16x32_bf16 v[216:219], a[192:195], v[48:51], v[108:111]\n"
"buffer_load_dwordx4 v249, s[28:31], s71 offen offset:3072 nt lds\n"
"v_mfma_f32_16x16x32_bf16 v[216:219], a[196:199], v[52:55], v[216:219]\n"
"s_add_u32 s72, s58, 0x8000\n"
"v_exp_f32 v173, v209\n"
"v_mfma_f32_16x16x32_bf16 v[216:219], a[200:203], v[56:59], v[216:219]\n"
"s_waitcnt lgkmcnt(0)\n"
"v_cvt_pk_bf16_f32 v128, v172, v173\n"
"v_mfma_f32_16x16x32_bf16 v[216:219], a[204:207], v[60:63], v[216:219]\n"
"s_sub_u32 s25, s22, s24\n"
"v_exp_f32 v174, v210\n"
"v_mfma_f32_16x16x32_bf16 v[220:223], a[208:211], v[48:51], v[108:111]\n"
"s_cmp_lt_i32 s25, 0\n"
"s_cbranch_scc1 .Lk1_tr8\n"
".Lk1_ct8:\n"
"v_mfma_f32_16x16x32_bf16 v[220:223], a[212:215], v[52:55], v[220:223]\n"
"v_exp_f32 v175, v211\n"
"v_mfma_f32_16x16x32_bf16 v[220:223], a[216:219], v[56:59], v[220:223]\n"
"s_waitcnt vmcnt(16)\n"
"v_cvt_pk_bf16_f32 v129, v174, v175\n"
"v_mfma_f32_16x16x32_bf16 v[220:223], a[220:223], v[60:63], v[220:223]\n"
"s_barrier\n"
"v_exp_f32 v172, v212\n"
"v_mfma_f32_16x16x32_bf16 a[128:131], a[224:227], v[136:139], a[128:131]\n"
"s_add_u32 s24, s22, 118\n"
"s_add_u32 s87, s87, 124\n"
"s_max_u32 s24, s24, s87\n"
"v_mfma_f32_16x16x32_bf16 a[160:163], a[224:227], v[140:143], a[160:163]\n"
"v_exp_f32 v173, v213\n"
"v_mfma_f32_16x16x32_bf16 a[132:135], a[228:231], v[136:139], a[132:135]\n"
"ds_read_b64_tr_b16 a[224:225], v241 offset:8192\n"
"v_cvt_pk_bf16_f32 v130, v172, v173\n"
"v_mfma_f32_16x16x32_bf16 a[164:167], a[228:231], v[140:143], a[164:167]\n"
"v_exp_f32 v174, v214\n"
"v_mfma_f32_16x16x32_bf16 a[136:139], a[232:235], v[136:139], a[136:139]\n"
"ds_read_b64_tr_b16 a[226:227], v241 offset:8448\n"
"v_or3_b32 v184, v128, v129, v130\n"
"v_mfma_f32_16x16x32_bf16 a[168:171], a[232:235], v[140:143], a[168:171]\n"
"v_exp_f32 v175, v215\n"
"v_mfma_f32_16x16x32_bf16 a[140:143], a[236:239], v[136:139], a[140:143]\n"
"ds_read_b64_tr_b16 a[228:229], v242 offset:8192\n"
"v_cvt_pk_bf16_f32 v131, v174, v175\n"
"v_mfma_f32_16x16x32_bf16 a[172:175], a[236:239], v[140:143], a[172:175]\n"
"v_exp_f32 v172, v216\n"
"v_mfma_f32_16x16x32_bf16 a[144:147], a[240:243], v[136:139], a[144:147]\n"
"ds_read_b64_tr_b16 a[230:231], v242 offset:8448\n"
"ds_read_b64_tr_b16 a[232:233], v241 offset:8704\n"
"v_mfma_f32_16x16x32_bf16 a[176:179], a[240:243], v[140:143], a[176:179]\n"
"v_exp_f32 v173, v217\n"
"v_mfma_f32_16x16x32_bf16 a[148:151], a[244:247], v[136:139], a[148:151]\n"
"ds_read_b64_tr_b16 a[234:235], v241 offset:8960\n"
"v_cvt_pk_bf16_f32 v132, v172, v173\n"
"v_mfma_f32_16x16x32_bf16 a[180:183], a[244:247], v[140:143], a[180:183]\n"
"v_exp_f32 v174, v218\n"
"v_mfma_f32_16x16x32_bf16 a[152:155], a[248:251], v[136:139], a[152:155]\n"
"ds_read_b64_tr_b16 a[236:237], v242 offset:8704\n"
"v_or3_b32 v184, v184, v131, v132\n"
"v_mfma_f32_16x16x32_bf16 a[184:187], a[248:251], v[140:143], a[184:187]\n"
"v_exp_f32 v175, v219\n"
"v_mfma_f32_16x16x32_bf16 a[156:159], a[252:255], v[136:139], a[156:159]\n"
"ds_read_b64_tr_b16 a[238:239], v242 offset:8960\n"
"v_cvt_pk_bf16_f32 v133, v174, v175\n"
"v_mfma_f32_16x16x32_bf16 a[188:191], a[252:255], v[140:143], a[188:191]\n"
"v_exp_f32 v172, v220\n"
"v_mfma_f32_16x16x32_bf16 v[160:163], v[168:171], v[136:139], v[160:163]\n"
"ds_read_b64_tr_b16 a[240:241], v241 offset:9216\n"
"ds_read_b64_tr_b16 a[242:243], v241 offset:9472\n"
"v_mfma_f32_16x16x32_bf16 v[164:167], v[168:171], v[140:143], v[164:167]\n"
"v_exp_f32 v173, v221\n"
"v_mfma_f32_16x16x32_bf16 v[224:227], a[192:195], v[64:67], v[112:115]\n"
"ds_read_b64_tr_b16 a[244:245], v242 offset:9216\n"
"v_cvt_pk_bf16_f32 v134, v172, v173\n"
"v_mfma_f32_16x16x32_bf16 v[224:227], a[196:199], v[68:71], v[224:227]\n"
"v_exp_f32 v174, v222\n"
"v_mfma_f32_16x16x32_bf16 v[224:227], a[200:203], v[72:75], v[224:227]\n"
"ds_read_b64_tr_b16 a[246:247], v242 offset:9472\n"
"v_or3_b32 v184, v184, v133, v134\n"
"v_mfma_f32_16x16x32_bf16 v[224:227], a[204:207], v[76:79], v[224:227]\n"
"v_exp_f32 v175, v223\n"
"v_mfma_f32_16x16x32_bf16 v[228:231], a[208:211], v[64:67], v[112:115]\n"
"v_cvt_pk_bf16_f32 v135, v174, v175\n"
"v_bitop3_b32 v184, v184, v135, s60 bitop3:0xa8\n"
"v_mfma_f32_16x16x32_bf16 v[228:231], a[212:215], v[68:71], v[228:231]\n"
"ds_read_b64_tr_b16 a[248:249], v241 offset:9728\n"
"v_cmp_ne_u32 vcc, 0, v184\n"
"v_mfma_f32_16x16x32_bf16 v[228:231], a[216:219], v[72:75], v[228:231]\n"
"ds_read_b64_tr_b16 a[250:251], v241 offset:9984\n"
"ds_read_b64_tr_b16 a[252:253], v242 offset:9728\n"
"v_mfma_f32_16x16x32_bf16 v[228:231], a[220:223], v[76:79], v[228:231]\n"
"s_cbranch_vccnz .Lk1_tr11\n"
".Lk1_ct11:\n"
"v_exp_f32 v172, v224\n"
"v_mfma_f32_16x16x32_bf16 v[232:235], a[192:195], v[80:83], v[116:119]\n"
"ds_read_b64_tr_b16 a[254:255], v242 offset:9984\n"
"ds_read_b128 a[192:195], v240 offset:0\n"
"v_mfma_f32_16x16x32_bf16 v[232:235], a[196:199], v[84:87], v[232:235]\n"
"v_exp_f32 v173, v225\n"
"v_mfma_f32_16x16x32_bf16 v[232:235], a[200:203], v[88:91], v[232:235]\n"
"ds_read_b128 a[196:199], v240 offset:512\n"
"v_cvt_pk_bf16_f32 v136, v172, v173\n"
"v_mfma_f32_16x16x32_bf16 v[232:235], a[204:207], v[92:95], v[232:235]\n"
"v_exp_f32 v174, v226\n"
"v_mfma_f32_16x16x32_bf16 v[236:239], a[208:211], v[80:83], v[116:119]\n"
"ds_read_b128 a[200:203], v240 offset:1024\n"
"ds_read_b128 a[204:207], v240 offset:1536\n"
"v_mfma_f32_16x16x32_bf16 v[236:239], a[212:215], v[84:87], v[236:239]\n"
"v_exp_f32 v175, v227\n"
"v_mfma_f32_16x16x32_bf16 v[236:239], a[216:219], v[88:91], v[236:239]\n"
"ds_read_b128 a[208:211], v240 offset:256\n"
"v_cvt_pk_bf16_f32 v137, v174, v175\n"
"v_mfma_f32_16x16x32_bf16 v[236:239], a[220:223], v[92:95], v[236:239]\n"
"s_waitcnt lgkmcnt(13)\n"
"v_exp_f32 v172, v228\n"
"v_mfma_f32_16x16x32_bf16 a[0:3], a[224:227], v[120:123], a[0:3]\n"
"ds_read_b128 a[212:215], v240 offset:768\n"
"ds_read_b128 a[216:219], v240 offset:1280\n"
"v_mfma_f32_16x16x32_bf16 a[32:35], a[224:227], v[124:127], a[32:35]\n"
"v_exp_f32 v173, v229\n"
"v_mfma_f32_16x16x32_bf16 a[4:7], a[228:231], v[120:123], a[4:7]\n"
"ds_read_b128 a[220:223], v240 offset:1792\n"
"v_cvt_pk_bf16_f32 v138, v172, v173\n"
"v_mfma_f32_16x16x32_bf16 a[36:39], a[228:231], v[124:127], a[36:39]\n"
"v_exp_f32 v174, v230\n"
"v_mfma_f32_16x16x32_bf16 a[8:11], a[232:235], v[120:123], a[8:11]\n"
"s_cmp_lt_u32 s72, s81\n"
"s_cselect_b32 s58, s72, s59\n"
"v_or3_b32 v184, v136, v137, v138\n"
"v_mfma_f32_16x16x32_bf16 a[40:43], a[232:235], v[124:127], a[40:43]\n"
"v_exp_f32 v175, v231\n"
"v_mfma_f32_16x16x32_bf16 a[12:15], a[236:239], v[120:123], a[12:15]\n"
"s_cmp_lt_u32 s78, 0x18000\n"
"s_cselect_b32 s75, s79, s80\n"
"v_cvt_pk_bf16_f32 v139, v174, v175\n"
"v_mfma_f32_16x16x32_bf16 a[44:47], a[236:239], v[124:127], a[44:47]\n"
"s_waitcnt lgkmcnt(8)\n"
"v_exp_f32 v172, v232\n"
"v_mfma_f32_16x16x32_bf16 a[16:19], a[240:243], v[120:123], a[16:19]\n"
"s_add_u32 s78, s78, s75\n"
"v_mfma_f32_16x16x32_bf16 a[48:51], a[240:243], v[124:127], a[48:51]\n"
"v_exp_f32 v173, v233\n"
"v_mfma_f32_16x16x32_bf16 a[20:23], a[244:247], v[120:123], a[20:23]\n"
"v_cvt_pk_bf16_f32 v140, v172, v173\n"
"v_add_u32 v241, s75, v241\n"
"v_mfma_f32_16x16x32_bf16 a[52:55], a[244:247], v[124:127], a[52:55]\n"
"v_exp_f32 v174, v234\n"
"v_mfma_f32_16x16x32_bf16 a[24:27], a[248:251], v[120:123], a[24:27]\n"
"v_or3_b32 v184, v184, v139, v140\n"
"v_add_u32 v242, s75, v242\n"
"v_mfma_f32_16x16x32_bf16 a[56:59], a[248:251], v[124:127], a[56:59]\n"
"v_exp_f32 v175, v235\n"
"v_mfma_f32_16x16x32_bf16 a[28:31], a[252:255], v[120:123], a[28:31]\n"
"v_cvt_pk_bf16_f32 v141, v174, v175\n"
"v_mfma_f32_16x16x32_bf16 a[60:63], a[252:255], v[124:127], a[60:63]\n"
"v_exp_f32 v172, v236\n"
"v_mfma_f32_16x16x32_bf16 v[144:147], v[168:171], v[120:123], v[144:147]\n"
"v_mfma_f32_16x16x32_bf16 v[148:151], v[168:171], v[124:127], v[148:151]\n"
"s_waitcnt lgkmcnt(4)\n"
"v_exp_f32 v173, v237\n"
"v_mfma_f32_16x16x32_bf16 v[192:195], a[192:195], v[0:3], v[96:99]\n"
"v_cvt_pk_bf16_f32 v142, v172, v173\n"
"v_mfma_f32_16x16x32_bf16 v[192:195], a[196:199], v[4:7], v[192:195]\n"
"v_exp_f32 v174, v238\n"
"v_mfma_f32_16x16x32_bf16 v[192:195], a[200:203], v[8:11], v[192:195]\n"
"s_mov_b32 m0, s58\n"
"s_add_u32 s71, s57, 4096\n"
"v_or3_b32 v184, v184, v141, v142\n"
"v_mfma_f32_16x16x32_bf16 v[192:195], a[204:207], v[12:15], v[192:195]\n"
"s_waitcnt lgkmcnt(0)\n"
"v_exp_f32 v175, v239\n"
"v_mfma_f32_16x16x32_bf16 v[196:199], a[208:211], v[0:3], v[96:99]\n"
"v_cvt_pk_bf16_f32 v143, v174, v175\n"
"v_bitop3_b32 v184, v184, v143, s60 bitop3:0xa8\n"
"v_mfma_f32_16x16x32_bf16 v[196:199], a[212:215], v[4:7], v[196:199]\n"
"v_cmp_ne_u32 vcc, 0, v184\n"
"v_mfma_f32_16x16x32_bf16 v[196:199], a[216:219], v[8:11], v[196:199]\n"
"buffer_load_dwordx4 v246, s[36:39], s57 offen offset:0 nt lds\n"
"v_mfma_f32_16x16x32_bf16 v[196:199], a[220:223], v[12:15], v[196:199]\n"
"s_cbranch_vccnz .Lk1_tr13\n"
".Lk1_ct13:\n"
"v_exp_f32 v172, v192\n"
"v_mfma_f32_16x16x32_bf16 v[200:203], a[192:195], v[16:19], v[100:103]\n"
"v_mfma_f32_16x16x32_bf16 v[200:203], a[196:199], v[20:23], v[200:203]\n"
"v_exp_f32 v173, v193\n"
"v_mfma_f32_16x16x32_bf16 v[200:203], a[200:203], v[24:27], v[200:203]\n"
"v_cvt_pk_bf16_f32 v120, v172, v173\n"
"v_mfma_f32_16x16x32_bf16 v[200:203], a[204:207], v[28:31], v[200:203]\n"
"v_exp_f32 v174, v194\n"
"v_mfma_f32_16x16x32_bf16 v[204:207], a[208:211], v[16:19], v[100:103]\n"
"buffer_load_dwordx4 v247, s[36:39], s57 offen offset:1024 nt lds\n"
"v_mfma_f32_16x16x32_bf16 v[204:207], a[212:215], v[20:23], v[204:207]\n"
"v_exp_f32 v175, v195\n"
"v_mfma_f32_16x16x32_bf16 v[204:207], a[216:219], v[24:27], v[204:207]\n"
"v_cvt_pk_bf16_f32 v121, v174, v175\n"
"v_mfma_f32_16x16x32_bf16 v[204:207], a[220:223], v[28:31], v[204:207]\n"
"v_exp_f32 v172, v196\n"
"v_mfma_f32_16x16x32_bf16 a[64:67], a[224:227], v[128:131], a[64:67]\n"
"buffer_load_dwordx4 v248, s[36:39], s57 offen offset:2048 nt lds\n"
"v_mfma_f32_16x16x32_bf16 a[96:99], a[224:227], v[132:135], a[96:99]\n"
"v_exp_f32 v173, v197\n"
"v_mfma_f32_16x16x32_bf16 a[68:71], a[228:231], v[128:131], a[68:71]\n"
"v_cvt_pk_bf16_f32 v122, v172, v173\n"
"v_mfma_f32_16x16x32_bf16 a[100:103], a[228:231], v[132:135], a[100:103]\n"
"v_exp_f32 v174, v198\n"
"v_mfma_f32_16x16x32_bf16 a[72:75], a[232:235], v[128:131], a[72:75]\n"
"v_or3_b32 v184, v120, v121, v122\n"
"v_mfma_f32_16x16x32_bf16 a[104:107], a[232:235], v[132:135], a[104:107]\n"
"v_exp_f32 v175, v199\n"
"v_mfma_f32_16x16x32_bf16 a[76:79], a[236:239], v[128:131], a[76:79]\n"
"buffer_load_dwordx4 v249, s[36:39], s57 offen offset:3072 nt lds\n"
"v_cvt_pk_bf16_f32 v123, v174, v175\n"
"v_mfma_f32_16x16x32_bf16 a[108:111], a[236:239], v[132:135], a[108:111]\n"
"s_add_u32 m0, s58, 4096\n"
"v_exp_f32 v172, v200\n"
"v_mfma_f32_16x16x32_bf16 a[80:83], a[240:243], v[128:131], a[80:83]\n"
"s_add_u32 s57, s57, 16384\n"
"s_and_b32 s57, s57, 0x3ffff\n"
"v_mfma_f32_16x16x32_bf16 a[112:115], a[240:243], v[132:135], a[112:115]\n"
"v_exp_f32 v173, v201\n"
"v_mfma_f32_16x16x32_bf16 a[84:87], a[244:247], v[128:131], a[84:87]\n"
"buffer_load_dwordx4 v246, s[36:39], s71 offen offset:0 nt lds\n"
"v_cvt_pk_bf16_f32 v124, v172, v173\n"
"v_mfma_f32_16x16x32_bf16 a[116:119], a[244:247], v[132:135], a[116:119]\n"
"v_exp_f32 v174, v202\n"
"v_mfma_f32_16x16x32_bf16 a[88:91], a[248:251], v[128:131], a[88:91]\n"
"v_or3_b32 v184, v184, v123, v124\n"
"v_mfma_f32_16x16x32_bf16 a[120:123], a[248:251], v[132:135], a[120:123]\n"
"v_exp_f32 v175, v203\n"
"v_mfma_f32_16x16x32_bf16 a[92:95], a[252:255], v[128:131], a[92:95]\n"
"v_cvt_pk_bf16_f32 v125, v174, v175\n"
"v_mfma_f32_16x16x32_bf16 a[124:127], a[252:255], v[132:135], a[124:127]\n"
"v_exp_f32 v172, v204\n"
"v_mfma_f32_16x16x32_bf16 v[152:155], v[168:171], v[128:131], v[152:155]\n"
"buffer_load_dwordx4 v247, s[36:39], s71 offen offset:1024 nt lds\n"
"v_mfma_f32_16x16x32_bf16 v[156:159], v[168:171], v[132:135], v[156:159]\n"
"v_exp_f32 v173, v205\n"
"v_mfma_f32_16x16x32_bf16 v[208:211], a[192:195], v[32:35], v[104:107]\n"
"v_cvt_pk_bf16_f32 v126, v172, v173\n"
"v_mfma_f32_16x16x32_bf16 v[208:211], a[196:199], v[36:39], v[208:211]\n"
"s_memrealtime s[22:23]\n"
"v_exp_f32 v174, v206\n"
"v_mfma_f32_16x16x32_bf16 v[208:211], a[200:203], v[40:43], v[208:211]\n"
"buffer_load_dwordx4 v248, s[36:39], s71 offen offset:2048 nt lds\n"
"v_or3_b32 v184, v184, v125, v126\n"
"v_mfma_f32_16x16x32_bf16 v[208:211], a[204:207], v[44:47], v[208:211]\n"
"v_exp_f32 v175, v207\n"
"v_mfma_f32_16x16x32_bf16 v[212:215], a[208:211], v[32:35], v[104:107]\n"
"v_cvt_pk_bf16_f32 v127, v174, v175\n"
"v_bitop3_b32 v184, v184, v127, s60 bitop3:0xa8\n"
"v_mfma_f32_16x16x32_bf16 v[212:215], a[212:215], v[36:39], v[212:215]\n"
"v_cmp_ne_u32 vcc, 0, v184\n"
"v_mfma_f32_16x16x32_bf16 v[212:215], a[216:219], v[40:43], v[212:215]\n"
"v_mfma_f32_16x16x32_bf16 v[212:215], a[220:223], v[44:47], v[212:215]\n"
"s_cbranch_vccnz .Lk1_tr14\n"
".Lk1_ct14:\n"
"v_exp_f32 v172, v208\n"
"v_mfma_f32_16x16x32_bf16 v[216:219], a[192:195], v[48:51], v[108:111]\n"
"buffer_load_dwordx4 v249, s[36:39], s71 offen offset:3072 nt lds\n"
"v_mfma_f32_16x16x32_bf16 v[216:219], a[196:199], v[52:55], v[216:219]\n"
"s_add_u32 s72, s58, 0x8000\n"
"v_exp_f32 v173, v209\n"
"v_mfma_f32_16x16x32_bf16 v[216:219], a[200:203], v[56:59], v[216:219]\n"
"s_waitcnt lgkmcnt(0)\n"
"v_cvt_pk_bf16_f32 v128, v172, v173\n"
"v_mfma_f32_16x16x32_bf16 v[216:219], a[204:207], v[60:63], v[216:219]\n"
"s_sub_u32 s25, s22, s24\n"
"v_exp_f32 v174, v210\n"
"v_mfma_f32_16x16x32_bf16 v[220:223], a[208:211], v[48:51], v[108:111]\n"
"s_cmp_lt_i32 s25, 0\n"
"s_cbranch_scc1 .Lk1_tr12\n"
".Lk1_ct12:\n"
"v_mfma_f32_16x16x32_bf16 v[220:223], a[212:215], v[52:55], v[220:223]\n"
"v_exp_f32 v175, v211\n"
"v_mfma_f32_16x16x32_bf16 v[220:223], a[216:219], v[56:59], v[220:223]\n"
"s_waitcnt vmcnt(16)\n"
"v_cvt_pk_bf16_f32 v129, v174, v175\n"
"v_mfma_f32_16x16x32_bf16 v[220:223], a[220:223], v[60:63], v[220:223]\n"
"s_barrier\n"
"v_exp_f32 v172, v212\n"
"v_mfma_f32_16x16x32_bf16 a[128:131], a[224:227], v[136:139], a[128:131]\n"
"s_add_u32 s24, s22, 118\n"
"s_add_u32 s87, s87, 124\n"
"s_max_u32 s24, s24, s87\n"
"v_mfma_f32_16x16x32_bf16 a[160:163], a[224:227], v[140:143], a[160:163]\n"
"v_exp_f32 v173, v213\n"
"v_mfma_f32_16x16x32_bf16 a[132:135], a[228:231], v[136:139], a[132:135]\n"
"ds_read_b64_tr_b16 a[224:225], v241 offset:0\n"
"v_cvt_pk_bf16_f32 v130, v172, v173\n"
"v_mfma_f32_16x16x32_bf16 a[164:167], a[228:231], v[140:143], a[164:167]\n"
"v_exp_f32 v174, v214\n"
"v_mfma_f32_16x16x32_bf16 a[136:139], a[232:235], v[136:139], a[136:139]\n"
"ds_read_b64_tr_b16 a[226:227], v241 offset:256\n"
"v_or3_b32 v184, v128, v129, v130\n"
"v_mfma_f32_16x16x32_bf16 a[168:171], a[232:235], v[140:143], a[168:171]\n"
"v_exp_f32 v175, v215\n"
"v_mfma_f32_16x16x32_bf16 a[140:143], a[236:239], v[136:139], a[140:143]\n"
"ds_read_b64_tr_b16 a[228:229], v242 offset:0\n"
"v_cvt_pk_bf16_f32 v131, v174, v175\n"
"v_mfma_f32_16x16x32_bf16 a[172:175], a[236:239], v[140:143], a[172:175]\n"
"v_exp_f32 v172, v216\n"
"v_mfma_f32_16x16x32_bf16 a[144:147], a[240:243], v[136:139], a[144:147]\n"
"ds_read_b64_tr_b16 a[230:231], v242 offset:256\n"
"ds_read_b64_tr_b16 a[232:233], v241 offset:512\n"
"v_mfma_f32_16x16x32_bf16 a[176:179], a[240:243], v[140:143], a[176:179]\n"
"v_exp_f32 v173, v217\n"
"v_mfma_f32_16x16x32_bf16 a[148:151], a[244:247], v[136:139], a[148:151]\n"
"ds_read_b64_tr_b16 a[234:235], v241 offset:768\n"
"v_cvt_pk_bf16_f32 v132, v172, v173\n"
"v_mfma_f32_16x16x32_bf16 a[180:183], a[244:247], v[140:143], a[180:183]\n"
"v_exp_f32 v174, v218\n"
"v_mfma_f32_16x16x32_bf16 a[152:155], a[248:251], v[136:139], a[152:155]\n"
"ds_read_b64_tr_b16 a[236:237], v242 offset:512\n"
"v_or3_b32 v184, v184, v131, v132\n"
"v_mfma_f32_16x16x32_bf16 a[184:187], a[248:251], v[140:143], a[184:187]\n"
"v_exp_f32 v175, v219\n"
"v_mfma_f32_16x16x32_bf16 a[156:159], a[252:255], v[136:139], a[156:159]\n"
"ds_read_b64_tr_b16 a[238:239], v242 offset:768\n"
"v_cvt_pk_bf16_f32 v133, v174, v175\n"
"v_mfma_f32_16x16x32_bf16 a[188:191], a[252:255], v[140:143], a[188:191]\n"
"v_exp_f32 v172, v220\n"
"v_mfma_f32_16x16x32_bf16 v[160:163], v[168:171], v[136:139], v[160:163]\n"
"ds_read_b64_tr_b16 a[240:241], v241 offset:1024\n"
"ds_read_b64_tr_b16 a[242:243], v241 offset:1280\n"
"v_mfma_f32_16x16x32_bf16 v[164:167], v[168:171], v[140:143], v[164:167]\n"
"v_exp_f32 v173, v221\n"
"v_mfma_f32_16x16x32_bf16 v[224:227], a[192:195], v[64:67], v[112:115]\n"
"ds_read_b64_tr_b16 a[244:245], v242 offset:1024\n"
"v_cvt_pk_bf16_f32 v134, v172, v173\n"
"v_mfma_f32_16x16x32_bf16 v[224:227], a[196:199], v[68:71], v[224:227]\n"
"v_exp_f32 v174, v222\n"
"v_mfma_f32_16x16x32_bf16 v[224:227], a[200:203], v[72:75], v[224:227]\n"
"ds_read_b64_tr_b16 a[246:247], v242 offset:1280\n"
"v_or3_b32 v184, v184, v133, v134\n"
"v_mfma_f32_16x16x32_bf16 v[224:227], a[204:207], v[76:79], v[224:227]\n"
"v_exp_f32 v175, v223\n"
"v_mfma_f32_16x16x32_bf16 v[228:231], a[208:211], v[64:67], v[112:115]\n"
"v_cvt_pk_bf16_f32 v135, v174, v175\n"
"v_bitop3_b32 v184, v184, v135, s60 bitop3:0xa8\n"
"v_mfma_f32_16x16x32_bf16 v[228:231], a[212:215], v[68:71], v[228:231]\n"
"ds_read_b64_tr_b16 a[248:249], v241 offset:1536\n"
"v_cmp_ne_u32 vcc, 0, v184\n"
"v_mfma_f32_16x16x32_bf16 v[228:231], a[216:219], v[72:75], v[228:231]\n"
"ds_read_b64_tr_b16 a[250:251], v241 offset:1792\n"
"ds_read_b64_tr_b16 a[252:253], v242 offset:1536\n"
"v_mfma_f32_16x16x32_bf16 v[228:231], a[220:223], v[76:79], v[228:231]\n"
"s_cbranch_vccnz .Lk1_tr15\n"
".Lk1_ct15:\n"
"v_exp_f32 v172, v224\n"
"v_mfma_f32_16x16x32_bf16 v[232:235], a[192:195], v[80:83], v[116:119]\n"
"ds_read_b64_tr_b16 a[254:255], v242 offset:1792\n"
"ds_read_b128 a[192:195], v240 offset:8192\n"
"v_mfma_f32_16x16x32_bf16 v[232:235], a[196:199], v[84:87], v[232:235]\n"
"v_exp_f32 v173, v225\n"
"v_mfma_f32_16x16x32_bf16 v[232:235], a[200:203], v[88:91], v[232:235]\n"
"ds_read_b128 a[196:199], v240 offset:8704\n"
"v_cvt_pk_bf16_f32 v136, v172, v173\n"
"v_mfma_f32_16x16x32_bf16 v[232:235], a[204:207], v[92:95], v[232:235]\n"
"v_exp_f32 v174, v226\n"
"v_mfma_f32_16x16x32_bf16 v[236:239], a[208:211], v[80:83], v[116:119]\n"
"ds_read_b128 a[200:203], v240 offset:9216\n"
"ds_read_b128 a[204:207], v240 offset:9728\n"
"v_mfma_f32_16x16x32_bf16 v[236:239], a[212:215], v[84:87], v[236:239]\n"
"v_exp_f32 v175, v227\n"
"v_mfma_f32_16x16x32_bf16 v[236:239], a[216:219], v[88:91], v[236:239]\n"
"ds_read_b128 a[208:211], v240 offset:8448\n"
"v_cvt_pk_bf16_f32 v137, v174, v175\n"
"v_mfma_f32_16x16x32_bf16 v[236:239], a[220:223], v[92:95], v[236:239]\n"
"s_waitcnt lgkmcnt(13)\n"
"v_exp_f32 v172, v228\n"
"v_mfma_f32_16x16x32_bf16 a[0:3], a[224:227], v[120:123], a[0:3]\n"
"ds_read_b128 a[212:215], v240 offset:8960\n"
"ds_read_b128 a[216:219], v240 offset:9472\n"
"v_mfma_f32_16x16x32_bf16 a[32:35], a[224:227], v[124:127], a[32:35]\n"
"v_exp_f32 v173, v229\n"
"v_mfma_f32_16x16x32_bf16 a[4:7], a[228:231], v[120:123], a[4:7]\n"
"ds_read_b128 a[220:223], v240 offset:9984\n"
"v_cvt_pk_bf16_f32 v138, v172, v173\n"
"v_mfma_f32_16x16x32_bf16 a[36:39], a[228:231], v[124:127], a[36:39]\n"
"v_exp_f32 v174, v230\n"
"v_mfma_f32_16x16x32_bf16 a[8:11], a[232:235], v[120:123], a[8:11]\n"
"s_cmp_lt_u32 s72, s81\n"
"s_cselect_b32 s58, s72, s59\n"
"v_or3_b32 v184, v136, v137, v138\n"
"v_mfma_f32_16x16x32_bf16 a[40:43], a[232:235], v[124:127], a[40:43]\n"
"v_exp_f32 v175, v231\n"
"v_mfma_f32_16x16x32_bf16 a[12:15], a[236:239], v[120:123], a[12:15]\n"
"v_cvt_pk_bf16_f32 v139, v174, v175\n"
"v_mfma_f32_16x16x32_bf16 a[44:47], a[236:239], v[124:127], a[44:47]\n"
"s_waitcnt lgkmcnt(8)\n"
"v_exp_f32 v172, v232\n"
"v_mfma_f32_16x16x32_bf16 a[16:19], a[240:243], v[120:123], a[16:19]\n"
"v_mfma_f32_16x16x32_bf16 a[48:51], a[240:243], v[124:127], a[48:51]\n"
"v_exp_f32 v173, v233\n"
"v_mfma_f32_16x16x32_bf16 a[20:23], a[244:247], v[120:123], a[20:23]\n"
"v_cvt_pk_bf16_f32 v140, v172, v173\n"
"v_mfma_f32_16x16x32_bf16 a[52:55], a[244:247], v[124:127], a[52:55]\n"
"v_exp_f32 v174, v234\n"
"v_mfma_f32_16x16x32_bf16 a[24:27], a[248:251], v[120:123], a[24:27]\n"
"v_or3_b32 v184, v184, v139, v140\n"
"v_mfma_f32_16x16x32_bf16 a[56:59], a[248:251], v[124:127], a[56:59]\n"
"v_exp_f32 v175, v235\n"
"v_mfma_f32_16x16x32_bf16 a[28:31], a[252:255], v[120:123], a[28:31]\n"
"v_cvt_pk_bf16_f32 v141, v174, v175\n"
"v_mfma_f32_16x16x32_bf16 a[60:63], a[252:255], v[124:127], a[60:63]\n"
"v_exp_f32 v172, v236\n"
"v_mfma_f32_16x16x32_bf16 v[144:147], v[168:171], v[120:123], v[144:147]\n"
"s_sub_u32 s61, s61, 1\n"
"s_cbranch_scc0 .Lk1_loop\n"
"v_mfma_f32_16x16x32_bf16 v[148:151], v[168:171], v[124:127], v[148:151]\n"
"s_waitcnt lgkmcnt(4)\n"
"v_exp_f32 v173, v237\n"
"v_mfma_f32_16x16x32_bf16 v[192:195], a[192:195], v[0:3], v[96:99]\n"
"v_cvt_pk_bf16_f32 v142, v172, v173\n"
"v_mfma_f32_16x16x32_bf16 v[192:195], a[196:199], v[4:7], v[192:195]\n"
"v_exp_f32 v174, v238\n"
"v_mfma_f32_16x16x32_bf16 v[192:195], a[200:203], v[8:11], v[192:195]\n"
"s_mov_b32 m0, s58\n"
"s_add_u32 s71, s56, 4096\n"
"v_or3_b32 v184, v184, v141, v142\n"
"v_mfma_f32_16x16x32_bf16 v[192:195], a[204:207], v[12:15], v[192:195]\n"
"s_waitcnt lgkmcnt(0)\n"
"v_exp_f32 v175, v239\n"
"v_mfma_f32_16x16x32_bf16 v[196:199], a[208:211], v[0:3], v[96:99]\n"
"v_cvt_pk_bf16_f32 v143, v174, v175\n"
"v_bitop3_b32 v184, v184, v143, s60 bitop3:0xa8\n"
"v_mfma_f32_16x16x32_bf16 v[196:199], a[212:215], v[4:7], v[196:199]\n"
"buffer_load_dword v186, v185, s[52:55], s86 offen sc1\n"
"v_cmp_ne_u32 vcc, 0, v184\n"
"v_mfma_f32_16x16x32_bf16 v[196:199], a[216:219], v[8:11], v[196:199]\n"
"buffer_load_dwordx4 v246, s[28:31], s56 offen offset:0 nt lds\n"
"v_mfma_f32_16x16x32_bf16 v[196:199], a[220:223], v[12:15], v[196:199]\n"
"s_cbranch_vccnz .Lk1_tr105\n"
".Lk1_ct105:\n"
"v_exp_f32 v172, v192\n"
"v_mfma_f32_16x16x32_bf16 v[200:203], a[192:195], v[16:19], v[100:103]\n"
"s_cmp_lt_u32 s77, 0x18000\n"
"s_cselect_b32 s75, s79, s80\n"
"s_add_u32 s77, s77, s75\n"
"v_mfma_f32_16x16x32_bf16 v[200:203], a[196:199], v[20:23], v[200:203]\n"
"v_exp_f32 v173, v193\n"
"v_mfma_f32_16x16x32_bf16 v[200:203], a[200:203], v[24:27], v[200:203]\n"
"v_cvt_pk_bf16_f32 v120, v172, v173\n"
"v_add_u32 v240, s75, v240\n"
"v_mfma_f32_16x16x32_bf16 v[200:203], a[204:207], v[28:31], v[200:203]\n"
"v_exp_f32 v174, v194\n"
"v_mfma_f32_16x16x32_bf16 v[204:207], a[208:211], v[16:19], v[100:103]\n"
"buffer_load_dwordx4 v247, s[28:31], s56 offen offset:1024 nt lds\n"
"v_mov_b32 v190, v250\n"
"v_mfma_f32_16x16x32_bf16 v[204:207], a[212:215], v[20:23], v[204:207]\n"
"v_exp_f32 v175, v195\n"
"v_mfma_f32_16x16x32_bf16 v[204:207], a[216:219], v[24:27], v[204:207]\n"
"v_cvt_pk_bf16_f32 v121, v174, v175\n"
"v_mfma_f32_16x16x32_bf16 v[204:207], a[220:223], v[28:31], v[204:207]\n"
"v_exp_f32 v172, v196\n"
"v_mfma_f32_16x16x32_bf16 a[64:67], a[224:227], v[128:131], a[64:67]\n"
"buffer_load_dwordx4 v248, s[28:31], s56 offen offset:2048 nt lds\n"
"v_mfma_f32_16x16x32_bf16 a[96:99], a[224:227], v[132:135], a[96:99]\n"
"v_exp_f32 v173, v197\n"
"v_mfma_f32_16x16x32_bf16 a[68:71], a[228:231], v[128:131], a[68:71]\n"
"v_cvt_pk_bf16_f32 v122, v172, v173\n"
"v_mfma_f32_16x16x32_bf16 a[100:103], a[228:231], v[132:135], a[100:103]\n"
"v_exp_f32 v174, v198\n"
"v_mfma_f32_16x16x32_bf16 a[72:75], a[232:235], v[128:131], a[72:75]\n"
"v_or3_b32 v184, v120, v121, v122\n"
"v_mfma_f32_16x16x32_bf16 a[104:107], a[232:235], v[132:135], a[104:107]\n"
"v_exp_f32 v175, v199\n"
"v_mfma_f32_16x16x32_bf16 a[76:79], a[236:239], v[128:131], a[76:79]\n"
"buffer_load_dwordx4 v249, s[28:31], s56 offen offset:3072 nt lds\n"
"v_cvt_pk_bf16_f32 v123, v174, v175\n"
"v_mfma_f32_16x16x32_bf16 a[108:111], a[236:239], v[132:135], a[108:111]\n"
"s_add_u32 m0, s58, 4096\n"
"v_exp_f32 v172, v200\n"
"v_mfma_f32_16x16x32_bf16 a[80:83], a[240:243], v[128:131], a[80:83]\n"
"s_add_u32 s56, s56, 16384\n"
"s_and_b32 s56, s56, 0x3ffff\n"
"v_mfma_f32_16x16x32_bf16 a[112:115], a[240:243], v[132:135], a[112:115]\n"
"v_exp_f32 v173, v201\n"
"v_mfma_f32_16x16x32_bf16 a[84:87], a[244:247], v[128:131], a[84:87]\n"
"buffer_load_dwordx4 v246, s[28:31], s71 offen offset:0 nt lds\n"
"v_cvt_pk_bf16_f32 v124, v172, v173\n"
"v_mfma_f32_16x16x32_bf16 a[116:119], a[244:247], v[132:135], a[116:119]\n"
"v_exp_f32 v174, v202\n"
"v_mfma_f32_16x16x32_bf16 a[88:91], a[248:251], v[128:131], a[88:91]\n"
"v_or3_b32 v184, v184, v123, v124\n"
"v_mfma_f32_16x16x32_bf16 a[120:123], a[248:251], v[132:135], a[120:123]\n"
"v_exp_f32 v175, v203\n"
"v_mfma_f32_16x16x32_bf16 a[92:95], a[252:255], v[128:131], a[92:95]\n"
"v_cvt_pk_bf16_f32 v125, v174, v175\n"
"v_mfma_f32_16x16x32_bf16 a[124:127], a[252:255], v[132:135], a[124:127]\n"
"v_exp_f32 v172, v204\n"
"v_mfma_f32_16x16x32_bf16 v[152:155], v[168:171], v[128:131], v[152:155]\n"
"buffer_load_dwordx4 v247, s[28:31], s71 offen offset:1024 nt lds\n"
"v_mfma_f32_16x16x32_bf16 v[156:159], v[168:171], v[132:135], v[156:159]\n"
"v_exp_f32 v173, v205\n"
"v_mfma_f32_16x16x32_bf16 v[208:211], a[192:195], v[32:35], v[104:107]\n"
"v_cvt_pk_bf16_f32 v126, v172, v173\n"
"v_mfma_f32_16x16x32_bf16 v[208:211], a[196:199], v[36:39], v[208:211]\n"
"s_memrealtime s[22:23]\n"
"v_exp_f32 v174, v206\n"
"v_mfma_f32_16x16x32_bf16 v[208:211], a[200:203], v[40:43], v[208:211]\n"
"buffer_load_dwordx4 v248, s[28:31], s71 offen offset:2048 nt lds\n"
"v_or3_b32 v184, v184, v125, v126\n"
"v_mfma_f32_16x16x32_bf16 v[208:211], a[204:207], v[44:47], v[208:211]\n"
"v_exp_f32 v175, v207\n"
"v_mfma_f32_16x16x32_bf16 v[212:215], a[208:211], v[32:35], v[104:107]\n"
"v_cvt_pk_bf16_f32 v127, v174, v175\n"
"v_bitop3_b32 v184, v184, v127, s60 bitop3:0xa8\n"
"v_mfma_f32_16x16x32_bf16 v[212:215], a[212:215], v[36:39], v[212:215]\n"
"v_cmp_ne_u32 vcc, 0, v184\n"
"v_mfma_f32_16x16x32_bf16 v[212:215], a[216:219], v[40:43], v[212:215]\n"
"v_mfma_f32_16x16x32_bf16 v[212:215], a[220:223], v[44:47], v[212:215]\n"
"s_cbranch_vccnz .Lk1_tr106\n"
".Lk1_ct106:\n"
"v_exp_f32 v172, v208\n"
"v_mfma_f32_16x16x32_bf16 v[216:219], a[192:195], v[48:51], v[108:111]\n"
"buffer_load_dwordx4 v249, s[28:31], s71 offen offset:3072 nt lds\n"
"v_mfma_f32_16x16x32_bf16 v[216:219], a[196:199], v[52:55], v[216:219]\n"
"s_add_u32 s72, s58, 0x8000\n"
"v_exp_f32 v173, v209\n"
"v_mfma_f32_16x16x32_bf16 v[216:219], a[200:203], v[56:59], v[216:219]\n"
"s_waitcnt lgkmcnt(0)\n"
"v_cvt_pk_bf16_f32 v128, v172, v173\n"
"v_mfma_f32_16x16x32_bf16 v[216:219], a[204:207], v[60:63], v[216:219]\n"
"s_sub_u32 s25, s22, s24\n"
"v_exp_f32 v174, v210\n"
"v_mfma_f32_16x16x32_bf16 v[220:223], a[208:211], v[48:51], v[108:111]\n"
"s_cmp_lt_i32 s25, 0\n"
"s_cbranch_scc1 .Lk1_tr104\n"
".Lk1_ct104:\n"
"v_mfma_f32_16x16x32_bf16 v[220:223], a[212:215], v[52:55], v[220:223]\n"
"v_exp_f32 v175, v211\n"
"v_mfma_f32_16x16x32_bf16 v[220:223], a[216:219], v[56:59], v[220:223]\n"
"s_waitcnt vmcnt(17)\n"
"v_cvt_pk_bf16_f32 v129, v174, v175\n"
"v_mfma_f32_16x16x32_bf16 v[220:223], a[220:223], v[60:63], v[220:223]\n"
"s_barrier\n"
"v_exp_f32 v172, v212\n"
"v_mfma_f32_16x16x32_bf16 a[128:131], a[224:227], v[136:139], a[128:131]\n"
"s_add_u32 s24, s22, 118\n"
"s_add_u32 s87, s87, 124\n"
"s_max_u32 s24, s24, s87\n"
"v_mfma_f32_16x16x32_bf16 a[160:163], a[224:227], v[140:143], a[160:163]\n"
"v_exp_f32 v173, v213\n"
"v_mfma_f32_16x16x32_bf16 a[132:135], a[228:231], v[136:139], a[132:135]\n"
"ds_read_b64_tr_b16 a[224:225], v241 offset:8192\n"
"v_cvt_pk_bf16_f32 v130, v172, v173\n"
"v_mfma_f32_16x16x32_bf16 a[164:167], a[228:231], v[140:143], a[164:167]\n"
"v_exp_f32 v174, v214\n"
"v_mfma_f32_16x16x32_bf16 a[136:139], a[232:235], v[136:139], a[136:139]\n"
"ds_read_b64_tr_b16 a[226:227], v241 offset:8448\n"
"v_or3_b32 v184, v128, v129, v130\n"
"v_mfma_f32_16x16x32_bf16 a[168:171], a[232:235], v[140:143], a[168:171]\n"
"v_exp_f32 v175, v215\n"
"v_mfma_f32_16x16x32_bf16 a[140:143], a[236:239], v[136:139], a[140:143]\n"
"ds_read_b64_tr_b16 a[228:229], v242 offset:8192\n"
"v_cvt_pk_bf16_f32 v131, v174, v175\n"
"v_mfma_f32_16x16x32_bf16 a[172:175], a[236:239], v[140:143], a[172:175]\n"
"v_exp_f32 v172, v216\n"
"v_mfma_f32_16x16x32_bf16 a[144:147], a[240:243], v[136:139], a[144:147]\n"
"ds_read_b64_tr_b16 a[230:231], v242 offset:8448\n"
"ds_read_b64_tr_b16 a[232:233], v241 offset:8704\n"
"v_mfma_f32_16x16x32_bf16 a[176:179], a[240:243], v[140:143], a[176:179]\n"
"v_exp_f32 v173, v217\n"
"v_mfma_f32_16x16x32_bf16 a[148:151], a[244:247], v[136:139], a[148:151]\n"
"ds_read_b64_tr_b16 a[234:235], v241 offset:8960\n"
"v_cvt_pk_bf16_f32 v132, v172, v173\n"
"v_mfma_f32_16x16x32_bf16 a[180:183], a[244:247], v[140:143], a[180:183]\n"
"v_exp_f32 v174, v218\n"
"v_mfma_f32_16x16x32_bf16 a[152:155], a[248:251], v[136:139], a[152:155]\n"
"ds_read_b64_tr_b16 a[236:237], v242 offset:8704\n"
"v_or3_b32 v184, v184, v131, v132\n"
"v_mfma_f32_16x16x32_bf16 a[184:187], a[248:251], v[140:143], a[184:187]\n"
"v_exp_f32 v175, v219\n"
"v_mfma_f32_16x16x32_bf16 a[156:159], a[252:255], v[136:139], a[156:159]\n"
"ds_read_b64_tr_b16 a[238:239], v242 offset:8960\n"
"v_cvt_pk_bf16_f32 v133, v174, v175\n"
"v_mfma_f32_16x16x32_bf16 a[188:191], a[252:255], v[140:143], a[188:191]\n"
"v_exp_f32 v172, v220\n"
"v_mfma_f32_16x16x32_bf16 v[160:163], v[168:171], v[136:139], v[160:163]\n"
"ds_read_b64_tr_b16 a[240:241], v241 offset:9216\n"
"ds_read_b64_tr_b16 a[242:243], v241 offset:9472\n"
"v_mfma_f32_16x16x32_bf16 v[164:167], v[168:171], v[140:143], v[164:167]\n"
"v_exp_f32 v173, v221\n"
"v_mfma_f32_16x16x32_bf16 v[224:227], a[192:195], v[64:67], v[112:115]\n"
"ds_read_b64_tr_b16 a[244:245], v242 offset:9216\n"
"v_cvt_pk_bf16_f32 v134, v172, v173\n"
"v_mfma_f32_16x16x32_bf16 v[224:227], a[196:199], v[68:71], v[224:227]\n"
"v_exp_f32 v174, v222\n"
"v_mfma_f32_16x16x32_bf16 v[224:227], a[200:203], v[72:75], v[224:227]\n"
"ds_read_b64_tr_b16 a[246:247], v242 offset:9472\n"
"v_or3_b32 v184, v184, v133, v134\n"
"v_mfma_f32_16x16x32_bf16 v[224:227], a[204:207], v[76:79], v[224:227]\n"
"v_exp_f32 v175, v223\n"
"v_mfma_f32_16x16x32_bf16 v[228:231], a[208:211], v[64:67], v[112:115]\n"
"v_cvt_pk_bf16_f32 v135, v174, v175\n"
"v_bitop3_b32 v184, v184, v135, s60 bitop3:0xa8\n"
"v_mfma_f32_16x16x32_bf16 v[228:231], a[212:215], v[68:71], v[228:231]\n"
"ds_read_b64_tr_b16 a[248:249], v241 offset:9728\n"
"v_cmp_ne_u32 vcc, 0, v184\n"
"v_mfma_f32_16x16x32_bf16 v[228:231], a[216:219], v[72:75], v[228:231]\n"
"ds_read_b64_tr_b16 a[250:251], v241 offset:9984\n"
"ds_read_b64_tr_b16 a[252:253], v242 offset:9728\n"
"v_mfma_f32_16x16x32_bf16 v[228:231], a[220:223], v[76:79], v[228:231]\n"
"s_cbranch_vccnz .Lk1_tr107\n"
".Lk1_ct107:\n"
"v_exp_f32 v172, v224\n"
"v_mfma_f32_16x16x32_bf16 v[232:235], a[192:195], v[80:83], v[116:119]\n"
"ds_read_b64_tr_b16 a[254:255], v242 offset:9984\n"
"ds_read_b128 a[192:195], v240 offset:0\n"
"v_mfma_f32_16x16x32_bf16 v[232:235], a[196:199], v[84:87], v[232:235]\n"
"v_exp_f32 v173, v225\n"
"v_mfma_f32_16x16x32_bf16 v[232:235], a[200:203], v[88:91], v[232:235]\n"
"ds_read_b128 a[196:199], v240 offset:512\n"
"v_cvt_pk_bf16_f32 v136, v172, v173\n"
"v_mfma_f32_16x16x32_bf16 v[232:235], a[204:207], v[92:95], v[232:235]\n"
"v_exp_f32 v174, v226\n"
"v_mfma_f32_16x16x32_bf16 v[236:239], a[208:211], v[80:83], v[116:119]\n"
"ds_read_b128 a[200:203], v240 offset:1024\n"
"ds_read_b128 a[204:207], v240 offset:1536\n"
"v_mfma_f32_16x16x32_bf16 v[236:239], a[212:215], v[84:87], v[236:239]\n"
"v_exp_f32 v175, v227\n"
"v_mfma_f32_16x16x32_bf16 v[236:239], a[216:219], v[88:91], v[236:239]\n"
"ds_read_b128 a[208:211], v240 offset:256\n"
"v_cvt_pk_bf16_f32 v137, v174, v175\n"
"v_mfma_f32_16x16x32_bf16 v[236:239], a[220:223], v[92:95], v[236:239]\n"
"s_waitcnt lgkmcnt(13)\n"
"v_exp_f32 v172, v228\n"
"v_mfma_f32_16x16x32_bf16 a[0:3], a[224:227], v[120:123], a[0:3]\n"
"ds_read_b128 a[212:215], v240 offset:768\n"
"ds_read_b128 a[216:219], v240 offset:1280\n"
"v_mfma_f32_16x16x32_bf16 a[32:35], a[224:227], v[124:127], a[32:35]\n"
"v_exp_f32 v173, v229\n"
"v_mfma_f32_16x16x32_bf16 a[4:7], a[228:231], v[120:123], a[4:7]\n"
"ds_read_b128 a[220:223], v240 offset:1792\n"
"v_cvt_pk_bf16_f32 v138, v172, v173\n"
"v_mfma_f32_16x16x32_bf16 a[36:39], a[228:231], v[124:127], a[36:39]\n"
"v_exp_f32 v174, v230\n"
"v_mfma_f32_16x16x32_bf16 a[8:11], a[232:235], v[120:123], a[8:11]\n"
"s_cmp_lt_u32 s72, s81\n"
"s_cselect_b32 s58, s72, s59\n"
"v_or3_b32 v184, v136, v137, v138\n"
"v_mfma_f32_16x16x32_bf16 a[40:43], a[232:235], v[124:127], a[40:43]\n"
"v_exp_f32 v175, v231\n"
"v_mfma_f32_16x16x32_bf16 a[12:15], a[236:239], v[120:123], a[12:15]\n"
"s_cmp_lt_u32 s78, 0x18000\n"
"s_cselect_b32 s75, s79, s80\n"
"v_cvt_pk_bf16_f32 v139, v174, v175\n"
"v_mfma_f32_16x16x32_bf16 a[44:47], a[236:239], v[124:127], a[44:47]\n"
"s_waitcnt lgkmcnt(8)\n"
"v_exp_f32 v172, v232\n"
"v_mfma_f32_16x16x32_bf16 a[16:19], a[240:243], v[120:123], a[16:19]\n"
"s_add_u32 s78, s78, s75\n"
"v_mfma_f32_16x16x32_bf16 a[48:51], a[240:243], v[124:127], a[48:51]\n"
"v_exp_f32 v173, v233\n"
"v_mfma_f32_16x16x32_bf16 a[20:23], a[244:247], v[120:123], a[20:23]\n"
"v_cvt_pk_bf16_f32 v140, v172, v173\n"
"v_add_u32 v241, s75, v241\n"
"v_mfma_f32_16x16x32_bf16 a[52:55], a[244:247], v[124:127], a[52:55]\n"
"v_exp_f32 v174, v234\n"
"v_mfma_f32_16x16x32_bf16 a[24:27], a[248:251], v[120:123], a[24:27]\n"
"v_or3_b32 v184, v184, v139, v140\n"
"v_add_u32 v242, s75, v242\n"
"v_mfma_f32_16x16x32_bf16 a[56:59], a[248:251], v[124:127], a[56:59]\n"
"v_exp_f32 v175, v235\n"
"v_mfma_f32_16x16x32_bf16 a[28:31], a[252:255], v[120:123], a[28:31]\n"
"v_cvt_pk_bf16_f32 v141, v174, v175\n"
"v_mfma_f32_16x16x32_bf16 a[60:63], a[252:255], v[124:127], a[60:63]\n"
"v_exp_f32 v172, v236\n"
"v_mfma_f32_16x16x32_bf16 v[144:147], v[168:171], v[120:123], v[144:147]\n"
"v_mfma_f32_16x16x32_bf16 v[148:151], v[168:171], v[124:127], v[148:151]\n"
"s_waitcnt lgkmcnt(4)\n"
"v_exp_f32 v173, v237\n"
"v_mfma_f32_16x16x32_bf16 v[192:195], a[192:195], v[0:3], v[96:99]\n"
"v_cvt_pk_bf16_f32 v142, v172, v173\n"
"v_mfma_f32_16x16x32_bf16 v[192:195], a[196:199], v[4:7], v[192:195]\n"
"v_exp_f32 v174, v238\n"
"v_mfma_f32_16x16x32_bf16 v[192:195], a[200:203], v[8:11], v[192:195]\n"
"s_mov_b32 m0, s58\n"
"s_add_u32 s71, s57, 4096\n"
"v_or3_b32 v184, v184, v141, v142\n"
"v_mfma_f32_16x16x32_bf16 v[192:195], a[204:207], v[12:15], v[192:195]\n"
"s_waitcnt lgkmcnt(0)\n"
"v_exp_f32 v175, v239\n"
"v_mfma_f32_16x16x32_bf16 v[196:199], a[208:211], v[0:3], v[96:99]\n"
"v_cvt_pk_bf16_f32 v143, v174, v175\n"
"v_bitop3_b32 v184, v184, v143, s60 bitop3:0xa8\n"
"v_mfma_f32_16x16x32_bf16 v[196:199], a[212:215], v[4:7], v[196:199]\n"
"v_cmp_ne_u32 vcc, 0, v184\n"
"v_mfma_f32_16x16x32_bf16 v[196:199], a[216:219], v[8:11], v[196:199]\n"
"buffer_load_dwordx4 v246, s[36:39], s57 offen offset:0 nt lds\n"
"v_mfma_f32_16x16x32_bf16 v[196:199], a[220:223], v[12:15], v[196:199]\n"
"s_cbranch_vccnz .Lk1_tr109\n"
".Lk1_ct109:\n"
"v_exp_f32 v172, v192\n"
"v_mfma_f32_16x16x32_bf16 v[200:203], a[192:195], v[16:19], v[100:103]\n"
"v_mfma_f32_16x16x32_bf16 v[200:203], a[196:199], v[20:23], v[200:203]\n"
"v_exp_f32 v173, v193\n"
"v_mfma_f32_16x16x32_bf16 v[200:203], a[200:203], v[24:27], v[200:203]\n"
"v_cvt_pk_bf16_f32 v120, v172, v173\n"
"v_mfma_f32_16x16x32_bf16 v[200:203], a[204:207], v[28:31], v[200:203]\n"
"v_exp_f32 v174, v194\n"
"v_mfma_f32_16x16x32_bf16 v[204:207], a[208:211], v[16:19], v[100:103]\n"
"buffer_load_dwordx4 v247, s[36:39], s57 offen offset:1024 nt lds\n"
"v_mfma_f32_16x16x32_bf16 v[204:207], a[212:215], v[20:23], v[204:207]\n"
"v_exp_f32 v175, v195\n"
"v_mfma_f32_16x16x32_bf16 v[204:207], a[216:219], v[24:27], v[204:207]\n"
"v_cvt_pk_bf16_f32 v121, v174, v175\n"
"v_mfma_f32_16x16x32_bf16 v[204:207], a[220:223], v[28:31], v[204:207]\n"
"v_exp_f32 v172, v196\n"
"v_mfma_f32_16x16x32_bf16 a[64:67], a[224:227], v[128:131], a[64:67]\n"
"buffer_load_dwordx4 v248, s[36:39], s57 offen offset:2048 nt lds\n"
"v_mfma_f32_16x16x32_bf16 a[96:99], a[224:227], v[132:135], a[96:99]\n"
"v_exp_f32 v173, v197\n"
"v_mfma_f32_16x16x32_bf16 a[68:71], a[228:231], v[128:131], a[68:71]\n"
"v_cvt_pk_bf16_f32 v122, v172, v173\n"
"v_mfma_f32_16x16x32_bf16 a[100:103], a[228:231], v[132:135], a[100:103]\n"
"v_exp_f32 v174, v198\n"
"v_mfma_f32_16x16x32_bf16 a[72:75], a[232:235], v[128:131], a[72:75]\n"
"v_or3_b32 v184, v120, v121, v122\n"
"v_mfma_f32_16x16x32_bf16 a[104:107], a[232:235], v[132:135], a[104:107]\n"
"v_exp_f32 v175, v199\n"
"v_mfma_f32_16x16x32_bf16 a[76:79], a[236:239], v[128:131], a[76:79]\n"
"buffer_load_dwordx4 v249, s[36:39], s57 offen offset:3072 nt lds\n"
"v_cvt_pk_bf16_f32 v123, v174, v175\n"
"v_mfma_f32_16x16x32_bf16 a[108:111], a[236:239], v[132:135], a[108:111]\n"
"s_add_u32 m0, s58, 4096\n"
"v_exp_f32 v172, v200\n"
"v_mfma_f32_16x16x32_bf16 a[80:83], a[240:243], v[128:131], a[80:83]\n"
"s_add_u32 s57, s57, 16384\n"
"s_and_b32 s57, s57, 0x3ffff\n"
"v_mfma_f32_16x16x32_bf16 a[112:115], a[240:243], v[132:135], a[112:115]\n"
"v_exp_f32 v173, v201\n"
"v_mfma_f32_16x16x32_bf16 a[84:87], a[244:247], v[128:131], a[84:87]\n"
"buffer_load_dwordx4 v246, s[36:39], s71 offen offset:0 nt lds\n"
"v_cvt_pk_bf16_f32 v124, v172, v173\n"
"v_mfma_f32_16x16x32_bf16 a[116:119], a[244:247], v[132:135], a[116:119]\n"
"v_exp_f32 v174, v202\n"
"v_mfma_f32_16x16x32_bf16 a[88:91], a[248:251], v[128:131], a[88:91]\n"
"v_or3_b32 v184, v184, v123, v124\n"
"v_mfma_f32_16x16x32_bf16 a[120:123], a[248:251], v[132:135], a[120:123]\n"
"v_exp_f32 v175, v203\n"
"v_mfma_f32_16x16x32_bf16 a[92:95], a[252:255], v[128:131], a[92:95]\n"
"v_cvt_pk_bf16_f32 v125, v174, v175\n"
"v_mfma_f32_16x16x32_bf16 a[124:127], a[252:255], v[132:135], a[124:127]\n"
"v_exp_f32 v172, v204\n"
"v_mfma_f32_16x16x32_bf16 v[152:155], v[168:171], v[128:131], v[152:155]\n"
"buffer_load_dwordx4 v247, s[36:39], s71 offen offset:1024 nt lds\n"
"v_mfma_f32_16x16x32_bf16 v[156:159], v[168:171], v[132:135], v[156:159]\n"
"v_exp_f32 v173, v205\n"
"v_mfma_f32_16x16x32_bf16 v[208:211], a[192:195], v[32:35], v[104:107]\n"
"v_cvt_pk_bf16_f32 v126, v172, v173\n"
"v_mfma_f32_16x16x32_bf16 v[208:211], a[196:199], v[36:39], v[208:211]\n"
"s_memrealtime s[22:23]\n"
"v_exp_f32 v174, v206\n"
"v_mfma_f32_16x16x32_bf16 v[208:211], a[200:203], v[40:43], v[208:211]\n"
"buffer_load_dwordx4 v248, s[36:39], s71 offen offset:2048 nt lds\n"
"v_or3_b32 v184, v184, v125, v126\n"
"v_mfma_f32_16x16x32_bf16 v[208:211], a[204:207], v[44:47], v[208:211]\n"
"v_exp_f32 v175, v207\n"
"v_mfma_f32_16x16x32_bf16 v[212:215], a[208:211], v[32:35], v[104:107]\n"
"v_cvt_pk_bf16_f32 v127, v174, v175\n"
"v_bitop3_b32 v184, v184, v127, s60 bitop3:0xa8\n"
"v_mfma_f32_16x16x32_bf16 v[212:215], a[212:215], v[36:39], v[212:215]\n"
"v_cmp_ne_u32 vcc, 0, v184\n"
"v_mfma_f32_16x16x32_bf16 v[212:215], a[216:219], v[40:43], v[212:215]\n"
"v_mfma_f32_16x16x32_bf16 v[212:215], a[220:223], v[44:47], v[212:215]\n"
"s_cbranch_vccnz .Lk1_tr110\n"
".Lk1_ct110:\n"
"v_exp_f32 v172, v208\n"
"v_mfma_f32_16x16x32_bf16 v[216:219], a[192:195], v[48:51], v[108:111]\n"
"buffer_load_dwordx4 v249, s[36:39], s71 offen offset:3072 nt lds\n"
"v_mfma_f32_16x16x32_bf16 v[216:219], a[196:199], v[52:55], v[216:219]\n"
"s_add_u32 s72, s58, 0x8000\n"
"v_exp_f32 v173, v209\n"
"v_mfma_f32_16x16x32_bf16 v[216:219], a[200:203], v[56:59], v[216:219]\n"
"s_waitcnt lgkmcnt(0)\n"
"v_cvt_pk_bf16_f32 v128, v172, v173\n"
"v_mfma_f32_16x16x32_bf16 v[216:219], a[204:207], v[60:63], v[216:219]\n"
"s_sub_u32 s25, s22, s24\n"
"v_exp_f32 v174, v210\n"
"v_mfma_f32_16x16x32_bf16 v[220:223], a[208:211], v[48:51], v[108:111]\n"
"s_cmp_lt_i32 s25, 0\n"
"s_cbranch_scc1 .Lk1_tr108\n"
".Lk1_ct108:\n"
"v_mfma_f32_16x16x32_bf16 v[220:223], a[212:215], v[52:55], v[220:223]\n"
"v_exp_f32 v175, v211\n"
"v_mfma_f32_16x16x32_bf16 v[220:223], a[216:219], v[56:59], v[220:223]\n"
"s_waitcnt vmcnt(16)\n"
"v_cvt_pk_bf16_f32 v129, v174, v175\n"
"v_mfma_f32_16x16x32_bf16 v[220:223], a[220:223], v[60:63], v[220:223]\n"
"s_barrier\n"
"v_exp_f32 v172, v212\n"
"v_mfma_f32_16x16x32_bf16 a[128:131], a[224:227], v[136:139], a[128:131]\n"
"s_add_u32 s24, s22, 118\n"
"s_add_u32 s87, s87, 124\n"
"s_max_u32 s24, s24, s87\n"
"v_mfma_f32_16x16x32_bf16 a[160:163], a[224:227], v[140:143], a[160:163]\n"
"s_lshl_b32 s44, s65, 14\n"
"v_exp_f32 v173, v213\n"
"v_mfma_f32_16x16x32_bf16 a[132:135], a[228:231], v[136:139], a[132:135]\n"
"s_add_u32 s45, s66, 0x10000\n"
"ds_read_b64_tr_b16 a[224:225], v241 offset:0\n"
"v_cvt_pk_bf16_f32 v130, v172, v173\n"
"v_mfma_f32_16x16x32_bf16 a[164:167], a[228:231], v[140:143], a[164:167]\n"
"s_add_u32 s44, s44, s45\n"
"v_exp_f32 v174, v214\n"
"v_mfma_f32_16x16x32_bf16 a[136:139], a[232:235], v[136:139], a[136:139]\n"
"s_xor_b32 s46, s65, 1\n"
"ds_read_b64_tr_b16 a[226:227], v241 offset:256\n"
"v_or3_b32 v184, v128, v129, v130\n"
"v_mfma_f32_16x16x32_bf16 a[168:171], a[232:235], v[140:143], a[168:171]\n"
"s_lshl_b32 s46, s46, 14\n"
"v_exp_f32 v175, v215\n"
"v_mfma_f32_16x16x32_bf16 a[140:143], a[236:239], v[136:139], a[140:143]\n"
"s_add_u32 s45, s45, s46\n"
"ds_read_b64_tr_b16 a[228:229], v242 offset:0\n"
"v_cvt_pk_bf16_f32 v131, v174, v175\n"
"v_mfma_f32_16x16x32_bf16 a[172:175], a[236:239], v[140:143], a[172:175]\n"
"s_xor_b32 s46, s67, 2\n"
"v_exp_f32 v172, v216\n"
"v_mfma_f32_16x16x32_bf16 a[144:147], a[240:243], v[136:139], a[144:147]\n"
"s_lshl_b32 s46, s46, 13\n"
"ds_read_b64_tr_b16 a[230:231], v242 offset:256\n"
"ds_read_b64_tr_b16 a[232:233], v241 offset:512\n"
"v_mfma_f32_16x16x32_bf16 a[176:179], a[240:243], v[140:143], a[176:179]\n"
"s_add_u32 s46, s46, s66\n"
"v_exp_f32 v173, v217\n"
"v_mfma_f32_16x16x32_bf16 a[148:151], a[244:247], v[136:139], a[148:151]\n"
"s_bfe_u32 s57, s86, 0x40009\n"
"ds_read_b64_tr_b16 a[234:235], v241 offset:768\n"
"v_cvt_pk_bf16_f32 v132, v172, v173\n"
"v_mfma_f32_16x16x32_bf16 a[180:183], a[244:247], v[140:143], a[180:183]\n"
"s_mul_i32 s57, s57, 12288\n"
"v_exp_f32 v174, v218\n"
"v_mfma_f32_16x16x32_bf16 a[152:155], a[248:251], v[136:139], a[152:155]\n"
"s_mov_b32 s28, s90\n"
"ds_read_b64_tr_b16 a[236:237], v242 offset:512\n"
"v_or3_b32 v184, v184, v131, v132\n"
"v_mfma_f32_16x16x32_bf16 a[184:187], a[248:251], v[140:143], a[184:187]\n"
"s_and_b32 s29, s91, 0xffff\n"
"v_exp_f32 v175, v219\n"
"v_mfma_f32_16x16x32_bf16 a[156:159], a[252:255], v[136:139], a[156:159]\n"
"s_mov_b32 s30, 96320\n"
"ds_read_b64_tr_b16 a[238:239], v242 offset:768\n"
"v_cvt_pk_bf16_f32 v133, v174, v175\n"
"v_mfma_f32_16x16x32_bf16 a[188:191], a[252:255], v[140:143], a[188:191]\n"
"v_exp_f32 v172, v220\n"
"v_mfma_f32_16x16x32_bf16 v[160:163], v[168:171], v[136:139], v[160:163]\n"
"ds_read_b64_tr_b16 a[240:241], v241 offset:1024\n"
"ds_read_b64_tr_b16 a[242:243], v241 offset:1280\n"
"v_mfma_f32_16x16x32_bf16 v[164:167], v[168:171], v[140:143], v[164:167]\n"
"v_exp_f32 v173, v221\n"
"v_mfma_f32_16x16x32_bf16 v[224:227], a[192:195], v[64:67], v[112:115]\n"
"ds_read_b64_tr_b16 a[244:245], v242 offset:1024\n"
"v_cvt_pk_bf16_f32 v134, v172, v173\n"
"v_mfma_f32_16x16x32_bf16 v[224:227], a[196:199], v[68:71], v[224:227]\n"
"v_exp_f32 v174, v222\n"
"v_mfma_f32_16x16x32_bf16 v[224:227], a[200:203], v[72:75], v[224:227]\n"
"ds_read_b64_tr_b16 a[246:247], v242 offset:1280\n"
"v_or3_b32 v184, v184, v133, v134\n"
"v_mfma_f32_16x16x32_bf16 v[224:227], a[204:207], v[76:79], v[224:227]\n"
"v_exp_f32 v175, v223\n"
"v_mfma_f32_16x16x32_bf16 v[228:231], a[208:211], v[64:67], v[112:115]\n"
"v_cvt_pk_bf16_f32 v135, v174, v175\n"
"v_bitop3_b32 v184, v184, v135, s60 bitop3:0xa8\n"
"v_mfma_f32_16x16x32_bf16 v[228:231], a[212:215], v[68:71], v[228:231]\n"
"ds_read_b64_tr_b16 a[248:249], v241 offset:1536\n"
"v_cmp_ne_u32 vcc, 0, v184\n"
"v_mfma_f32_16x16x32_bf16 v[228:231], a[216:219], v[72:75], v[228:231]\n"
"ds_read_b64_tr_b16 a[250:251], v241 offset:1792\n"
"ds_read_b64_tr_b16 a[252:253], v242 offset:1536\n"
"v_mfma_f32_16x16x32_bf16 v[228:231], a[220:223], v[76:79], v[228:231]\n"
"s_cbranch_vccnz .Lk1_tr111\n"
".Lk1_ct111:\n"
"v_exp_f32 v172, v224\n"
"v_mfma_f32_16x16x32_bf16 v[232:235], a[192:195], v[80:83], v[116:119]\n"
"ds_read_b64_tr_b16 a[254:255], v242 offset:1792\n"
"ds_read_b128 a[192:195], v240 offset:8192\n"
"v_mfma_f32_16x16x32_bf16 v[232:235], a[196:199], v[84:87], v[232:235]\n"
"v_exp_f32 v173, v225\n"
"v_mfma_f32_16x16x32_bf16 v[232:235], a[200:203], v[88:91], v[232:235]\n"
"ds_read_b128 a[196:199], v240 offset:8704\n"
"v_cvt_pk_bf16_f32 v136, v172, v173\n"
"v_mfma_f32_16x16x32_bf16 v[232:235], a[204:207], v[92:95], v[232:235]\n"
"v_exp_f32 v174, v226\n"
"v_mfma_f32_16x16x32_bf16 v[236:239], a[208:211], v[80:83], v[116:119]\n"
"ds_read_b128 a[200:203], v240 offset:9216\n"
"ds_read_b128 a[204:207], v240 offset:9728\n"
"v_mfma_f32_16x16x32_bf16 v[236:239], a[212:215], v[84:87], v[236:239]\n"
"v_exp_f32 v175, v227\n"
"v_mfma_f32_16x16x32_bf16 v[236:239], a[216:219], v[88:91], v[236:239]\n"
"ds_read_b128 a[208:211], v240 offset:8448\n"
"v_cvt_pk_bf16_f32 v137, v174, v175\n"
"v_mfma_f32_16x16x32_bf16 v[236:239], a[220:223], v[92:95], v[236:239]\n"
"s_waitcnt lgkmcnt(13)\n"
"v_exp_f32 v172, v228\n"
"v_mfma_f32_16x16x32_bf16 a[0:3], a[224:227], v[120:123], a[0:3]\n"
"ds_read_b128 a[212:215], v240 offset:8960\n"
"ds_read_b128 a[216:219], v240 offset:9472\n"
"v_mfma_f32_16x16x32_bf16 a[32:35], a[224:227], v[124:127], a[32:35]\n"
"v_exp_f32 v173, v229\n"
"v_mfma_f32_16x16x32_bf16 a[4:7], a[228:231], v[120:123], a[4:7]\n"
"ds_read_b128 a[220:223], v240 offset:9984\n"
"v_cvt_pk_bf16_f32 v138, v172, v173\n"
"v_mfma_f32_16x16x32_bf16 a[36:39], a[228:231], v[124:127], a[36:39]\n"
"v_exp_f32 v174, v230\n"
"v_mfma_f32_16x16x32_bf16 a[8:11], a[232:235], v[120:123], a[8:11]\n"
"s_cmp_lt_u32 s72, s81\n"
"s_cselect_b32 s58, s72, s59\n"
"v_or3_b32 v184, v136, v137, v138\n"
"v_mfma_f32_16x16x32_bf16 a[40:43], a[232:235], v[124:127], a[40:43]\n"
"v_exp_f32 v175, v231\n"
"v_mfma_f32_16x16x32_bf16 a[12:15], a[236:239], v[120:123], a[12:15]\n"
"v_cvt_pk_bf16_f32 v139, v174, v175\n"
"v_subrev_u32 v186, s82, v186\n"
"v_mfma_f32_16x16x32_bf16 a[44:47], a[236:239], v[124:127], a[44:47]\n"
"s_waitcnt lgkmcnt(8)\n"
"v_exp_f32 v172, v232\n"
"v_mfma_f32_16x16x32_bf16 a[16:19], a[240:243], v[120:123], a[16:19]\n"
"v_cmp_lt_u32 vcc, 1, v186\n"
"v_mfma_f32_16x16x32_bf16 a[48:51], a[240:243], v[124:127], a[48:51]\n"
"v_exp_f32 v173, v233\n"
"v_mfma_f32_16x16x32_bf16 a[20:23], a[244:247], v[120:123], a[20:23]\n"
"s_and_b32 s33, vcc_lo, 0xdfc\n"
"s_cbranch_scc1 .Lk1_hw\n"
".Lk1_hd:\n"
"v_cvt_pk_bf16_f32 v140, v172, v173\n"
"v_mfma_f32_16x16x32_bf16 a[52:55], a[244:247], v[124:127], a[52:55]\n"
"s_add_u32 s82, s82, 1\n"
"v_exp_f32 v174, v234\n"
"v_mfma_f32_16x16x32_bf16 a[24:27], a[248:251], v[120:123], a[24:27]\n"
"v_or3_b32 v184, v184, v139, v140\n"
"v_mfma_f32_16x16x32_bf16 a[56:59], a[248:251], v[124:127], a[56:59]\n"
"v_exp_f32 v175, v235\n"
"v_mfma_f32_16x16x32_bf16 a[28:31], a[252:255], v[120:123], a[28:31]\n"
"v_cvt_pk_bf16_f32 v141, v174, v175\n"
"v_mfma_f32_16x16x32_bf16 a[60:63], a[252:255], v[124:127], a[60:63]\n"
"v_exp_f32 v172, v236\n"
"v_mfma_f32_16x16x32_bf16 v[144:147], v[168:171], v[120:123], v[144:147]\n"
"v_mfma_f32_16x16x32_bf16 v[148:151], v[168:171], v[124:127], v[148:151]\n"
"s_waitcnt lgkmcnt(4)\n"
"v_exp_f32 v173, v237\n"
"v_mfma_f32_16x16x32_bf16 v[192:195], a[192:195], v[0:3], v[96:99]\n"
"v_cvt_pk_bf16_f32 v142, v172, v173\n"
"v_add_u32 v246, s44, v243\n"
"v_mfma_f32_16x16x32_bf16 v[192:195], a[196:199], v[4:7], v[192:195]\n"
"v_exp_f32 v174, v238\n"
"v_mfma_f32_16x16x32_bf16 v[192:195], a[200:203], v[8:11], v[192:195]\n"
"s_add_u32 s73, s94, 0x0\n"
"s_and_b32 s73, s73, 0x380\n"
"v_or3_b32 v184, v184, v141, v142\n"
"v_mfma_f32_16x16x32_bf16 v[192:195], a[204:207], v[12:15], v[192:195]\n"
"s_waitcnt lgkmcnt(0)\n"
"v_exp_f32 v175, v239\n"
"v_mfma_f32_16x16x32_bf16 v[196:199], a[208:211], v[0:3], v[96:99]\n"
"s_add_u32 s73, s73, s96\n"
"v_cvt_pk_bf16_f32 v143, v174, v175\n"
"v_bitop3_b32 v184, v184, v143, s60 bitop3:0xa8\n"
"v_mfma_f32_16x16x32_bf16 v[196:199], a[212:215], v[4:7], v[196:199]\n"
"s_add_u32 m0, s68, 0x10000\n"
"s_add_u32 s74, s73, 0x0\n"
"v_cmp_ne_u32 vcc, 0, v184\n"
"v_mfma_f32_16x16x32_bf16 v[196:199], a[216:219], v[8:11], v[196:199]\n"
"buffer_load_dwordx4 v188, s[40:43], s74 offen offset:0 lds\n"
"buffer_load_dword v254, v245, s[52:55], 0 offen sc1\n"
"v_mfma_f32_16x16x32_bf16 v[196:199], a[220:223], v[12:15], v[196:199]\n"
"s_cbranch_vccnz .Lk1_tr112\n"
".Lk1_ct112:\n"
"v_exp_f32 v172, v192\n"
"v_mfma_f32_16x16x32_bf16 v[200:203], a[192:195], v[16:19], v[100:103]\n"
"s_cmp_lt_u32 s77, 0x18000\n"
"s_cselect_b32 s75, s79, s80\n"
"s_add_u32 s77, s77, s75\n"
"v_mfma_f32_16x16x32_bf16 v[200:203], a[196:199], v[20:23], v[200:203]\n"
"v_exp_f32 v173, v193\n"
"v_mfma_f32_16x16x32_bf16 v[200:203], a[200:203], v[24:27], v[200:203]\n"
"v_cvt_pk_bf16_f32 v120, v172, v173\n"
"v_add_u32 v240, s75, v240\n"
"v_mfma_f32_16x16x32_bf16 v[200:203], a[204:207], v[28:31], v[200:203]\n"
"v_exp_f32 v174, v194\n"
"v_mfma_f32_16x16x32_bf16 v[204:207], a[208:211], v[16:19], v[100:103]\n"
"buffer_load_dwordx4 v189, s[40:43], s74 offen offset:1024 lds\n"
"v_xor_b32 v247, 32, v246\n"
"v_mfma_f32_16x16x32_bf16 v[204:207], a[212:215], v[20:23], v[204:207]\n"
"s_add_u32 s74, s73, 0x3800\n"
"v_exp_f32 v175, v195\n"
"v_mfma_f32_16x16x32_bf16 v[204:207], a[216:219], v[24:27], v[204:207]\n"
"v_cvt_pk_bf16_f32 v121, v174, v175\n"
"v_add_u32 v248, s45, v243\n"
"v_mfma_f32_16x16x32_bf16 v[204:207], a[220:223], v[28:31], v[204:207]\n"
"v_exp_f32 v172, v196\n"
"v_mfma_f32_16x16x32_bf16 a[64:67], a[224:227], v[128:131], a[64:67]\n"
"buffer_load_dwordx4 v188, s[40:43], s74 offen offset:2048 lds\n"
"v_xor_b32 v249, 32, v248\n"
"v_mfma_f32_16x16x32_bf16 a[96:99], a[224:227], v[132:135], a[96:99]\n"
"v_exp_f32 v173, v197\n"
"v_mfma_f32_16x16x32_bf16 a[68:71], a[228:231], v[128:131], a[68:71]\n"
"v_cvt_pk_bf16_f32 v122, v172, v173\n"
"v_add_u32 v250, s68, v252\n"
"v_mfma_f32_16x16x32_bf16 a[100:103], a[228:231], v[132:135], a[100:103]\n"
"v_exp_f32 v174, v198\n"
"v_mfma_f32_16x16x32_bf16 a[72:75], a[232:235], v[128:131], a[72:75]\n"
"v_or3_b32 v184, v120, v121, v122\n"
"v_add_u32 v251, s46, v252\n"
"v_mfma_f32_16x16x32_bf16 a[104:107], a[232:235], v[132:135], a[104:107]\n"
"v_exp_f32 v175, v199\n"
"v_mfma_f32_16x16x32_bf16 a[76:79], a[236:239], v[128:131], a[76:79]\n"
"buffer_load_dwordx4 v189, s[40:43], s74 offen offset:3072 lds\n"
"v_cvt_pk_bf16_f32 v123, v174, v175\n"
"v_mfma_f32_16x16x32_bf16 a[108:111], a[236:239], v[132:135], a[108:111]\n"
"s_add_u32 m0, s68, 0x11000\n"
"v_exp_f32 v172, v200\n"
"v_mfma_f32_16x16x32_bf16 a[80:83], a[240:243], v[128:131], a[80:83]\n"
"s_add_u32 s74, s73, 0x8000\n"
"v_mfma_f32_16x16x32_bf16 a[112:115], a[240:243], v[132:135], a[112:115]\n"
"v_exp_f32 v173, v201\n"
"v_mfma_f32_16x16x32_bf16 a[84:87], a[244:247], v[128:131], a[84:87]\n"
"buffer_load_dwordx4 v188, s[40:43], s74 offen offset:0 lds\n"
"v_cvt_pk_bf16_f32 v124, v172, v173\n"
"v_mfma_f32_16x16x32_bf16 a[116:119], a[244:247], v[132:135], a[116:119]\n"
"v_exp_f32 v174, v202\n"
"v_mfma_f32_16x16x32_bf16 a[88:91], a[248:251], v[128:131], a[88:91]\n"
"v_or3_b32 v184, v184, v123, v124\n"
"v_mfma_f32_16x16x32_bf16 a[120:123], a[248:251], v[132:135], a[120:123]\n"
"v_exp_f32 v175, v203\n"
"v_mfma_f32_16x16x32_bf16 a[92:95], a[252:255], v[128:131], a[92:95]\n"
"v_cvt_pk_bf16_f32 v125, v174, v175\n"
"v_mfma_f32_16x16x32_bf16 a[124:127], a[252:255], v[132:135], a[124:127]\n"
"v_exp_f32 v172, v204\n"
"v_mfma_f32_16x16x32_bf16 v[152:155], v[168:171], v[128:131], v[152:155]\n"
"buffer_load_dwordx4 v189, s[40:43], s74 offen offset:1024 lds\n"
"v_mfma_f32_16x16x32_bf16 v[156:159], v[168:171], v[132:135], v[156:159]\n"
"s_add_u32 s74, s73, 0xb800\n"
"v_exp_f32 v173, v205\n"
"v_mfma_f32_16x16x32_bf16 v[208:211], a[192:195], v[32:35], v[104:107]\n"
"v_cvt_pk_bf16_f32 v126, v172, v173\n"
"v_mfma_f32_16x16x32_bf16 v[208:211], a[196:199], v[36:39], v[208:211]\n"
"v_exp_f32 v174, v206\n"
"v_mfma_f32_16x16x32_bf16 v[208:211], a[200:203], v[40:43], v[208:211]\n"
"buffer_load_dwordx4 v188, s[40:43], s74 offen offset:2048 lds\n"
"v_or3_b32 v184, v184, v125, v126\n"
"v_mfma_f32_16x16x32_bf16 v[208:211], a[204:207], v[44:47], v[208:211]\n"
"v_exp_f32 v175, v207\n"
"v_mfma_f32_16x16x32_bf16 v[212:215], a[208:211], v[32:35], v[104:107]\n"
"v_cvt_pk_bf16_f32 v127, v174, v175\n"
"v_bitop3_b32 v184, v184, v127, s60 bitop3:0xa8\n"
"v_mfma_f32_16x16x32_bf16 v[212:215], a[212:215], v[36:39], v[212:215]\n"
"v_cmp_ne_u32 vcc, 0, v184\n"
"v_mfma_f32_16x16x32_bf16 v[212:215], a[216:219], v[40:43], v[212:215]\n"
"v_mfma_f32_16x16x32_bf16 v[212:215], a[220:223], v[44:47], v[212:215]\n"
"s_cbranch_vccnz .Lk1_tr113\n"
".Lk1_ct113:\n"
"v_exp_f32 v172, v208\n"
"v_mfma_f32_16x16x32_bf16 v[216:219], a[192:195], v[48:51], v[108:111]\n"
"buffer_load_dwordx4 v189, s[40:43], s74 offen offset:3072 lds\n"
"v_mfma_f32_16x16x32_bf16 v[216:219], a[196:199], v[52:55], v[216:219]\n"
"v_exp_f32 v173, v209\n"
"v_mfma_f32_16x16x32_bf16 v[216:219], a[200:203], v[56:59], v[216:219]\n"
"buffer_load_dword v191, v190, s[40:43], 0 offen sc1\n"
"v_cvt_pk_bf16_f32 v128, v172, v173\n"
"v_mfma_f32_16x16x32_bf16 v[216:219], a[204:207], v[60:63], v[216:219]\n"
"v_exp_f32 v174, v210\n"
"v_mfma_f32_16x16x32_bf16 v[220:223], a[208:211], v[48:51], v[108:111]\n"
"v_mfma_f32_16x16x32_bf16 v[220:223], a[212:215], v[52:55], v[220:223]\n"
"v_exp_f32 v175, v211\n"
"v_mfma_f32_16x16x32_bf16 v[220:223], a[216:219], v[56:59], v[220:223]\n"
"s_waitcnt vmcnt(18)\n"
"v_cvt_pk_bf16_f32 v129, v174, v175\n"
"v_mfma_f32_16x16x32_bf16 v[220:223], a[220:223], v[60:63], v[220:223]\n"
"s_barrier\n"
"v_exp_f32 v172, v212\n"
"v_mfma_f32_16x16x32_bf16 a[128:131], a[224:227], v[136:139], a[128:131]\n"
"v_mfma_f32_16x16x32_bf16 a[160:163], a[224:227], v[140:143], a[160:163]\n"
"v_exp_f32 v173, v213\n"
"v_mfma_f32_16x16x32_bf16 a[132:135], a[228:231], v[136:139], a[132:135]\n"
"ds_read_b64_tr_b16 a[224:225], v241 offset:8192\n"
"v_cvt_pk_bf16_f32 v130, v172, v173\n"
"v_mfma_f32_16x16x32_bf16 a[164:167], a[228:231], v[140:143], a[164:167]\n"
"v_exp_f32 v174, v214\n"
"v_mfma_f32_16x16x32_bf16 a[136:139], a[232:235], v[136:139], a[136:139]\n"
"ds_read_b64_tr_b16 a[226:227], v241 offset:8448\n"
"v_or3_b32 v184, v128, v129, v130\n"
"v_mfma_f32_16x16x32_bf16 a[168:171], a[232:235], v[140:143], a[168:171]\n"
"v_exp_f32 v175, v215\n"
"v_mfma_f32_16x16x32_bf16 a[140:143], a[236:239], v[136:139], a[140:143]\n"
"ds_read_b64_tr_b16 a[228:229], v242 offset:8192\n"
"v_cvt_pk_bf16_f32 v131, v174, v175\n"
"v_mfma_f32_16x16x32_bf16 a[172:175], a[236:239], v[140:143], a[172:175]\n"
"v_exp_f32 v172, v216\n"
"v_mfma_f32_16x16x32_bf16 a[144:147], a[240:243], v[136:139], a[144:147]\n"
"ds_read_b64_tr_b16 a[230:231], v242 offset:8448\n"
"ds_read_b64_tr_b16 a[232:233], v241 offset:8704\n"
"v_mfma_f32_16x16x32_bf16 a[176:179], a[240:243], v[140:143], a[176:179]\n"
"v_exp_f32 v173, v217\n"
"v_mfma_f32_16x16x32_bf16 a[148:151], a[244:247], v[136:139], a[148:151]\n"
"ds_read_b64_tr_b16 a[234:235], v241 offset:8960\n"
"v_cvt_pk_bf16_f32 v132, v172, v173\n"
"v_mfma_f32_16x16x32_bf16 a[180:183], a[244:247], v[140:143], a[180:183]\n"
"v_exp_f32 v174, v218\n"
"v_mfma_f32_16x16x32_bf16 a[152:155], a[248:251], v[136:139], a[152:155]\n"
"ds_read_b64_tr_b16 a[236:237], v242 offset:8704\n"
"v_or3_b32 v184, v184, v131, v132\n"
"v_mfma_f32_16x16x32_bf16 a[184:187], a[248:251], v[140:143], a[184:187]\n"
"v_exp_f32 v175, v219\n"
"v_mfma_f32_16x16x32_bf16 a[156:159], a[252:255], v[136:139], a[156:159]\n"
"ds_read_b64_tr_b16 a[238:239], v242 offset:8960\n"
"v_cvt_pk_bf16_f32 v133, v174, v175\n"
"v_mfma_f32_16x16x32_bf16 a[188:191], a[252:255], v[140:143], a[188:191]\n"
"v_exp_f32 v172, v220\n"
"v_mfma_f32_16x16x32_bf16 v[160:163], v[168:171], v[136:139], v[160:163]\n"
"ds_read_b64_tr_b16 a[240:241], v241 offset:9216\n"
"ds_read_b64_tr_b16 a[242:243], v241 offset:9472\n"
"v_mfma_f32_16x16x32_bf16 v[164:167], v[168:171], v[140:143], v[164:167]\n"
"v_exp_f32 v173, v221\n"
"v_mfma_f32_16x16x32_bf16 v[224:227], a[192:195], v[64:67], v[112:115]\n"
"ds_read_b64_tr_b16 a[244:245], v242 offset:9216\n"
"v_cvt_pk_bf16_f32 v134, v172, v173\n"
"v_mfma_f32_16x16x32_bf16 v[224:227], a[196:199], v[68:71], v[224:227]\n"
"v_exp_f32 v174, v222\n"
"v_mfma_f32_16x16x32_bf16 v[224:227], a[200:203], v[72:75], v[224:227]\n"
"ds_read_b64_tr_b16 a[246:247], v242 offset:9472\n"
"v_or3_b32 v184, v184, v133, v134\n"
"v_mfma_f32_16x16x32_bf16 v[224:227], a[204:207], v[76:79], v[224:227]\n"
"v_exp_f32 v175, v223\n"
"v_mfma_f32_16x16x32_bf16 v[228:231], a[208:211], v[64:67], v[112:115]\n"
"v_cvt_pk_bf16_f32 v135, v174, v175\n"
"v_bitop3_b32 v184, v184, v135, s60 bitop3:0xa8\n"
"v_mfma_f32_16x16x32_bf16 v[228:231], a[212:215], v[68:71], v[228:231]\n"
"ds_read_b64_tr_b16 a[248:249], v241 offset:9728\n"
"v_cmp_ne_u32 vcc, 0, v184\n"
"v_mfma_f32_16x16x32_bf16 v[228:231], a[216:219], v[72:75], v[228:231]\n"
"ds_read_b64_tr_b16 a[250:251], v241 offset:9984\n"
"ds_read_b64_tr_b16 a[252:253], v242 offset:9728\n"
"v_mfma_f32_16x16x32_bf16 v[228:231], a[220:223], v[76:79], v[228:231]\n"
"s_cbranch_vccnz .Lk1_tr114\n"
".Lk1_ct114:\n"
"v_exp_f32 v172, v224\n"
"v_mfma_f32_16x16x32_bf16 v[232:235], a[192:195], v[80:83], v[116:119]\n"
"ds_read_b64_tr_b16 a[254:255], v242 offset:9984\n"
"ds_read_b128 a[192:195], v240 offset:0\n"
"v_mfma_f32_16x16x32_bf16 v[232:235], a[196:199], v[84:87], v[232:235]\n"
"v_exp_f32 v173, v225\n"
"v_mfma_f32_16x16x32_bf16 v[232:235], a[200:203], v[88:91], v[232:235]\n"
"ds_read_b128 a[196:199], v240 offset:512\n"
"v_cvt_pk_bf16_f32 v136, v172, v173\n"
"v_mfma_f32_16x16x32_bf16 v[232:235], a[204:207], v[92:95], v[232:235]\n"
"v_exp_f32 v174, v226\n"
"v_mfma_f32_16x16x32_bf16 v[236:239], a[208:211], v[80:83], v[116:119]\n"
"ds_read_b128 a[200:203], v240 offset:1024\n"
"ds_read_b128 a[204:207], v240 offset:1536\n"
"v_mfma_f32_16x16x32_bf16 v[236:239], a[212:215], v[84:87], v[236:239]\n"
"v_exp_f32 v175, v227\n"
"v_mfma_f32_16x16x32_bf16 v[236:239], a[216:219], v[88:91], v[236:239]\n"
"ds_read_b128 a[208:211], v240 offset:256\n"
"v_cvt_pk_bf16_f32 v137, v174, v175\n"
"v_mfma_f32_16x16x32_bf16 v[236:239], a[220:223], v[92:95], v[236:239]\n"
"s_waitcnt lgkmcnt(13)\n"
"v_exp_f32 v172, v228\n"
"v_mfma_f32_16x16x32_bf16 a[0:3], a[224:227], v[120:123], a[0:3]\n"
"ds_read_b128 a[212:215], v240 offset:768\n"
"ds_read_b128 a[216:219], v240 offset:1280\n"
"v_mfma_f32_16x16x32_bf16 a[32:35], a[224:227], v[124:127], a[32:35]\n"
"v_exp_f32 v173, v229\n"
"v_mfma_f32_16x16x32_bf16 a[4:7], a[228:231], v[120:123], a[4:7]\n"
"ds_read_b128 a[220:223], v240 offset:1792\n"
"v_cvt_pk_bf16_f32 v138, v172, v173\n"
"v_mfma_f32_16x16x32_bf16 a[36:39], a[228:231], v[124:127], a[36:39]\n"
"v_exp_f32 v174, v230\n"
"v_mfma_f32_16x16x32_bf16 a[8:11], a[232:235], v[120:123], a[8:11]\n"
"s_cmp_lt_u32 s78, 0x18000\n"
"s_cselect_b32 s75, s79, s80\n"
"v_or3_b32 v184, v136, v137, v138\n"
"v_mfma_f32_16x16x32_bf16 a[40:43], a[232:235], v[124:127], a[40:43]\n"
"s_add_u32 s78, s78, s75\n"
"v_exp_f32 v175, v231\n"
"v_mfma_f32_16x16x32_bf16 a[12:15], a[236:239], v[120:123], a[12:15]\n"
"v_cvt_pk_bf16_f32 v139, v174, v175\n"
"v_add_u32 v241, s75, v241\n"
"v_mfma_f32_16x16x32_bf16 a[44:47], a[236:239], v[124:127], a[44:47]\n"
"s_waitcnt lgkmcnt(8)\n"
"v_exp_f32 v172, v232\n"
"v_mfma_f32_16x16x32_bf16 a[16:19], a[240:243], v[120:123], a[16:19]\n"
"v_add_u32 v242, s75, v242\n"
"v_mfma_f32_16x16x32_bf16 a[48:51], a[240:243], v[124:127], a[48:51]\n"
"v_exp_f32 v173, v233\n"
"v_mfma_f32_16x16x32_bf16 a[20:23], a[244:247], v[120:123], a[20:23]\n"
"v_cvt_pk_bf16_f32 v140, v172, v173\n"
"v_mfma_f32_16x16x32_bf16 a[52:55], a[244:247], v[124:127], a[52:55]\n"
"v_exp_f32 v174, v234\n"
"v_mfma_f32_16x16x32_bf16 a[24:27], a[248:251], v[120:123], a[24:27]\n"
"v_or3_b32 v184, v184, v139, v140\n"
"v_mfma_f32_16x16x32_bf16 a[56:59], a[248:251], v[124:127], a[56:59]\n"
"v_exp_f32 v175, v235\n"
"v_mfma_f32_16x16x32_bf16 a[28:31], a[252:255], v[120:123], a[28:31]\n"
"v_cvt_pk_bf16_f32 v141, v174, v175\n"
"v_mfma_f32_16x16x32_bf16 a[60:63], a[252:255], v[124:127], a[60:63]\n"
"v_exp_f32 v172, v236\n"
"v_mfma_f32_16x16x32_bf16 v[144:147], v[168:171], v[120:123], v[144:147]\n"
"v_mfma_f32_16x16x32_bf16 v[148:151], v[168:171], v[124:127], v[148:151]\n"
"s_waitcnt lgkmcnt(4)\n"
"v_exp_f32 v173, v237\n"
"v_mfma_f32_16x16x32_bf16 v[192:195], a[192:195], v[0:3], v[96:99]\n"
"v_cvt_pk_bf16_f32 v142, v172, v173\n"
"v_bfe_u32 v186, v255, 4, 1\n"
"v_mfma_f32_16x16x32_bf16 v[192:195], a[196:199], v[4:7], v[192:195]\n"
"v_exp_f32 v174, v238\n"
"v_mfma_f32_16x16x32_bf16 v[192:195], a[200:203], v[8:11], v[192:195]\n"
"s_add_u32 s73, s94, 0x80\n"
"s_and_b32 s73, s73, 0x380\n"
"v_or3_b32 v184, v184, v141, v142\n"
"v_mfma_f32_16x16x32_bf16 v[192:195], a[204:207], v[12:15], v[192:195]\n"
"s_waitcnt lgkmcnt(0)\n"
"v_exp_f32 v175, v239\n"
"v_mfma_f32_16x16x32_bf16 v[196:199], a[208:211], v[0:3], v[96:99]\n"
"s_add_u32 s73, s73, s96\n"
"v_cvt_pk_bf16_f32 v143, v174, v175\n"
"v_bitop3_b32 v184, v184, v143, s60 bitop3:0xa8\n"
"v_mfma_f32_16x16x32_bf16 v[196:199], a[212:215], v[4:7], v[196:199]\n"
"s_add_u32 m0, s68, 0x18000\n"
"s_add_u32 s74, s73, 0x0\n"
"v_cmp_ne_u32 vcc, 0, v184\n"
"v_mfma_f32_16x16x32_bf16 v[196:199], a[216:219], v[8:11], v[196:199]\n"
"buffer_load_dwordx4 v188, s[40:43], s74 offen offset:0 lds\n"
"v_lshlrev_b32 v187, 9, v185\n"
"v_mfma_f32_16x16x32_bf16 v[196:199], a[220:223], v[12:15], v[196:199]\n"
"s_cbranch_vccnz .Lk1_tr115\n"
".Lk1_ct115:\n"
"v_exp_f32 v172, v192\n"
"v_mfma_f32_16x16x32_bf16 v[200:203], a[192:195], v[16:19], v[100:103]\n"
"v_lshl_add_u32 v187, v186, 5, v187\n"
"v_lshrrev_b32 v186, 5, v255\n"
"v_mfma_f32_16x16x32_bf16 v[200:203], a[196:199], v[20:23], v[200:203]\n"
"v_exp_f32 v173, v193\n"
"v_mfma_f32_16x16x32_bf16 v[200:203], a[200:203], v[24:27], v[200:203]\n"
"v_cvt_pk_bf16_f32 v120, v172, v173\n"
"v_lshl_add_u32 v187, v186, 4, v187\n"
"v_mfma_f32_16x16x32_bf16 v[200:203], a[204:207], v[28:31], v[200:203]\n"
"v_exp_f32 v174, v194\n"
"v_mfma_f32_16x16x32_bf16 v[204:207], a[208:211], v[16:19], v[100:103]\n"
"buffer_load_dwordx4 v189, s[40:43], s74 offen offset:1024 lds\n"
"v_mfma_f32_16x16x32_bf16 v[204:207], a[212:215], v[20:23], v[204:207]\n"
"s_add_u32 s74, s73, 0x3800\n"
"v_exp_f32 v175, v195\n"
"v_mfma_f32_16x16x32_bf16 v[204:207], a[216:219], v[24:27], v[204:207]\n"
"v_cvt_pk_bf16_f32 v121, v174, v175\n"
"v_mfma_f32_16x16x32_bf16 v[204:207], a[220:223], v[28:31], v[204:207]\n"
"v_exp_f32 v172, v196\n"
"v_mfma_f32_16x16x32_bf16 a[64:67], a[224:227], v[128:131], a[64:67]\n"
"buffer_load_dwordx4 v188, s[40:43], s74 offen offset:2048 lds\n"
"v_mfma_f32_16x16x32_bf16 a[96:99], a[224:227], v[132:135], a[96:99]\n"
"v_exp_f32 v173, v197\n"
"v_mfma_f32_16x16x32_bf16 a[68:71], a[228:231], v[128:131], a[68:71]\n"
"v_cvt_pk_bf16_f32 v122, v172, v173\n"
"v_mfma_f32_16x16x32_bf16 a[100:103], a[228:231], v[132:135], a[100:103]\n"
"v_exp_f32 v174, v198\n"
"v_mfma_f32_16x16x32_bf16 a[72:75], a[232:235], v[128:131], a[72:75]\n"
"v_or3_b32 v184, v120, v121, v122\n"
"v_mfma_f32_16x16x32_bf16 a[104:107], a[232:235], v[132:135], a[104:107]\n"
"v_exp_f32 v175, v199\n"
"v_mfma_f32_16x16x32_bf16 a[76:79], a[236:239], v[128:131], a[76:79]\n"
"buffer_load_dwordx4 v189, s[40:43], s74 offen offset:3072 lds\n"
"v_cvt_pk_bf16_f32 v123, v174, v175\n"
"v_mfma_f32_16x16x32_bf16 a[108:111], a[236:239], v[132:135], a[108:111]\n"
"s_add_u32 m0, s68, 0x19000\n"
"v_exp_f32 v172, v200\n"
"v_mfma_f32_16x16x32_bf16 a[80:83], a[240:243], v[128:131], a[80:83]\n"
"s_add_u32 s74, s73, 0x8000\n"
"v_mfma_f32_16x16x32_bf16 a[112:115], a[240:243], v[132:135], a[112:115]\n"
"v_exp_f32 v173, v201\n"
"v_mfma_f32_16x16x32_bf16 a[84:87], a[244:247], v[128:131], a[84:87]\n"
"buffer_load_dwordx4 v188, s[40:43], s74 offen offset:0 lds\n"
"v_cvt_pk_bf16_f32 v124, v172, v173\n"
"v_mfma_f32_16x16x32_bf16 a[116:119], a[244:247], v[132:135], a[116:119]\n"
"v_exp_f32 v174, v202\n"
"v_mfma_f32_16x16x32_bf16 a[88:91], a[248:251], v[128:131], a[88:91]\n"
"v_or3_b32 v184, v184, v123, v124\n"
"v_mfma_f32_16x16x32_bf16 a[120:123], a[248:251], v[132:135], a[120:123]\n"
"v_exp_f32 v175, v203\n"
"v_mfma_f32_16x16x32_bf16 a[92:95], a[252:255], v[128:131], a[92:95]\n"
"v_cvt_pk_bf16_f32 v125, v174, v175\n"
"v_mfma_f32_16x16x32_bf16 a[124:127], a[252:255], v[132:135], a[124:127]\n"
"v_exp_f32 v172, v204\n"
"v_mfma_f32_16x16x32_bf16 v[152:155], v[168:171], v[128:131], v[152:155]\n"
"buffer_load_dwordx4 v189, s[40:43], s74 offen offset:1024 lds\n"
"v_mfma_f32_16x16x32_bf16 v[156:159], v[168:171], v[132:135], v[156:159]\n"
"s_add_u32 s74, s73, 0xb800\n"
"v_exp_f32 v173, v205\n"
"v_mfma_f32_16x16x32_bf16 v[208:211], a[192:195], v[32:35], v[104:107]\n"
"v_cvt_pk_bf16_f32 v126, v172, v173\n"
"v_mfma_f32_16x16x32_bf16 v[208:211], a[196:199], v[36:39], v[208:211]\n"
"v_exp_f32 v174, v206\n"
"v_mfma_f32_16x16x32_bf16 v[208:211], a[200:203], v[40:43], v[208:211]\n"
"buffer_load_dwordx4 v188, s[40:43], s74 offen offset:2048 lds\n"
"v_or3_b32 v184, v184, v125, v126\n"
"v_mfma_f32_16x16x32_bf16 v[208:211], a[204:207], v[44:47], v[208:211]\n"
"v_exp_f32 v175, v207\n"
"v_mfma_f32_16x16x32_bf16 v[212:215], a[208:211], v[32:35], v[104:107]\n"
"v_cvt_pk_bf16_f32 v127, v174, v175\n"
"v_bitop3_b32 v184, v184, v127, s60 bitop3:0xa8\n"
"v_mfma_f32_16x16x32_bf16 v[212:215], a[212:215], v[36:39], v[212:215]\n"
"v_cmp_ne_u32 vcc, 0, v184\n"
"v_mfma_f32_16x16x32_bf16 v[212:215], a[216:219], v[40:43], v[212:215]\n"
"v_mfma_f32_16x16x32_bf16 v[212:215], a[220:223], v[44:47], v[212:215]\n"
"s_cbranch_vccnz .Lk1_tr116\n"
".Lk1_ct116:\n"
"v_exp_f32 v172, v208\n"
"v_mfma_f32_16x16x32_bf16 v[216:219], a[192:195], v[48:51], v[108:111]\n"
"buffer_load_dwordx4 v189, s[40:43], s74 offen offset:3072 lds\n"
"v_mfma_f32_16x16x32_bf16 v[216:219], a[196:199], v[52:55], v[216:219]\n"
"v_exp_f32 v173, v209\n"
"v_mfma_f32_16x16x32_bf16 v[216:219], a[200:203], v[56:59], v[216:219]\n"
"v_cvt_pk_bf16_f32 v128, v172, v173\n"
"v_mfma_f32_16x16x32_bf16 v[216:219], a[204:207], v[60:63], v[216:219]\n"
"v_exp_f32 v174, v210\n"
"v_mfma_f32_16x16x32_bf16 v[220:223], a[208:211], v[48:51], v[108:111]\n"
"v_mfma_f32_16x16x32_bf16 v[220:223], a[212:215], v[52:55], v[220:223]\n"
"v_exp_f32 v175, v211\n"
"v_mfma_f32_16x16x32_bf16 v[220:223], a[216:219], v[56:59], v[220:223]\n"
"s_waitcnt vmcnt(9)\n"
"v_cvt_pk_bf16_f32 v129, v174, v175\n"
"v_mfma_f32_16x16x32_bf16 v[220:223], a[220:223], v[60:63], v[220:223]\n"
"s_barrier\n"
"v_exp_f32 v172, v212\n"
"v_mfma_f32_16x16x32_bf16 a[128:131], a[224:227], v[136:139], a[128:131]\n"
"v_mfma_f32_16x16x32_bf16 a[160:163], a[224:227], v[140:143], a[160:163]\n"
"s_bfe_u32 s44, s86, 0x60007\n"
"v_exp_f32 v173, v213\n"
"v_mfma_f32_16x16x32_bf16 a[132:135], a[228:231], v[136:139], a[132:135]\n"
"s_mul_i32 s45, s67, 3072\n"
"ds_read_b64_tr_b16 a[224:225], v241 offset:0\n"
"v_cvt_pk_bf16_f32 v130, v172, v173\n"
"v_mfma_f32_16x16x32_bf16 a[164:167], a[228:231], v[140:143], a[164:167]\n"
"s_sub_u32 s45, 0x150000, s45\n"
"v_exp_f32 v174, v214\n"
"v_mfma_f32_16x16x32_bf16 a[136:139], a[232:235], v[136:139], a[136:139]\n"
"s_xor_b32 s46, s44, 0\n"
"ds_read_b64_tr_b16 a[226:227], v241 offset:256\n"
"v_or3_b32 v184, v128, v129, v130\n"
"v_mfma_f32_16x16x32_bf16 a[168:171], a[232:235], v[140:143], a[168:171]\n"
"s_mul_i32 s46, s46, 12288\n"
"v_exp_f32 v175, v215\n"
"v_mfma_f32_16x16x32_bf16 a[140:143], a[236:239], v[136:139], a[140:143]\n"
"s_add_u32 s58, s46, s45\n"
"ds_read_b64_tr_b16 a[228:229], v242 offset:0\n"
"v_cvt_pk_bf16_f32 v131, v174, v175\n"
"v_mfma_f32_16x16x32_bf16 a[172:175], a[236:239], v[140:143], a[172:175]\n"
"s_xor_b32 s46, s44, 4\n"
"v_exp_f32 v172, v216\n"
"v_mfma_f32_16x16x32_bf16 a[144:147], a[240:243], v[136:139], a[144:147]\n"
"s_mul_i32 s46, s46, 12288\n"
"ds_read_b64_tr_b16 a[230:231], v242 offset:256\n"
"ds_read_b64_tr_b16 a[232:233], v241 offset:512\n"
"v_mfma_f32_16x16x32_bf16 a[176:179], a[240:243], v[140:143], a[176:179]\n"
"s_add_u32 s77, s46, s45\n"
"v_exp_f32 v173, v217\n"
"v_mfma_f32_16x16x32_bf16 a[148:151], a[244:247], v[136:139], a[148:151]\n"
"s_xor_b32 s46, s44, 6\n"
"ds_read_b64_tr_b16 a[234:235], v241 offset:768\n"
"v_cvt_pk_bf16_f32 v132, v172, v173\n"
"v_mfma_f32_16x16x32_bf16 a[180:183], a[244:247], v[140:143], a[180:183]\n"
"s_mul_i32 s46, s46, 12288\n"
"v_exp_f32 v174, v218\n"
"v_mfma_f32_16x16x32_bf16 a[152:155], a[248:251], v[136:139], a[152:155]\n"
"s_add_u32 s78, s46, s45\n"
"ds_read_b64_tr_b16 a[236:237], v242 offset:512\n"
"v_or3_b32 v184, v184, v131, v132\n"
"v_mfma_f32_16x16x32_bf16 a[184:187], a[248:251], v[140:143], a[184:187]\n"
"v_exp_f32 v175, v219\n"
"v_mfma_f32_16x16x32_bf16 a[156:159], a[252:255], v[136:139], a[156:159]\n"
"ds_read_b64_tr_b16 a[238:239], v242 offset:768\n"
"v_cvt_pk_bf16_f32 v133, v174, v175\n"
"v_mfma_f32_16x16x32_bf16 a[188:191], a[252:255], v[140:143], a[188:191]\n"
"v_exp_f32 v172, v220\n"
"v_mfma_f32_16x16x32_bf16 v[160:163], v[168:171], v[136:139], v[160:163]\n"
"ds_read_b64_tr_b16 a[240:241], v241 offset:1024\n"
"ds_read_b64_tr_b16 a[242:243], v241 offset:1280\n"
"v_mfma_f32_16x16x32_bf16 v[164:167], v[168:171], v[140:143], v[164:167]\n"
"v_exp_f32 v173, v221\n"
"v_mfma_f32_16x16x32_bf16 v[224:227], a[192:195], v[64:67], v[112:115]\n"
"ds_read_b64_tr_b16 a[244:245], v242 offset:1024\n"
"v_cvt_pk_bf16_f32 v134, v172, v173\n"
"v_mfma_f32_16x16x32_bf16 v[224:227], a[196:199], v[68:71], v[224:227]\n"
"v_exp_f32 v174, v222\n"
"v_mfma_f32_16x16x32_bf16 v[224:227], a[200:203], v[72:75], v[224:227]\n"
"ds_read_b64_tr_b16 a[246:247], v242 offset:1280\n"
"v_or3_b32 v184, v184, v133, v134\n"
"v_mfma_f32_16x16x32_bf16 v[224:227], a[204:207], v[76:79], v[224:227]\n"
"v_exp_f32 v175, v223\n"
"v_mfma_f32_16x16x32_bf16 v[228:231], a[208:211], v[64:67], v[112:115]\n"
"v_cvt_pk_bf16_f32 v135, v174, v175\n"
"v_bitop3_b32 v184, v184, v135, s60 bitop3:0xa8\n"
"v_mfma_f32_16x16x32_bf16 v[228:231], a[212:215], v[68:71], v[228:231]\n"
"ds_read_b64_tr_b16 a[248:249], v241 offset:1536\n"
"v_cmp_ne_u32 vcc, 0, v184\n"
"v_mfma_f32_16x16x32_bf16 v[228:231], a[216:219], v[72:75], v[228:231]\n"
"ds_read_b64_tr_b16 a[250:251], v241 offset:1792\n"
"ds_read_b64_tr_b16 a[252:253], v242 offset:1536\n"
"v_mfma_f32_16x16x32_bf16 v[228:231], a[220:223], v[76:79], v[228:231]\n"
"s_cbranch_vccnz .Lk1_tr117\n"
".Lk1_ct117:\n"
"v_exp_f32 v172, v224\n"
"v_mfma_f32_16x16x32_bf16 v[232:235], a[192:195], v[80:83], v[116:119]\n"
"ds_read_b64_tr_b16 a[254:255], v242 offset:1792\n"
"ds_read_b128 a[192:195], v240 offset:8192\n"
"v_mfma_f32_16x16x32_bf16 v[232:235], a[196:199], v[84:87], v[232:235]\n"
"v_exp_f32 v173, v225\n"
"v_mfma_f32_16x16x32_bf16 v[232:235], a[200:203], v[88:91], v[232:235]\n"
"ds_read_b128 a[196:199], v240 offset:8704\n"
"v_cvt_pk_bf16_f32 v136, v172, v173\n"
"v_mfma_f32_16x16x32_bf16 v[232:235], a[204:207], v[92:95], v[232:235]\n"
"v_exp_f32 v174, v226\n"
"v_mfma_f32_16x16x32_bf16 v[236:239], a[208:211], v[80:83], v[116:119]\n"
"ds_read_b128 a[200:203], v240 offset:9216\n"
"ds_read_b128 a[204:207], v240 offset:9728\n"
"v_mfma_f32_16x16x32_bf16 v[236:239], a[212:215], v[84:87], v[236:239]\n"
"v_exp_f32 v175, v227\n"
"v_mfma_f32_16x16x32_bf16 v[236:239], a[216:219], v[88:91], v[236:239]\n"
"s_add_u32 s73, s94, 0x100\n"
"ds_read_b128 a[208:211], v240 offset:8448\n"
"v_cvt_pk_bf16_f32 v137, v174, v175\n"
"v_mfma_f32_16x16x32_bf16 v[236:239], a[220:223], v[92:95], v[236:239]\n"
"s_waitcnt lgkmcnt(13)\n"
"v_exp_f32 v172, v228\n"
"v_mfma_f32_16x16x32_bf16 a[0:3], a[224:227], v[120:123], a[0:3]\n"
"s_and_b32 s73, s73, 0x380\n"
"ds_read_b128 a[212:215], v240 offset:8960\n"
"ds_read_b128 a[216:219], v240 offset:9472\n"
"v_mfma_f32_16x16x32_bf16 a[32:35], a[224:227], v[124:127], a[32:35]\n"
"s_add_u32 s73, s73, s96\n"
"v_exp_f32 v173, v229\n"
"v_mfma_f32_16x16x32_bf16 a[4:7], a[228:231], v[120:123], a[4:7]\n"
"s_add_u32 m0, s68, 0x20000\n"
"ds_read_b128 a[220:223], v240 offset:9984\n"
"v_cvt_pk_bf16_f32 v138, v172, v173\n"
"v_mfma_f32_16x16x32_bf16 a[36:39], a[228:231], v[124:127], a[36:39]\n"
"s_add_u32 s74, s73, 0x0\n"
"v_exp_f32 v174, v230\n"
"v_mfma_f32_16x16x32_bf16 a[8:11], a[232:235], v[120:123], a[8:11]\n"
"s_mov_b64 exec, 0x37f\n"
"buffer_store_dword v254, v253, s[52:55], 0 offen offset:8 sc1\n"
"s_mov_b64 exec, -1\n"
"buffer_load_dwordx4 v188, s[40:43], s74 offen offset:0 lds\n"
"v_or3_b32 v184, v136, v137, v138\n"
"v_mfma_f32_16x16x32_bf16 a[40:43], a[232:235], v[124:127], a[40:43]\n"
"v_exp_f32 v175, v231\n"
"v_mfma_f32_16x16x32_bf16 a[12:15], a[236:239], v[120:123], a[12:15]\n"
"v_cvt_pk_bf16_f32 v139, v174, v175\n"
"v_mfma_f32_16x16x32_bf16 a[44:47], a[236:239], v[124:127], a[44:47]\n"
"s_waitcnt lgkmcnt(8)\n"
"v_exp_f32 v172, v232\n"
"v_mfma_f32_16x16x32_bf16 a[16:19], a[240:243], v[120:123], a[16:19]\n"
"buffer_load_dwordx4 v189, s[40:43], s74 offen offset:1024 lds\n"
"v_add_u32 v254, 1, v254\n"
"v_mfma_f32_16x16x32_bf16 a[48:51], a[240:243], v[124:127], a[48:51]\n"
"s_add_u32 s74, s73, 0x3800\n"
"v_exp_f32 v173, v233\n"
"v_mfma_f32_16x16x32_bf16 a[20:23], a[244:247], v[120:123], a[20:23]\n"
"v_cvt_pk_bf16_f32 v140, v172, v173\n"
"v_mfma_f32_16x16x32_bf16 a[52:55], a[244:247], v[124:127], a[52:55]\n"
"v_exp_f32 v174, v234\n"
"v_mfma_f32_16x16x32_bf16 a[24:27], a[248:251], v[120:123], a[24:27]\n"
"buffer_load_dwordx4 v188, s[40:43], s74 offen offset:2048 lds\n"
"v_or3_b32 v184, v184, v139, v140\n"
"v_mfma_f32_16x16x32_bf16 a[56:59], a[248:251], v[124:127], a[56:59]\n"
"v_exp_f32 v175, v235\n"
"v_mfma_f32_16x16x32_bf16 a[28:31], a[252:255], v[120:123], a[28:31]\n"
"v_cvt_pk_bf16_f32 v141, v174, v175\n"
"v_mfma_f32_16x16x32_bf16 a[60:63], a[252:255], v[124:127], a[60:63]\n"
"v_exp_f32 v172, v236\n"
"v_mfma_f32_16x16x32_bf16 v[144:147], v[168:171], v[120:123], v[144:147]\n"
"buffer_load_dwordx4 v189, s[40:43], s74 offen offset:3072 lds\n"
"v_mfma_f32_16x16x32_bf16 v[148:151], v[168:171], v[124:127], v[148:151]\n"
"s_waitcnt lgkmcnt(4)\n"
"v_exp_f32 v173, v237\n"
"v_mfma_f32_16x16x32_bf16 v[192:195], a[192:195], v[0:3], v[96:99]\n"
"v_cvt_pk_bf16_f32 v142, v172, v173\n"
"v_mfma_f32_16x16x32_bf16 v[192:195], a[196:199], v[4:7], v[192:195]\n"
"v_exp_f32 v174, v238\n"
"v_mfma_f32_16x16x32_bf16 v[192:195], a[200:203], v[8:11], v[192:195]\n"
"s_add_u32 m0, s68, 0x21000\n"
"s_add_u32 s74, s73, 0x8000\n"
"v_or3_b32 v184, v184, v141, v142\n"
"v_mfma_f32_16x16x32_bf16 v[192:195], a[204:207], v[12:15], v[192:195]\n"
"s_waitcnt lgkmcnt(0)\n"
"v_exp_f32 v175, v239\n"
"v_mfma_f32_16x16x32_bf16 v[196:199], a[208:211], v[0:3], v[96:99]\n"
"v_cvt_pk_bf16_f32 v143, v174, v175\n"
"v_bitop3_b32 v184, v184, v143, s60 bitop3:0xa8\n"
"v_mfma_f32_16x16x32_bf16 v[196:199], a[212:215], v[4:7], v[196:199]\n"
"v_cmp_ne_u32 vcc, 0, v184\n"
"v_mfma_f32_16x16x32_bf16 v[196:199], a[216:219], v[8:11], v[196:199]\n"
"buffer_load_dwordx4 v188, s[40:43], s74 offen offset:0 lds\n"
"v_mfma_f32_16x16x32_bf16 v[196:199], a[220:223], v[12:15], v[196:199]\n"
"s_cbranch_vccnz .Lk1_tr118\n"
".Lk1_ct118:\n"
"v_exp_f32 v172, v192\n"
"v_mfma_f32_16x16x32_bf16 v[200:203], a[192:195], v[16:19], v[100:103]\n"
"v_mfma_f32_16x16x32_bf16 v[200:203], a[196:199], v[20:23], v[200:203]\n"
"v_exp_f32 v173, v193\n"
"v_mfma_f32_16x16x32_bf16 v[200:203], a[200:203], v[24:27], v[200:203]\n"
"v_cvt_pk_bf16_f32 v120, v172, v173\n"
"v_mfma_f32_16x16x32_bf16 v[200:203], a[204:207], v[28:31], v[200:203]\n"
"v_exp_f32 v174, v194\n"
"v_mfma_f32_16x16x32_bf16 v[204:207], a[208:211], v[16:19], v[100:103]\n"
"buffer_load_dwordx4 v189, s[40:43], s74 offen offset:1024 lds\n"
"v_mfma_f32_16x16x32_bf16 v[204:207], a[212:215], v[20:23], v[204:207]\n"
"v_exp_f32 v175, v195\n"
"v_mfma_f32_16x16x32_bf16 v[204:207], a[216:219], v[24:27], v[204:207]\n"
"s_add_u32 s74, s73, 0xb800\n"
"v_cvt_pk_bf16_f32 v121, v174, v175\n"
"v_mfma_f32_16x16x32_bf16 v[204:207], a[220:223], v[28:31], v[204:207]\n"
"v_exp_f32 v172, v196\n"
"v_mfma_f32_16x16x32_bf16 a[64:67], a[224:227], v[128:131], a[64:67]\n"
"v_mfma_f32_16x16x32_bf16 a[96:99], a[224:227], v[132:135], a[96:99]\n"
"v_exp_f32 v173, v197\n"
"v_mfma_f32_16x16x32_bf16 a[68:71], a[228:231], v[128:131], a[68:71]\n"
"buffer_load_dwordx4 v188, s[40:43], s74 offen offset:2048 lds\n"
"v_cvt_pk_bf16_f32 v122, v172, v173\n"
"v_mfma_f32_16x16x32_bf16 a[100:103], a[228:231], v[132:135], a[100:103]\n"
"v_exp_f32 v174, v198\n"
"v_mfma_f32_16x16x32_bf16 a[72:75], a[232:235], v[128:131], a[72:75]\n"
"v_or3_b32 v184, v120, v121, v122\n"
"v_mfma_f32_16x16x32_bf16 a[104:107], a[232:235], v[132:135], a[104:107]\n"
"v_exp_f32 v175, v199\n"
"v_mfma_f32_16x16x32_bf16 a[76:79], a[236:239], v[128:131], a[76:79]\n"
"v_cvt_pk_bf16_f32 v123, v174, v175\n"
"v_mfma_f32_16x16x32_bf16 a[108:111], a[236:239], v[132:135], a[108:111]\n"
"v_exp_f32 v172, v200\n"
"v_mfma_f32_16x16x32_bf16 a[80:83], a[240:243], v[128:131], a[80:83]\n"
"buffer_load_dwordx4 v189, s[40:43], s74 offen offset:3072 lds\n"
"v_mfma_f32_16x16x32_bf16 a[112:115], a[240:243], v[132:135], a[112:115]\n"
"v_exp_f32 v173, v201\n"
"v_mfma_f32_16x16x32_bf16 a[84:87], a[244:247], v[128:131], a[84:87]\n"
"v_cvt_pk_bf16_f32 v124, v172, v173\n"
"v_mfma_f32_16x16x32_bf16 a[116:119], a[244:247], v[132:135], a[116:119]\n"
"v_exp_f32 v174, v202\n"
"v_mfma_f32_16x16x32_bf16 a[88:91], a[248:251], v[128:131], a[88:91]\n"
"v_or3_b32 v184, v184, v123, v124\n"
"v_mfma_f32_16x16x32_bf16 a[120:123], a[248:251], v[132:135], a[120:123]\n"
"v_exp_f32 v175, v203\n"
"v_mfma_f32_16x16x32_bf16 a[92:95], a[252:255], v[128:131], a[92:95]\n"
"v_cvt_pk_bf16_f32 v125, v174, v175\n"
"v_mfma_f32_16x16x32_bf16 a[124:127], a[252:255], v[132:135], a[124:127]\n"
"v_exp_f32 v172, v204\n"
"v_mfma_f32_16x16x32_bf16 v[152:155], v[168:171], v[128:131], v[152:155]\n"
"v_mfma_f32_16x16x32_bf16 v[156:159], v[168:171], v[132:135], v[156:159]\n"
"v_exp_f32 v173, v205\n"
"v_mfma_f32_16x16x32_bf16 a[128:131], a[224:227], v[136:139], a[128:131]\n"
"v_cvt_pk_bf16_f32 v126, v172, v173\n"
"v_mfma_f32_16x16x32_bf16 a[160:163], a[224:227], v[140:143], a[160:163]\n"
"v_exp_f32 v174, v206\n"
"v_mfma_f32_16x16x32_bf16 a[132:135], a[228:231], v[136:139], a[132:135]\n"
"ds_read_b64_tr_b16 a[224:225], v241 offset:8192\n"
"v_or3_b32 v184, v184, v125, v126\n"
"v_mfma_f32_16x16x32_bf16 a[164:167], a[228:231], v[140:143], a[164:167]\n"
"v_exp_f32 v175, v207\n"
"v_mfma_f32_16x16x32_bf16 a[136:139], a[232:235], v[136:139], a[136:139]\n"
"v_cvt_pk_bf16_f32 v127, v174, v175\n"
"v_bitop3_b32 v184, v184, v127, s60 bitop3:0xa8\n"
"v_mfma_f32_16x16x32_bf16 a[168:171], a[232:235], v[140:143], a[168:171]\n"
"ds_read_b64_tr_b16 a[226:227], v241 offset:8448\n"
"v_cmp_ne_u32 vcc, 0, v184\n"
"v_mfma_f32_16x16x32_bf16 a[140:143], a[236:239], v[136:139], a[140:143]\n"
"ds_read_b64_tr_b16 a[228:229], v242 offset:8192\n"
"ds_read_b64_tr_b16 a[230:231], v242 offset:8448\n"
"v_mfma_f32_16x16x32_bf16 a[172:175], a[236:239], v[140:143], a[172:175]\n"
"s_cbranch_vccnz .Lk1_tr119\n"
".Lk1_ct119:\n"
"ds_read_b64_tr_b16 a[232:233], v241 offset:8704\n"
"ds_read_b64_tr_b16 a[234:235], v241 offset:8960\n"
"v_mfma_f32_16x16x32_bf16 a[144:147], a[240:243], v[136:139], a[144:147]\n"
"ds_read_b64_tr_b16 a[236:237], v242 offset:8704\n"
"ds_read_b64_tr_b16 a[238:239], v242 offset:8960\n"
"v_mfma_f32_16x16x32_bf16 a[176:179], a[240:243], v[140:143], a[176:179]\n"
"ds_read_b64_tr_b16 a[240:241], v241 offset:9216\n"
"ds_read_b64_tr_b16 a[242:243], v241 offset:9472\n"
"v_mfma_f32_16x16x32_bf16 a[148:151], a[244:247], v[136:139], a[148:151]\n"
"v_mfma_f32_16x16x32_bf16 a[180:183], a[244:247], v[140:143], a[180:183]\n"
"ds_read_b64_tr_b16 a[244:245], v242 offset:9216\n"
"ds_read_b64_tr_b16 a[246:247], v242 offset:9472\n"
"v_mfma_f32_16x16x32_bf16 a[152:155], a[248:251], v[136:139], a[152:155]\n"
"v_mfma_f32_16x16x32_bf16 a[184:187], a[248:251], v[140:143], a[184:187]\n"
"ds_read_b64_tr_b16 a[248:249], v241 offset:9728\n"
"ds_read_b64_tr_b16 a[250:251], v241 offset:9984\n"
"v_mfma_f32_16x16x32_bf16 a[156:159], a[252:255], v[136:139], a[156:159]\n"
"v_mfma_f32_16x16x32_bf16 a[188:191], a[252:255], v[140:143], a[188:191]\n"
"ds_read_b64_tr_b16 a[252:253], v242 offset:9728\n"
"ds_read_b64_tr_b16 a[254:255], v242 offset:9984\n"
"v_mfma_f32_16x16x32_bf16 v[160:163], v[168:171], v[136:139], v[160:163]\n"
"v_mfma_f32_16x16x32_bf16 v[164:167], v[168:171], v[140:143], v[164:167]\n"
"v_mfma_f32_16x16x32_bf16 v[144:147], v[168:171], v[120:123], v[144:147]\n"
"v_mfma_f32_16x16x32_bf16 v[148:151], v[168:171], v[124:127], v[148:151]\n"
"s_waitcnt lgkmcnt(8)\n"
"v_mfma_f32_16x16x32_bf16 a[0:3], a[224:227], v[120:123], a[0:3]\n"
"v_mfma_f32_16x16x32_bf16 a[32:35], a[224:227], v[124:127], a[32:35]\n"
"v_rcp_f32 v182, v144\n"
"v_mfma_f32_16x16x32_bf16 a[4:7], a[228:231], v[120:123], a[4:7]\n"
"v_mfma_f32_16x16x32_bf16 a[36:39], a[228:231], v[124:127], a[36:39]\n"
"v_accvgpr_read_b32 v192, a0\n"
"v_accvgpr_read_b32 v193, a1\n"
"v_mfma_f32_16x16x32_bf16 a[8:11], a[232:235], v[120:123], a[8:11]\n"
"v_accvgpr_read_b32 v194, a2\n"
"v_accvgpr_read_b32 v195, a3\n"
"v_mfma_f32_16x16x32_bf16 a[40:43], a[232:235], v[124:127], a[40:43]\n"
"v_mov_b32 v183, v182\n"
"v_accvgpr_read_b32 v196, a4\n"
"v_mfma_f32_16x16x32_bf16 a[12:15], a[236:239], v[120:123], a[12:15]\n"
"v_accvgpr_read_b32 v197, a5\n"
"v_accvgpr_read_b32 v198, a6\n"
"v_accvgpr_read_b32 v199, a7\n"
"v_pk_mul_f32 v[192:193], v[192:193], v[182:183]\n"
"v_pk_mul_f32 v[194:195], v[194:195], v[182:183]\n"
"v_pk_mul_f32 v[196:197], v[196:197], v[182:183]\n"
"v_pk_mul_f32 v[198:199], v[198:199], v[182:183]\n"
"v_mfma_f32_16x16x32_bf16 a[44:47], a[236:239], v[124:127], a[44:47]\n"
"v_cvt_pk_bf16_f32 v0, v192, v193\n"
"v_cvt_pk_bf16_f32 v1, v194, v195\n"
"s_waitcnt lgkmcnt(0)\n"
"v_mfma_f32_16x16x32_bf16 a[16:19], a[240:243], v[120:123], a[16:19]\n"
"v_cvt_pk_bf16_f32 v2, v196, v197\n"
"v_cvt_pk_bf16_f32 v3, v198, v199\n"
"v_mfma_f32_16x16x32_bf16 a[48:51], a[240:243], v[124:127], a[48:51]\n"
"buffer_store_dwordx4 v[0:3], v252, s[48:51], s58 offen offset:0 sc1\n"
"v_mfma_f32_16x16x32_bf16 a[20:23], a[244:247], v[120:123], a[20:23]\n"
"v_accvgpr_read_b32 v200, a8\n"
"v_accvgpr_read_b32 v201, a9\n"
"v_mfma_f32_16x16x32_bf16 a[52:55], a[244:247], v[124:127], a[52:55]\n"
"v_accvgpr_read_b32 v202, a10\n"
"v_accvgpr_read_b32 v203, a11\n"
"v_mfma_f32_16x16x32_bf16 a[24:27], a[248:251], v[120:123], a[24:27]\n"
"v_accvgpr_read_b32 v204, a12\n"
"v_accvgpr_read_b32 v205, a13\n"
"v_mfma_f32_16x16x32_bf16 a[56:59], a[248:251], v[124:127], a[56:59]\n"
"v_accvgpr_read_b32 v206, a14\n"
"v_accvgpr_read_b32 v207, a15\n"
"v_pk_mul_f32 v[200:201], v[200:201], v[182:183]\n"
"v_pk_mul_f32 v[202:203], v[202:203], v[182:183]\n"
"v_pk_mul_f32 v[204:205], v[204:205], v[182:183]\n"
"v_pk_mul_f32 v[206:207], v[206:207], v[182:183]\n"
"v_mfma_f32_16x16x32_bf16 a[28:31], a[252:255], v[120:123], a[28:31]\n"
"v_cvt_pk_bf16_f32 v4, v200, v201\n"
"v_cvt_pk_bf16_f32 v5, v202, v203\n"
"v_mfma_f32_16x16x32_bf16 a[60:63], a[252:255], v[124:127], a[60:63]\n"
"v_cvt_pk_bf16_f32 v6, v204, v205\n"
"v_cvt_pk_bf16_f32 v7, v206, v207\n"
"v_mfma_f32_16x16x32_bf16 v[208:211], a[192:195], v[32:35], v[104:107]\n"
"buffer_store_dwordx4 v[4:7], v252, s[48:51], s58 offen offset:1024 sc1\n"
"v_mfma_f32_16x16x32_bf16 v[208:211], a[196:199], v[36:39], v[208:211]\n"
"v_accvgpr_read_b32 v192, a16\n"
"v_accvgpr_read_b32 v193, a17\n"
"v_mfma_f32_16x16x32_bf16 v[208:211], a[200:203], v[40:43], v[208:211]\n"
"v_accvgpr_read_b32 v194, a18\n"
"v_accvgpr_read_b32 v195, a19\n"
"v_mfma_f32_16x16x32_bf16 v[208:211], a[204:207], v[44:47], v[208:211]\n"
"v_accvgpr_read_b32 v196, a20\n"
"v_accvgpr_read_b32 v197, a21\n"
"v_mfma_f32_16x16x32_bf16 v[212:215], a[208:211], v[32:35], v[104:107]\n"
"v_accvgpr_read_b32 v198, a22\n"
"v_accvgpr_read_b32 v199, a23\n"
"v_pk_mul_f32 v[192:193], v[192:193], v[182:183]\n"
"v_pk_mul_f32 v[194:195], v[194:195], v[182:183]\n"
"v_pk_mul_f32 v[196:197], v[196:197], v[182:183]\n"
"v_pk_mul_f32 v[198:199], v[198:199], v[182:183]\n"
"v_mfma_f32_16x16x32_bf16 v[212:215], a[212:215], v[36:39], v[212:215]\n"
"v_cvt_pk_bf16_f32 v8, v192, v193\n"
"v_cvt_pk_bf16_f32 v9, v194, v195\n"
"v_mfma_f32_16x16x32_bf16 v[212:215], a[216:219], v[40:43], v[212:215]\n"
"v_cvt_pk_bf16_f32 v10, v196, v197\n"
"v_cvt_pk_bf16_f32 v11, v198, v199\n"
"v_mfma_f32_16x16x32_bf16 v[212:215], a[220:223], v[44:47], v[212:215]\n"
"buffer_store_dwordx4 v[8:11], v252, s[48:51], s58 offen offset:2048 sc1\n"
"v_mfma_f32_16x16x32_bf16 v[216:219], a[192:195], v[48:51], v[108:111]\n"
"v_accvgpr_read_b32 v200, a24\n"
"v_accvgpr_read_b32 v201, a25\n"
"v_mfma_f32_16x16x32_bf16 v[216:219], a[196:199], v[52:55], v[216:219]\n"
"v_accvgpr_read_b32 v202, a26\n"
"v_accvgpr_read_b32 v203, a27\n"
"v_mfma_f32_16x16x32_bf16 v[216:219], a[200:203], v[56:59], v[216:219]\n"
"v_accvgpr_read_b32 v204, a28\n"
"v_accvgpr_read_b32 v205, a29\n"
"v_mfma_f32_16x16x32_bf16 v[216:219], a[204:207], v[60:63], v[216:219]\n"
"v_accvgpr_read_b32 v206, a30\n"
"v_accvgpr_read_b32 v207, a31\n"
"v_pk_mul_f32 v[200:201], v[200:201], v[182:183]\n"
"v_pk_mul_f32 v[202:203], v[202:203], v[182:183]\n"
"v_pk_mul_f32 v[204:205], v[204:205], v[182:183]\n"
"v_pk_mul_f32 v[206:207], v[206:207], v[182:183]\n"
"v_mfma_f32_16x16x32_bf16 v[220:223], a[208:211], v[48:51], v[108:111]\n"
"v_cvt_pk_bf16_f32 v12, v200, v201\n"
"v_cvt_pk_bf16_f32 v13, v202, v203\n"
"v_mfma_f32_16x16x32_bf16 v[220:223], a[212:215], v[52:55], v[220:223]\n"
"v_cvt_pk_bf16_f32 v14, v204, v205\n"
"v_cvt_pk_bf16_f32 v15, v206, v207\n"
"v_mfma_f32_16x16x32_bf16 v[220:223], a[216:219], v[56:59], v[220:223]\n"
"v_exp_f32 v172, v208\n"
"v_mfma_f32_16x16x32_bf16 v[220:223], a[220:223], v[60:63], v[220:223]\n"
"v_exp_f32 v173, v209\n"
"v_mfma_f32_16x16x32_bf16 v[224:227], a[192:195], v[64:67], v[112:115]\n"
"v_exp_f32 v174, v210\n"
"v_mfma_f32_16x16x32_bf16 v[224:227], a[196:199], v[68:71], v[224:227]\n"
"buffer_store_dwordx4 v[12:15], v252, s[48:51], s58 offen offset:3072 sc1\n"
"v_mfma_f32_16x16x32_bf16 v[224:227], a[200:203], v[72:75], v[224:227]\n"
"v_exp_f32 v175, v211\n"
"v_mfma_f32_16x16x32_bf16 v[224:227], a[204:207], v[76:79], v[224:227]\n"
"v_cvt_pk_bf16_f32 v128, v172, v173\n"
"v_exp_f32 v172, v212\n"
"v_mfma_f32_16x16x32_bf16 v[228:231], a[208:211], v[64:67], v[112:115]\n"
"v_exp_f32 v173, v213\n"
"v_mfma_f32_16x16x32_bf16 v[228:231], a[212:215], v[68:71], v[228:231]\n"
"v_cvt_pk_bf16_f32 v129, v174, v175\n"
"v_exp_f32 v174, v214\n"
"v_mfma_f32_16x16x32_bf16 v[228:231], a[216:219], v[72:75], v[228:231]\n"
"v_cvt_pk_bf16_f32 v130, v172, v173\n"
"v_or3_b32 v184, v128, v129, v130\n"
"v_mfma_f32_16x16x32_bf16 v[228:231], a[220:223], v[76:79], v[228:231]\n"
"v_exp_f32 v175, v215\n"
"v_mfma_f32_16x16x32_bf16 v[232:235], a[192:195], v[80:83], v[116:119]\n"
"v_exp_f32 v172, v216\n"
"v_mfma_f32_16x16x32_bf16 v[232:235], a[196:199], v[84:87], v[232:235]\n"
"v_exp_f32 v173, v217\n"
"v_mfma_f32_16x16x32_bf16 v[232:235], a[200:203], v[88:91], v[232:235]\n"
"v_cvt_pk_bf16_f32 v131, v174, v175\n"
"v_exp_f32 v174, v218\n"
"v_mfma_f32_16x16x32_bf16 v[232:235], a[204:207], v[92:95], v[232:235]\n"
"v_cvt_pk_bf16_f32 v132, v172, v173\n"
"v_or3_b32 v184, v184, v131, v132\n"
"v_mfma_f32_16x16x32_bf16 v[236:239], a[208:211], v[80:83], v[116:119]\n"
"v_exp_f32 v175, v219\n"
"v_mfma_f32_16x16x32_bf16 v[236:239], a[212:215], v[84:87], v[236:239]\n"
"v_exp_f32 v172, v220\n"
"v_mfma_f32_16x16x32_bf16 v[236:239], a[216:219], v[88:91], v[236:239]\n"
"v_exp_f32 v173, v221\n"
"v_mfma_f32_16x16x32_bf16 v[236:239], a[220:223], v[92:95], v[236:239]\n"
"v_cvt_pk_bf16_f32 v133, v174, v175\n"
"v_exp_f32 v174, v222\n"
"v_cvt_pk_bf16_f32 v134, v172, v173\n"
"v_or3_b32 v184, v184, v133, v134\n"
"v_exp_f32 v175, v223\n"
"s_nop 0\n"
"v_cvt_pk_bf16_f32 v135, v174, v175\n"
"v_bitop3_b32 v184, v184, v135, s60 bitop3:0xa8\n"
"v_cmp_ne_u32 vcc, 0, v184\n"
"v_rcp_f32 v182, v148\n"
"v_accvgpr_read_b32 v192, a32\n"
"s_cbranch_vccnz .Lk1_tr120\n"
".Lk1_ct120:\n"
"v_mfma_f32_16x16x32_bf16 v[152:155], v[168:171], v[128:131], v[152:155]\n"
"v_accvgpr_read_b32 v193, a33\n"
"v_accvgpr_read_b32 v194, a34\n"
"v_mfma_f32_16x16x32_bf16 v[156:159], v[168:171], v[132:135], v[156:159]\n"
"v_accvgpr_read_b32 v195, a35\n"
"v_mov_b32 v183, v182\n"
"v_mfma_f32_16x16x32_bf16 a[64:67], a[224:227], v[128:131], a[64:67]\n"
"v_accvgpr_read_b32 v196, a36\n"
"v_accvgpr_read_b32 v197, a37\n"
"v_mfma_f32_16x16x32_bf16 a[96:99], a[224:227], v[132:135], a[96:99]\n"
"v_accvgpr_read_b32 v198, a38\n"
"v_accvgpr_read_b32 v199, a39\n"
"v_pk_mul_f32 v[192:193], v[192:193], v[182:183]\n"
"v_pk_mul_f32 v[194:195], v[194:195], v[182:183]\n"
"v_pk_mul_f32 v[196:197], v[196:197], v[182:183]\n"
"v_pk_mul_f32 v[198:199], v[198:199], v[182:183]\n"
"v_mfma_f32_16x16x32_bf16 a[68:71], a[228:231], v[128:131], a[68:71]\n"
"v_cvt_pk_bf16_f32 v16, v192, v193\n"
"v_cvt_pk_bf16_f32 v17, v194, v195\n"
"v_mfma_f32_16x16x32_bf16 a[100:103], a[228:231], v[132:135], a[100:103]\n"
"v_cvt_pk_bf16_f32 v18, v196, v197\n"
"v_cvt_pk_bf16_f32 v19, v198, v199\n"
"v_mfma_f32_16x16x32_bf16 a[72:75], a[232:235], v[128:131], a[72:75]\n"
"s_add_u32 s58, s58, 4096\n"
"buffer_store_dwordx4 v[16:19], v252, s[48:51], s58 offen offset:0 sc1\n"
"v_mfma_f32_16x16x32_bf16 a[104:107], a[232:235], v[132:135], a[104:107]\n"
"v_accvgpr_read_b32 v200, a40\n"
"v_accvgpr_read_b32 v201, a41\n"
"v_mfma_f32_16x16x32_bf16 a[76:79], a[236:239], v[128:131], a[76:79]\n"
"v_accvgpr_read_b32 v202, a42\n"
"v_accvgpr_read_b32 v203, a43\n"
"v_mfma_f32_16x16x32_bf16 a[108:111], a[236:239], v[132:135], a[108:111]\n"
"v_accvgpr_read_b32 v204, a44\n"
"v_accvgpr_read_b32 v205, a45\n"
"v_mfma_f32_16x16x32_bf16 a[80:83], a[240:243], v[128:131], a[80:83]\n"
"v_accvgpr_read_b32 v206, a46\n"
"v_accvgpr_read_b32 v207, a47\n"
"v_pk_mul_f32 v[200:201], v[200:201], v[182:183]\n"
"v_pk_mul_f32 v[202:203], v[202:203], v[182:183]\n"
"v_pk_mul_f32 v[204:205], v[204:205], v[182:183]\n"
"v_pk_mul_f32 v[206:207], v[206:207], v[182:183]\n"
"v_mfma_f32_16x16x32_bf16 a[112:115], a[240:243], v[132:135], a[112:115]\n"
"v_cvt_pk_bf16_f32 v20, v200, v201\n"
"v_cvt_pk_bf16_f32 v21, v202, v203\n"
"v_mfma_f32_16x16x32_bf16 a[84:87], a[244:247], v[128:131], a[84:87]\n"
"v_cvt_pk_bf16_f32 v22, v204, v205\n"
"v_cvt_pk_bf16_f32 v23, v206, v207\n"
"v_mfma_f32_16x16x32_bf16 a[116:119], a[244:247], v[132:135], a[116:119]\n"
"buffer_store_dwordx4 v[20:23], v252, s[48:51], s58 offen offset:1024 sc1\n"
"v_mfma_f32_16x16x32_bf16 a[88:91], a[248:251], v[128:131], a[88:91]\n"
"v_accvgpr_read_b32 v192, a48\n"
"v_accvgpr_read_b32 v193, a49\n"
"v_mfma_f32_16x16x32_bf16 a[120:123], a[248:251], v[132:135], a[120:123]\n"
"v_accvgpr_read_b32 v194, a50\n"
"v_accvgpr_read_b32 v195, a51\n"
"v_mfma_f32_16x16x32_bf16 a[92:95], a[252:255], v[128:131], a[92:95]\n"
"v_accvgpr_read_b32 v196, a52\n"
"v_accvgpr_read_b32 v197, a53\n"
"v_mfma_f32_16x16x32_bf16 a[124:127], a[252:255], v[132:135], a[124:127]\n"
"v_accvgpr_read_b32 v198, a54\n"
"v_accvgpr_read_b32 v199, a55\n"
"v_pk_mul_f32 v[192:193], v[192:193], v[182:183]\n"
"v_pk_mul_f32 v[194:195], v[194:195], v[182:183]\n"
"v_pk_mul_f32 v[196:197], v[196:197], v[182:183]\n"
"v_pk_mul_f32 v[198:199], v[198:199], v[182:183]\n"
"v_cvt_pk_bf16_f32 v24, v192, v193\n"
"v_cvt_pk_bf16_f32 v25, v194, v195\n"
"v_cvt_pk_bf16_f32 v26, v196, v197\n"
"v_cvt_pk_bf16_f32 v27, v198, v199\n"
"buffer_store_dwordx4 v[24:27], v252, s[48:51], s58 offen offset:2048 sc1\n"
"v_accvgpr_read_b32 v200, a56\n"
"v_accvgpr_read_b32 v201, a57\n"
"v_accvgpr_read_b32 v202, a58\n"
"v_accvgpr_read_b32 v203, a59\n"
"v_accvgpr_read_b32 v204, a60\n"
"v_accvgpr_read_b32 v205, a61\n"
"v_accvgpr_read_b32 v206, a62\n"
"v_accvgpr_read_b32 v207, a63\n"
"v_pk_mul_f32 v[200:201], v[200:201], v[182:183]\n"
"v_pk_mul_f32 v[202:203], v[202:203], v[182:183]\n"
"v_pk_mul_f32 v[204:205], v[204:205], v[182:183]\n"
"v_pk_mul_f32 v[206:207], v[206:207], v[182:183]\n"
"v_cvt_pk_bf16_f32 v28, v200, v201\n"
"v_cvt_pk_bf16_f32 v29, v202, v203\n"
"v_cvt_pk_bf16_f32 v30, v204, v205\n"
"v_cvt_pk_bf16_f32 v31, v206, v207\n"
"ds_read_b64_tr_b16 a[192:193], v246 offset:0\n"
"ds_read_b64_tr_b16 a[194:195], v246 offset:2048\n"
"ds_read_b64_tr_b16 a[196:197], v247 offset:0\n"
"ds_read_b64_tr_b16 a[198:199], v247 offset:2048\n"
"ds_read_b64_tr_b16 a[200:201], v246 offset:4096\n"
"ds_read_b64_tr_b16 a[202:203], v246 offset:6144\n"
"ds_read_b64_tr_b16 a[204:205], v247 offset:4096\n"
"ds_read_b64_tr_b16 a[206:207], v247 offset:6144\n"
"ds_read_b64_tr_b16 a[208:209], v246 offset:8192\n"
"ds_read_b64_tr_b16 a[210:211], v246 offset:10240\n"
"ds_read_b64_tr_b16 a[212:213], v247 offset:8192\n"
"ds_read_b64_tr_b16 a[214:215], v247 offset:10240\n"
"ds_read_b64_tr_b16 a[216:217], v246 offset:12288\n"
"ds_read_b64_tr_b16 a[218:219], v246 offset:14336\n"
"ds_read_b64_tr_b16 a[220:221], v247 offset:12288\n"
"ds_read_b64_tr_b16 a[222:223], v247 offset:14336\n"
"ds_read_b64_tr_b16 a[32:33], v248 offset:0\n"
"ds_read_b64_tr_b16 a[34:35], v248 offset:2048\n"
"ds_read_b64_tr_b16 a[36:37], v249 offset:0\n"
"ds_read_b64_tr_b16 a[38:39], v249 offset:2048\n"
"ds_read_b64_tr_b16 a[40:41], v248 offset:4096\n"
"ds_read_b64_tr_b16 a[42:43], v248 offset:6144\n"
"ds_read_b64_tr_b16 a[44:45], v249 offset:4096\n"
"ds_read_b64_tr_b16 a[46:47], v249 offset:6144\n"
"buffer_store_dwordx4 v[28:31], v252, s[48:51], s58 offen offset:3072 sc1\n"
"ds_read_b64_tr_b16 a[48:49], v248 offset:8192\n"
"ds_read_b64_tr_b16 a[50:51], v248 offset:10240\n"
"ds_read_b64_tr_b16 a[52:53], v249 offset:8192\n"
"ds_read_b64_tr_b16 a[54:55], v249 offset:10240\n"
"ds_read_b64_tr_b16 a[56:57], v248 offset:12288\n"
"ds_read_b64_tr_b16 a[58:59], v248 offset:14336\n"
"ds_read_b64_tr_b16 a[60:61], v249 offset:12288\n"
"ds_read_b64_tr_b16 a[62:63], v249 offset:14336\n"
"v_rcp_f32 v182, v152\n"
"v_accvgpr_read_b32 v192, a64\n"
"v_accvgpr_read_b32 v193, a65\n"
"v_accvgpr_read_b32 v194, a66\n"
"v_accvgpr_read_b32 v195, a67\n"
"v_mov_b32 v183, v182\n"
"v_accvgpr_read_b32 v196, a68\n"
"v_accvgpr_read_b32 v197, a69\n"
"v_accvgpr_read_b32 v198, a70\n"
"v_accvgpr_read_b32 v199, a71\n"
"v_pk_mul_f32 v[192:193], v[192:193], v[182:183]\n"
"v_pk_mul_f32 v[194:195], v[194:195], v[182:183]\n"
"v_pk_mul_f32 v[196:197], v[196:197], v[182:183]\n"
"v_pk_mul_f32 v[198:199], v[198:199], v[182:183]\n"
"v_cvt_pk_bf16_f32 v32, v192, v193\n"
"v_cvt_pk_bf16_f32 v33, v194, v195\n"
"v_cvt_pk_bf16_f32 v34, v196, v197\n"
"v_cvt_pk_bf16_f32 v35, v198, v199\n"
"s_add_u32 s58, s58, 4096\n"
"v_accvgpr_read_b32 v200, a72\n"
"s_waitcnt vmcnt(17)\n"
"s_waitcnt lgkmcnt(0)\n"
"s_barrier\n"
"s_add_u32 s73, s94, 0x180\n"
"s_and_b32 s73, s73, 0x380\n"
"s_add_u32 s73, s73, s96\n"
"buffer_store_dwordx4 v[32:35], v252, s[48:51], s58 offen offset:0 sc1\n"
"s_add_u32 m0, s68, 0x10000\n"
"s_add_u32 s74, s73, 0x0\n"
"v_accvgpr_read_b32 v201, a73\n"
"buffer_load_dwordx4 v188, s[40:43], s74 offen offset:0 lds\n"
"v_accvgpr_read_b32 v202, a74\n"
"v_accvgpr_read_b32 v203, a75\n"
"v_accvgpr_read_b32 v204, a76\n"
"v_accvgpr_read_b32 v205, a77\n"
"v_accvgpr_read_b32 v206, a78\n"
"v_accvgpr_read_b32 v207, a79\n"
"v_pk_mul_f32 v[200:201], v[200:201], v[182:183]\n"
"v_pk_mul_f32 v[202:203], v[202:203], v[182:183]\n"
"v_pk_mul_f32 v[204:205], v[204:205], v[182:183]\n"
"buffer_load_dwordx4 v189, s[40:43], s74 offen offset:1024 lds\n"
"s_add_u32 s74, s73, 0x3800\n"
"v_pk_mul_f32 v[206:207], v[206:207], v[182:183]\n"
"v_cvt_pk_bf16_f32 v36, v200, v201\n"
"v_cvt_pk_bf16_f32 v37, v202, v203\n"
"v_cvt_pk_bf16_f32 v38, v204, v205\n"
"v_cvt_pk_bf16_f32 v39, v206, v207\n"
"v_accvgpr_read_b32 v192, a80\n"
"v_accvgpr_read_b32 v193, a81\n"
"v_accvgpr_read_b32 v194, a82\n"
"v_accvgpr_read_b32 v195, a83\n"
"v_accvgpr_read_b32 v196, a84\n"
"v_accvgpr_read_b32 v197, a85\n"
"v_accvgpr_read_b32 v198, a86\n"
"v_accvgpr_read_b32 v199, a87\n"
"v_pk_mul_f32 v[192:193], v[192:193], v[182:183]\n"
"v_pk_mul_f32 v[194:195], v[194:195], v[182:183]\n"
"v_pk_mul_f32 v[196:197], v[196:197], v[182:183]\n"
"v_pk_mul_f32 v[198:199], v[198:199], v[182:183]\n"
"v_cvt_pk_bf16_f32 v40, v192, v193\n"
"buffer_load_dwordx4 v188, s[40:43], s74 offen offset:2048 lds\n"
"v_cvt_pk_bf16_f32 v41, v194, v195\n"
"v_cvt_pk_bf16_f32 v42, v196, v197\n"
"v_cvt_pk_bf16_f32 v43, v198, v199\n"
"v_accvgpr_read_b32 v200, a88\n"
"v_accvgpr_read_b32 v201, a89\n"
"v_accvgpr_read_b32 v202, a90\n"
"v_accvgpr_read_b32 v203, a91\n"
"buffer_store_dwordx4 v[36:39], v252, s[48:51], s58 offen offset:1024 sc1\n"
"v_accvgpr_read_b32 v204, a92\n"
"s_waitcnt vmcnt(9)\n"
"s_mov_b64 exec, 0x300\n"
"buffer_store_dword v254, v253, s[52:55], 0 offen offset:8 sc1\n"
"s_mov_b64 exec, -1\n"
"buffer_load_dwordx4 v189, s[40:43], s74 offen offset:3072 lds\n"
"s_add_u32 m0, s68, 0x11000\n"
"s_add_u32 s74, s73, 0x8000\n"
"v_accvgpr_read_b32 v205, a93\n"
"v_accvgpr_read_b32 v206, a94\n"
"v_accvgpr_read_b32 v207, a95\n"
"v_pk_mul_f32 v[200:201], v[200:201], v[182:183]\n"
"v_pk_mul_f32 v[202:203], v[202:203], v[182:183]\n"
"v_pk_mul_f32 v[204:205], v[204:205], v[182:183]\n"
"v_pk_mul_f32 v[206:207], v[206:207], v[182:183]\n"
"v_cvt_pk_bf16_f32 v44, v200, v201\n"
"v_cvt_pk_bf16_f32 v45, v202, v203\n"
"v_cvt_pk_bf16_f32 v46, v204, v205\n"
"v_cvt_pk_bf16_f32 v47, v206, v207\n"
"v_exp_f32 v172, v224\n"
"v_exp_f32 v173, v225\n"
"v_exp_f32 v174, v226\n"
"v_exp_f32 v175, v227\n"
"buffer_load_dwordx4 v188, s[40:43], s74 offen offset:0 lds\n"
"v_cvt_pk_bf16_f32 v136, v172, v173\n"
"v_exp_f32 v172, v228\n"
"v_exp_f32 v173, v229\n"
"v_cvt_pk_bf16_f32 v137, v174, v175\n"
"v_exp_f32 v174, v230\n"
"v_cvt_pk_bf16_f32 v138, v172, v173\n"
"v_or3_b32 v184, v136, v137, v138\n"
"buffer_store_dwordx4 v[40:43], v252, s[48:51], s58 offen offset:2048 sc1\n"
"v_exp_f32 v175, v231\n"
"v_exp_f32 v172, v232\n"
"buffer_load_dwordx4 v189, s[40:43], s74 offen offset:1024 lds\n"
"s_add_u32 s74, s73, 0xb800\n"
"v_exp_f32 v173, v233\n"
"v_cvt_pk_bf16_f32 v139, v174, v175\n"
"v_exp_f32 v174, v234\n"
"v_cvt_pk_bf16_f32 v140, v172, v173\n"
"v_or3_b32 v184, v184, v139, v140\n"
"v_exp_f32 v175, v235\n"
"v_exp_f32 v172, v236\n"
"v_exp_f32 v173, v237\n"
"v_cvt_pk_bf16_f32 v141, v174, v175\n"
"v_exp_f32 v174, v238\n"
"buffer_load_dwordx4 v188, s[40:43], s74 offen offset:2048 lds\n"
"v_cvt_pk_bf16_f32 v142, v172, v173\n"
"v_or3_b32 v184, v184, v141, v142\n"
"v_exp_f32 v175, v239\n"
"s_nop 0\n"
"v_cvt_pk_bf16_f32 v143, v174, v175\n"
"v_bitop3_b32 v184, v184, v143, s60 bitop3:0xa8\n"
"v_cmp_ne_u32 vcc, 0, v184\n"
"v_rcp_f32 v182, v156\n"
"v_accvgpr_read_b32 v192, a96\n"
"s_cbranch_vccnz .Lk1_tr121\n"
".Lk1_ct121:\n"
"v_mfma_f32_16x16x32_bf16 v[160:163], v[168:171], v[136:139], v[160:163]\n"
"v_accvgpr_read_b32 v193, a97\n"
"v_accvgpr_read_b32 v194, a98\n"
"v_mfma_f32_16x16x32_bf16 v[164:167], v[168:171], v[140:143], v[164:167]\n"
"buffer_store_dwordx4 v[44:47], v252, s[48:51], s58 offen offset:3072 sc1\n"
"v_mfma_f32_16x16x32_bf16 a[128:131], a[224:227], v[136:139], a[128:131]\n"
"v_accvgpr_read_b32 v195, a99\n"
"v_mov_b32 v183, v182\n"
"v_mfma_f32_16x16x32_bf16 a[160:163], a[224:227], v[140:143], a[160:163]\n"
"buffer_load_dwordx4 v189, s[40:43], s74 offen offset:3072 lds\n"
"v_accvgpr_read_b32 v196, a100\n"
"v_mfma_f32_16x16x32_bf16 a[132:135], a[228:231], v[136:139], a[132:135]\n"
"v_accvgpr_read_b32 v197, a101\n"
"v_accvgpr_read_b32 v198, a102\n"
"v_accvgpr_read_b32 v199, a103\n"
"v_pk_mul_f32 v[192:193], v[192:193], v[182:183]\n"
"v_pk_mul_f32 v[194:195], v[194:195], v[182:183]\n"
"v_pk_mul_f32 v[196:197], v[196:197], v[182:183]\n"
"v_pk_mul_f32 v[198:199], v[198:199], v[182:183]\n"
"v_mfma_f32_16x16x32_bf16 a[164:167], a[228:231], v[140:143], a[164:167]\n"
"v_cvt_pk_bf16_f32 v48, v192, v193\n"
"v_cvt_pk_bf16_f32 v49, v194, v195\n"
"v_mfma_f32_16x16x32_bf16 a[136:139], a[232:235], v[136:139], a[136:139]\n"
"v_cvt_pk_bf16_f32 v50, v196, v197\n"
"v_cvt_pk_bf16_f32 v51, v198, v199\n"
"v_mfma_f32_16x16x32_bf16 a[168:171], a[232:235], v[140:143], a[168:171]\n"
"v_accvgpr_read_b32 v200, a104\n"
"v_accvgpr_read_b32 v201, a105\n"
"v_mfma_f32_16x16x32_bf16 a[140:143], a[236:239], v[136:139], a[140:143]\n"
"v_accvgpr_read_b32 v202, a106\n"
"v_accvgpr_read_b32 v203, a107\n"
"v_mfma_f32_16x16x32_bf16 a[172:175], a[236:239], v[140:143], a[172:175]\n"
"buffer_load_dword v240, v185, s[52:55], s86 offen sc1\n"
"ds_write_b128 v250, v[48:51] offset:0\n"
"v_mfma_f32_16x16x32_bf16 a[144:147], a[240:243], v[136:139], a[144:147]\n"
"v_accvgpr_read_b32 v204, a108\n"
"v_accvgpr_read_b32 v205, a109\n"
"v_mfma_f32_16x16x32_bf16 a[176:179], a[240:243], v[140:143], a[176:179]\n"
"v_accvgpr_read_b32 v206, a110\n"
"v_accvgpr_read_b32 v207, a111\n"
"v_pk_mul_f32 v[200:201], v[200:201], v[182:183]\n"
"v_pk_mul_f32 v[202:203], v[202:203], v[182:183]\n"
"v_pk_mul_f32 v[204:205], v[204:205], v[182:183]\n"
"v_pk_mul_f32 v[206:207], v[206:207], v[182:183]\n"
"v_mfma_f32_16x16x32_bf16 a[148:151], a[244:247], v[136:139], a[148:151]\n"
"v_cvt_pk_bf16_f32 v52, v200, v201\n"
"v_cvt_pk_bf16_f32 v53, v202, v203\n"
"v_mfma_f32_16x16x32_bf16 a[180:183], a[244:247], v[140:143], a[180:183]\n"
"v_cvt_pk_bf16_f32 v54, v204, v205\n"
"v_cvt_pk_bf16_f32 v55, v206, v207\n"
"v_mfma_f32_16x16x32_bf16 a[152:155], a[248:251], v[136:139], a[152:155]\n"
"v_accvgpr_read_b32 v192, a112\n"
"v_accvgpr_read_b32 v193, a113\n"
"v_mfma_f32_16x16x32_bf16 a[184:187], a[248:251], v[140:143], a[184:187]\n"
"v_accvgpr_read_b32 v194, a114\n"
"v_accvgpr_read_b32 v195, a115\n"
"v_mfma_f32_16x16x32_bf16 a[156:159], a[252:255], v[136:139], a[156:159]\n"
"ds_write_b128 v250, v[52:55] offset:1024\n"
"v_mfma_f32_16x16x32_bf16 a[188:191], a[252:255], v[140:143], a[188:191]\n"
"v_accvgpr_read_b32 v196, a116\n"
"v_accvgpr_read_b32 v197, a117\n"
"v_mfma_f32_16x16x32_bf16 v[96:99], a[192:195], v[48:51], 0\n"
"v_accvgpr_read_b32 v198, a118\n"
"v_accvgpr_read_b32 v199, a119\n"
"v_pk_mul_f32 v[192:193], v[192:193], v[182:183]\n"
"v_pk_mul_f32 v[194:195], v[194:195], v[182:183]\n"
"v_pk_mul_f32 v[196:197], v[196:197], v[182:183]\n"
"v_pk_mul_f32 v[198:199], v[198:199], v[182:183]\n"
"v_mfma_f32_16x16x32_bf16 v[100:103], a[196:199], v[48:51], 0\n"
"v_cvt_pk_bf16_f32 v56, v192, v193\n"
"v_cvt_pk_bf16_f32 v57, v194, v195\n"
"v_mfma_f32_16x16x32_bf16 v[96:99], a[200:203], v[52:55], v[96:99]\n"
"v_cvt_pk_bf16_f32 v58, v196, v197\n"
"v_cvt_pk_bf16_f32 v59, v198, v199\n"
"v_mfma_f32_16x16x32_bf16 v[100:103], a[204:207], v[52:55], v[100:103]\n"
"v_accvgpr_read_b32 v200, a120\n"
"v_accvgpr_read_b32 v201, a121\n"
"v_mfma_f32_16x16x32_bf16 v[96:99], a[208:211], v[56:59], v[96:99]\n"
"v_accvgpr_read_b32 v202, a122\n"
"v_accvgpr_read_b32 v203, a123\n"
"v_mfma_f32_16x16x32_bf16 v[100:103], a[212:215], v[56:59], v[100:103]\n"
"ds_write_b128 v250, v[56:59] offset:2048\n"
"v_accvgpr_read_b32 v204, a124\n"
"v_accvgpr_read_b32 v205, a125\n"
"v_accvgpr_read_b32 v206, a126\n"
"v_accvgpr_read_b32 v207, a127\n"
"v_pk_mul_f32 v[200:201], v[200:201], v[182:183]\n"
"v_pk_mul_f32 v[202:203], v[202:203], v[182:183]\n"
"v_pk_mul_f32 v[204:205], v[204:205], v[182:183]\n"
"v_pk_mul_f32 v[206:207], v[206:207], v[182:183]\n"
"v_cvt_pk_bf16_f32 v60, v200, v201\n"
"v_cvt_pk_bf16_f32 v61, v202, v203\n"
"v_cvt_pk_bf16_f32 v62, v204, v205\n"
"v_cvt_pk_bf16_f32 v63, v206, v207\n"
"s_waitcnt vmcnt(14)\n"
"s_mov_b64 exec, 0x300\n"
"buffer_store_dword v254, v253, s[52:55], 0 offen offset:16 sc1\n"
"s_mov_b64 exec, -1\n"
"v_mfma_f32_16x16x32_bf16 v[96:99], a[216:219], v[60:63], v[96:99]\n"
"ds_write_b128 v250, v[60:63] offset:3072\n"
"v_mfma_f32_16x16x32_bf16 v[100:103], a[220:223], v[60:63], v[100:103]\n"
"v_bfe_u32 v176, v255, 2, 2\n"
"v_lshrrev_b32 v177, 4, v255\n"
"v_cmp_eq_u32 vcc, v176, v177\n"
"v_and_b32 v176, 1, v255\n"
"v_mov_b32 v177, 0x3f80\n"
"v_lshlrev_b32 v176, 4, v176\n"
"v_lshlrev_b32 v176, v176, v177\n"
"v_cndmask_b32 v176, 0, v176, vcc\n"
"v_and_b32 v177, 2, v255\n"
"v_cmp_eq_u32 vcc, 0, v177\n"
"s_nop 1\n"
"v_cndmask_b32 v168, 0, v176, vcc\n"
"v_cndmask_b32 v169, v176, 0, vcc\n"
"v_mov_b32 v170, 0\n"
"v_mov_b32 v171, 0\n"
"v_mov_b32 v172, v168\n"
"v_mov_b32 v173, v169\n"
"v_rcp_f32 v182, v160\n"
"v_accvgpr_read_b32 v192, a128\n"
"v_accvgpr_read_b32 v193, a129\n"
"v_accvgpr_read_b32 v194, a130\n"
"v_accvgpr_read_b32 v195, a131\n"
"v_mov_b32 v183, v182\n"
"v_accvgpr_read_b32 v196, a132\n"
"v_accvgpr_read_b32 v197, a133\n"
"v_accvgpr_read_b32 v198, a134\n"
"v_accvgpr_read_b32 v199, a135\n"
"v_pk_mul_f32 v[192:193], v[192:193], v[182:183]\n"
"v_pk_mul_f32 v[194:195], v[194:195], v[182:183]\n"
"v_pk_mul_f32 v[196:197], v[196:197], v[182:183]\n"
"v_pk_mul_f32 v[198:199], v[198:199], v[182:183]\n"
"v_cvt_pk_bf16_f32 v64, v192, v193\n"
"v_cvt_pk_bf16_f32 v65, v194, v195\n"
"v_cvt_pk_bf16_f32 v66, v196, v197\n"
"v_cvt_pk_bf16_f32 v67, v198, v199\n"
"v_accvgpr_read_b32 v200, a136\n"
"v_accvgpr_read_b32 v201, a137\n"
"v_mfma_f32_16x16x32_bf16 v[104:107], a[192:195], v[64:67], 0\n"
"v_accvgpr_read_b32 v202, a138\n"
"v_accvgpr_read_b32 v203, a139\n"
"v_mfma_f32_16x16x32_bf16 v[108:111], a[196:199], v[64:67], 0\n"
"ds_write_b128 v250, v[64:67] offset:4096\n"
"v_accvgpr_read_b32 v204, a140\n"
"v_accvgpr_read_b32 v205, a141\n"
"v_accvgpr_read_b32 v206, a142\n"
"v_accvgpr_read_b32 v207, a143\n"
"v_pk_mul_f32 v[200:201], v[200:201], v[182:183]\n"
"v_pk_mul_f32 v[202:203], v[202:203], v[182:183]\n"
"v_pk_mul_f32 v[204:205], v[204:205], v[182:183]\n"
"v_pk_mul_f32 v[206:207], v[206:207], v[182:183]\n"
"v_cvt_pk_bf16_f32 v68, v200, v201\n"
"v_cvt_pk_bf16_f32 v69, v202, v203\n"
"v_cvt_pk_bf16_f32 v70, v204, v205\n"
"v_cvt_pk_bf16_f32 v71, v206, v207\n"
"v_accvgpr_read_b32 v192, a144\n"
"v_accvgpr_read_b32 v193, a145\n"
"v_mfma_f32_16x16x32_bf16 v[104:107], a[200:203], v[68:71], v[104:107]\n"
"v_accvgpr_read_b32 v194, a146\n"
"v_accvgpr_read_b32 v195, a147\n"
"v_mfma_f32_16x16x32_bf16 v[108:111], a[204:207], v[68:71], v[108:111]\n"
"ds_write_b128 v250, v[68:71] offset:5120\n"
"v_accvgpr_read_b32 v196, a148\n"
"v_accvgpr_read_b32 v197, a149\n"
"v_accvgpr_read_b32 v198, a150\n"
"v_accvgpr_read_b32 v199, a151\n"
"v_pk_mul_f32 v[192:193], v[192:193], v[182:183]\n"
"v_pk_mul_f32 v[194:195], v[194:195], v[182:183]\n"
"v_pk_mul_f32 v[196:197], v[196:197], v[182:183]\n"
"v_pk_mul_f32 v[198:199], v[198:199], v[182:183]\n"
"v_cvt_pk_bf16_f32 v72, v192, v193\n"
"v_cvt_pk_bf16_f32 v73, v194, v195\n"
"v_cvt_pk_bf16_f32 v74, v196, v197\n"
"v_cvt_pk_bf16_f32 v75, v198, v199\n"
"v_accvgpr_read_b32 v200, a152\n"
"v_accvgpr_read_b32 v201, a153\n"
"v_mfma_f32_16x16x32_bf16 v[104:107], a[208:211], v[72:75], v[104:107]\n"
"v_accvgpr_read_b32 v202, a154\n"
"v_accvgpr_read_b32 v203, a155\n"
"v_mfma_f32_16x16x32_bf16 v[108:111], a[212:215], v[72:75], v[108:111]\n"
"ds_write_b128 v250, v[72:75] offset:6144\n"
"v_accvgpr_read_b32 v204, a156\n"
"v_accvgpr_read_b32 v205, a157\n"
"v_accvgpr_read_b32 v206, a158\n"
"v_accvgpr_read_b32 v207, a159\n"
"v_pk_mul_f32 v[200:201], v[200:201], v[182:183]\n"
"v_pk_mul_f32 v[202:203], v[202:203], v[182:183]\n"
"v_pk_mul_f32 v[204:205], v[204:205], v[182:183]\n"
"v_pk_mul_f32 v[206:207], v[206:207], v[182:183]\n"
"v_cvt_pk_bf16_f32 v76, v200, v201\n"
"v_cvt_pk_bf16_f32 v77, v202, v203\n"
"v_cvt_pk_bf16_f32 v78, v204, v205\n"
"v_cvt_pk_bf16_f32 v79, v206, v207\n"
"s_nop 1\n"
"v_mfma_f32_16x16x32_bf16 v[104:107], a[216:219], v[76:79], v[104:107]\n"
"ds_write_b128 v250, v[76:79] offset:7168\n"
"v_mfma_f32_16x16x32_bf16 v[108:111], a[220:223], v[76:79], v[108:111]\n"
"buffer_load_dword v241, v185, s[52:55], s86 offen sc1\n"
"s_waitcnt lgkmcnt(0)\n"
"s_barrier\n"
"ds_read_b128 a[96:99], v251 offset:0\n"
"ds_read_b128 a[100:103], v251 offset:1024\n"
"ds_read_b128 a[104:107], v251 offset:2048\n"
"ds_read_b128 a[108:111], v251 offset:3072\n"
"ds_read_b128 a[112:115], v251 offset:4096\n"
"ds_read_b128 a[116:119], v251 offset:5120\n"
"ds_read_b128 a[120:123], v251 offset:6144\n"
"ds_read_b128 a[124:127], v251 offset:7168\n"
"s_waitcnt lgkmcnt(7)\n"
"v_mfma_f32_16x16x32_bf16 v[96:99], a[32:35], a[96:99], v[96:99]\n"
"v_rcp_f32 v182, v164\n"
"s_waitcnt lgkmcnt(3)\n"
"v_mfma_f32_16x16x32_bf16 v[104:107], a[32:35], a[112:115], v[104:107]\n"
"v_accvgpr_read_b32 v192, a160\n"
"v_accvgpr_read_b32 v193, a161\n"
"v_mfma_f32_16x16x32_bf16 v[100:103], a[36:39], a[96:99], v[100:103]\n"
"v_accvgpr_read_b32 v194, a162\n"
"v_accvgpr_read_b32 v195, a163\n"
"v_mfma_f32_16x16x32_bf16 v[108:111], a[36:39], a[112:115], v[108:111]\n"
"v_mov_b32 v183, v182\n"
"v_accvgpr_read_b32 v196, a164\n"
"v_mfma_f32_16x16x32_bf16 v[96:99], a[40:43], a[100:103], v[96:99]\n"
"v_accvgpr_read_b32 v197, a165\n"
"v_accvgpr_read_b32 v198, a166\n"
"v_accvgpr_read_b32 v199, a167\n"
"v_pk_mul_f32 v[192:193], v[192:193], v[182:183]\n"
"v_pk_mul_f32 v[194:195], v[194:195], v[182:183]\n"
"v_pk_mul_f32 v[196:197], v[196:197], v[182:183]\n"
"v_pk_mul_f32 v[198:199], v[198:199], v[182:183]\n"
"s_waitcnt lgkmcnt(2)\n"
"v_mfma_f32_16x16x32_bf16 v[104:107], a[40:43], a[116:119], v[104:107]\n"
"v_cvt_pk_bf16_f32 v80, v192, v193\n"
"v_cvt_pk_bf16_f32 v81, v194, v195\n"
"v_mfma_f32_16x16x32_bf16 v[100:103], a[44:47], a[100:103], v[100:103]\n"
"v_cvt_pk_bf16_f32 v82, v196, v197\n"
"v_cvt_pk_bf16_f32 v83, v198, v199\n"
"v_mfma_f32_16x16x32_bf16 v[108:111], a[44:47], a[116:119], v[108:111]\n"
"v_accvgpr_read_b32 v200, a168\n"
"v_accvgpr_read_b32 v201, a169\n"
"v_mfma_f32_16x16x32_bf16 v[96:99], a[48:51], a[104:107], v[96:99]\n"
"v_accvgpr_read_b32 v202, a170\n"
"v_accvgpr_read_b32 v203, a171\n"
"s_waitcnt lgkmcnt(1)\n"
"v_mfma_f32_16x16x32_bf16 v[104:107], a[48:51], a[120:123], v[104:107]\n"
"s_waitcnt vmcnt(4)\n"
"s_mov_b64 exec, 0x300\n"
"buffer_store_dword v254, v253, s[52:55], 0 offen offset:24 sc1\n"
"s_mov_b64 exec, -1\n"
"v_mfma_f32_16x16x32_bf16 v[100:103], a[52:55], a[104:107], v[100:103]\n"
"ds_write_b128 v250, v[80:83] offset:32768\n"
"v_mfma_f32_16x16x32_bf16 v[108:111], a[52:55], a[120:123], v[108:111]\n"
"v_accvgpr_read_b32 v204, a172\n"
"v_accvgpr_read_b32 v205, a173\n"
"v_mfma_f32_16x16x32_bf16 v[96:99], a[56:59], a[108:111], v[96:99]\n"
"v_accvgpr_read_b32 v206, a174\n"
"v_accvgpr_read_b32 v207, a175\n"
"v_pk_mul_f32 v[200:201], v[200:201], v[182:183]\n"
"v_pk_mul_f32 v[202:203], v[202:203], v[182:183]\n"
"v_pk_mul_f32 v[204:205], v[204:205], v[182:183]\n"
"v_pk_mul_f32 v[206:207], v[206:207], v[182:183]\n"
"s_waitcnt lgkmcnt(1)\n"
"v_mfma_f32_16x16x32_bf16 v[104:107], a[56:59], a[124:127], v[104:107]\n"
"v_cvt_pk_bf16_f32 v84, v200, v201\n"
"v_cvt_pk_bf16_f32 v85, v202, v203\n"
"v_mfma_f32_16x16x32_bf16 v[100:103], a[60:63], a[108:111], v[100:103]\n"
"v_cvt_pk_bf16_f32 v86, v204, v205\n"
"v_cvt_pk_bf16_f32 v87, v206, v207\n"
"v_mfma_f32_16x16x32_bf16 v[108:111], a[60:63], a[124:127], v[108:111]\n"
"s_waitcnt vmcnt(3)\n"
"v_cmp_ne_u32 s[22:23], s82, v240\n"
"s_nop 0\n"
"s_and_b32 s22, s22, 0xc00\n"
"s_cbranch_scc1 .Lk1_pw0\n"
".Lk1_pd0:\n"
"v_mfma_f32_16x16x32_bf16 v[112:115], a[192:195], v[80:83], 0\n"
"ds_read_b64_tr_b16 a[192:193], v246 offset:32768\n"
"ds_read_b64_tr_b16 a[194:195], v246 offset:34816\n"
"v_mfma_f32_16x16x32_bf16 v[116:119], a[196:199], v[80:83], 0\n"
"ds_read_b64_tr_b16 a[196:197], v247 offset:32768\n"
"ds_read_b64_tr_b16 a[198:199], v247 offset:34816\n"
"v_mfma_f32_16x16x32_bf16 v[112:115], a[200:203], v[84:87], v[112:115]\n"
"ds_read_b64_tr_b16 a[200:201], v246 offset:36864\n"
"ds_read_b64_tr_b16 a[202:203], v246 offset:38912\n"
"v_mfma_f32_16x16x32_bf16 v[116:119], a[204:207], v[84:87], v[116:119]\n"
"ds_read_b64_tr_b16 a[204:205], v247 offset:36864\n"
"ds_read_b64_tr_b16 a[206:207], v247 offset:38912\n"
"buffer_load_dwordx4 a[0:3], v252, s[48:51], s77 offen offset:0 sc1\n"
"v_accvgpr_read_b32 v192, a176\n"
"v_accvgpr_read_b32 v193, a177\n"
"v_accvgpr_read_b32 v194, a178\n"
"v_accvgpr_read_b32 v195, a179\n"
"ds_write_b128 v250, v[84:87] offset:33792\n"
"buffer_load_dwordx4 a[4:7], v252, s[48:51], s77 offen offset:1024 sc1\n"
"v_accvgpr_read_b32 v196, a180\n"
"v_accvgpr_read_b32 v197, a181\n"
"v_accvgpr_read_b32 v198, a182\n"
"v_accvgpr_read_b32 v199, a183\n"
"v_pk_mul_f32 v[192:193], v[192:193], v[182:183]\n"
"v_pk_mul_f32 v[194:195], v[194:195], v[182:183]\n"
"v_pk_mul_f32 v[196:197], v[196:197], v[182:183]\n"
"buffer_load_dwordx4 a[8:11], v252, s[48:51], s77 offen offset:2048 sc1\n"
"v_pk_mul_f32 v[198:199], v[198:199], v[182:183]\n"
"v_cvt_pk_bf16_f32 v88, v192, v193\n"
"v_cvt_pk_bf16_f32 v89, v194, v195\n"
"v_cvt_pk_bf16_f32 v90, v196, v197\n"
"v_cvt_pk_bf16_f32 v91, v198, v199\n"
"v_accvgpr_read_b32 v200, a184\n"
"v_accvgpr_read_b32 v201, a185\n"
"v_mfma_f32_16x16x32_bf16 v[112:115], a[208:211], v[88:91], v[112:115]\n"
"ds_read_b64_tr_b16 a[208:209], v246 offset:40960\n"
"ds_read_b64_tr_b16 a[210:211], v246 offset:43008\n"
"v_mfma_f32_16x16x32_bf16 v[116:119], a[212:215], v[88:91], v[116:119]\n"
"ds_read_b64_tr_b16 a[212:213], v247 offset:40960\n"
"ds_read_b64_tr_b16 a[214:215], v247 offset:43008\n"
"buffer_load_dwordx4 a[12:15], v252, s[48:51], s77 offen offset:3072 sc1\n"
"v_accvgpr_read_b32 v202, a186\n"
"v_accvgpr_read_b32 v203, a187\n"
"ds_write_b128 v250, v[88:91] offset:34816\n"
"buffer_load_dwordx4 a[16:19], v252, s[48:51], s78 offen offset:0 sc1\n"
"v_accvgpr_read_b32 v204, a188\n"
"v_accvgpr_read_b32 v205, a189\n"
"v_accvgpr_read_b32 v206, a190\n"
"v_accvgpr_read_b32 v207, a191\n"
"v_pk_mul_f32 v[200:201], v[200:201], v[182:183]\n"
"v_pk_mul_f32 v[202:203], v[202:203], v[182:183]\n"
"v_pk_mul_f32 v[204:205], v[204:205], v[182:183]\n"
"buffer_load_dwordx4 a[20:23], v252, s[48:51], s78 offen offset:1024 sc1\n"
"v_pk_mul_f32 v[206:207], v[206:207], v[182:183]\n"
"v_cvt_pk_bf16_f32 v92, v200, v201\n"
"v_cvt_pk_bf16_f32 v93, v202, v203\n"
"v_cvt_pk_bf16_f32 v94, v204, v205\n"
"v_cvt_pk_bf16_f32 v95, v206, v207\n"
"s_nop 1\n"
"v_mfma_f32_16x16x32_bf16 v[112:115], a[216:219], v[92:95], v[112:115]\n"
"ds_read_b64_tr_b16 a[216:217], v246 offset:45056\n"
"ds_read_b64_tr_b16 a[218:219], v246 offset:47104\n"
"v_mfma_f32_16x16x32_bf16 v[116:119], a[220:223], v[92:95], v[116:119]\n"
"ds_read_b64_tr_b16 a[220:221], v247 offset:45056\n"
"ds_read_b64_tr_b16 a[222:223], v247 offset:47104\n"
"buffer_load_dwordx4 a[24:27], v252, s[48:51], s78 offen offset:2048 sc1\n"
"ds_write_b128 v250, v[92:95] offset:35840\n"
"s_waitcnt lgkmcnt(0)\n"
"s_barrier\n"
"buffer_load_dword v242, v185, s[52:55], s86 offen sc1\n"
"s_add_u32 s73, s94, 0x200\n"
"buffer_load_dwordx4 a[28:31], v252, s[48:51], s78 offen offset:3072 sc1\n"
"s_and_b32 s73, s73, 0x380\n"
"s_add_u32 s73, s73, s96\n"
"s_add_u32 m0, s68, 0x0\n"
"s_add_u32 s74, s73, 0x0\n"
"ds_read_b128 a[128:131], v251 offset:32768\n"
"ds_read_b128 a[132:135], v251 offset:33792\n"
"ds_read_b128 a[136:139], v251 offset:34816\n"
"buffer_load_dwordx4 v188, s[40:43], s74 offen offset:0 lds\n"
"ds_read_b128 a[140:143], v251 offset:35840\n"
"s_waitcnt lgkmcnt(3)\n"
"v_mfma_f32_16x16x32_bf16 v[112:115], a[32:35], a[128:131], v[112:115]\n"
"ds_read_b64_tr_b16 a[32:33], v248 offset:32768\n"
"ds_read_b64_tr_b16 a[34:35], v248 offset:34816\n"
"v_mfma_f32_16x16x32_bf16 v[116:119], a[36:39], a[128:131], v[116:119]\n"
"ds_read_b64_tr_b16 a[36:37], v249 offset:32768\n"
"ds_read_b64_tr_b16 a[38:39], v249 offset:34816\n"
"s_waitcnt lgkmcnt(6)\n"
"v_mfma_f32_16x16x32_bf16 v[112:115], a[40:43], a[132:135], v[112:115]\n"
"ds_read_b64_tr_b16 a[40:41], v248 offset:36864\n"
"ds_read_b64_tr_b16 a[42:43], v248 offset:38912\n"
"v_mfma_f32_16x16x32_bf16 v[116:119], a[44:47], a[132:135], v[116:119]\n"
"ds_read_b64_tr_b16 a[44:45], v249 offset:36864\n"
"ds_read_b64_tr_b16 a[46:47], v249 offset:38912\n"
"s_waitcnt lgkmcnt(9)\n"
"v_mfma_f32_16x16x32_bf16 v[112:115], a[48:51], a[136:139], v[112:115]\n"
"ds_read_b64_tr_b16 a[48:49], v248 offset:40960\n"
"ds_read_b64_tr_b16 a[50:51], v248 offset:43008\n"
"v_mfma_f32_16x16x32_bf16 v[116:119], a[52:55], a[136:139], v[116:119]\n"
"ds_read_b64_tr_b16 a[52:53], v249 offset:40960\n"
"ds_read_b64_tr_b16 a[54:55], v249 offset:43008\n"
"s_waitcnt lgkmcnt(12)\n"
"v_mfma_f32_16x16x32_bf16 v[112:115], a[56:59], a[140:143], v[112:115]\n"
"ds_read_b64_tr_b16 a[56:57], v248 offset:45056\n"
"ds_read_b64_tr_b16 a[58:59], v248 offset:47104\n"
"v_mfma_f32_16x16x32_bf16 v[116:119], a[60:63], a[140:143], v[116:119]\n"
"ds_read_b64_tr_b16 a[60:61], v249 offset:45056\n"
"ds_read_b64_tr_b16 a[62:63], v249 offset:47104\n"
"v_mfma_f32_16x16x32_bf16 v[120:123], a[192:195], v[48:51], 0\n"
"buffer_load_dwordx4 v189, s[40:43], s74 offen offset:1024 lds\n"
"s_add_u32 s74, s73, 0x3800\n"
"v_mfma_f32_16x16x32_bf16 v[128:131], a[192:195], v[64:67], 0\n"
"v_add_u32 v246, 0x10000, v246\n"
"v_mfma_f32_16x16x32_bf16 v[136:139], a[192:195], v[80:83], 0\n"
"v_mfma_f32_16x16x32_bf16 v[124:127], a[196:199], v[48:51], 0\n"
"buffer_load_dwordx4 v188, s[40:43], s74 offen offset:2048 lds\n"
"v_mfma_f32_16x16x32_bf16 v[132:135], a[196:199], v[64:67], 0\n"
"v_add_u32 v247, 0x10000, v247\n"
"v_mfma_f32_16x16x32_bf16 v[140:143], a[196:199], v[80:83], 0\n"
"v_mfma_f32_16x16x32_bf16 v[120:123], a[200:203], v[52:55], v[120:123]\n"
"s_waitcnt lgkmcnt(0)\n"
"s_barrier\n"
"v_mfma_f32_16x16x32_bf16 v[128:131], a[200:203], v[68:71], v[128:131]\n"
"buffer_load_dwordx4 v189, s[40:43], s74 offen offset:3072 lds\n"
"s_add_u32 m0, s68, 0x1000\n"
"v_mfma_f32_16x16x32_bf16 v[136:139], a[200:203], v[84:87], v[136:139]\n"
"ds_read_b64_tr_b16 a[200:201], v246 offset:4096\n"
"ds_read_b64_tr_b16 a[202:203], v246 offset:6144\n"
"v_mfma_f32_16x16x32_bf16 v[124:127], a[204:207], v[52:55], v[124:127]\n"
"ds_read_b64_tr_b16 a[192:193], v246 offset:0\n"
"ds_read_b64_tr_b16 a[194:195], v246 offset:2048\n"
"v_mfma_f32_16x16x32_bf16 v[132:135], a[204:207], v[68:71], v[132:135]\n"
"s_add_u32 s74, s73, 0x8000\n"
"v_mfma_f32_16x16x32_bf16 v[140:143], a[204:207], v[84:87], v[140:143]\n"
"ds_read_b64_tr_b16 a[204:205], v247 offset:4096\n"
"ds_read_b64_tr_b16 a[206:207], v247 offset:6144\n"
"v_mfma_f32_16x16x32_bf16 v[120:123], a[208:211], v[56:59], v[120:123]\n"
"ds_read_b64_tr_b16 a[196:197], v247 offset:0\n"
"ds_read_b64_tr_b16 a[198:199], v247 offset:2048\n"
"v_mfma_f32_16x16x32_bf16 v[128:131], a[208:211], v[72:75], v[128:131]\n"
"s_waitcnt vmcnt(14)\n"
"v_cmp_ne_u32 s[22:23], s82, v241\n"
"s_nop 0\n"
"s_and_b32 s22, s22, 0x3000\n"
"s_cbranch_scc1 .Lk1_pw1\n"
".Lk1_pd1:\n"
"v_mfma_f32_16x16x32_bf16 v[136:139], a[208:211], v[88:91], v[136:139]\n"
"ds_read_b64_tr_b16 a[208:209], v246 offset:8192\n"
"ds_read_b64_tr_b16 a[210:211], v246 offset:10240\n"
"v_mfma_f32_16x16x32_bf16 v[124:127], a[212:215], v[56:59], v[124:127]\n"
"s_add_u32 s77, s77, 4096\n"
"buffer_load_dwordx4 a[64:67], v252, s[48:51], s77 offen offset:0 sc1\n"
"v_mfma_f32_16x16x32_bf16 v[132:135], a[212:215], v[72:75], v[132:135]\n"
"v_mfma_f32_16x16x32_bf16 v[140:143], a[212:215], v[88:91], v[140:143]\n"
"ds_read_b64_tr_b16 a[212:213], v247 offset:8192\n"
"ds_read_b64_tr_b16 a[214:215], v247 offset:10240\n"
"v_mfma_f32_16x16x32_bf16 v[120:123], a[216:219], v[60:63], v[120:123]\n"
"buffer_load_dwordx4 a[68:71], v252, s[48:51], s77 offen offset:1024 sc1\n"
"v_mfma_f32_16x16x32_bf16 v[128:131], a[216:219], v[76:79], v[128:131]\n"
"v_mfma_f32_16x16x32_bf16 v[136:139], a[216:219], v[92:95], v[136:139]\n"
"ds_read_b64_tr_b16 a[216:217], v246 offset:12288\n"
"ds_read_b64_tr_b16 a[218:219], v246 offset:14336\n"
"v_mfma_f32_16x16x32_bf16 v[124:127], a[220:223], v[60:63], v[124:127]\n"
"buffer_load_dwordx4 a[72:75], v252, s[48:51], s77 offen offset:2048 sc1\n"
"v_mfma_f32_16x16x32_bf16 v[132:135], a[220:223], v[76:79], v[132:135]\n"
"v_mfma_f32_16x16x32_bf16 v[140:143], a[220:223], v[92:95], v[140:143]\n"
"ds_read_b64_tr_b16 a[220:221], v247 offset:12288\n"
"ds_read_b64_tr_b16 a[222:223], v247 offset:14336\n"
"v_mfma_f32_16x16x32_bf16 v[120:123], a[32:35], a[96:99], v[120:123]\n"
"buffer_load_dwordx4 a[76:79], v252, s[48:51], s77 offen offset:3072 sc1\n"
"s_add_u32 s78, s78, 4096\n"
"v_mfma_f32_16x16x32_bf16 v[128:131], a[32:35], a[112:115], v[128:131]\n"
"v_add_u32 v248, 0x10000, v248\n"
"v_mfma_f32_16x16x32_bf16 v[136:139], a[32:35], a[128:131], v[136:139]\n"
"ds_read_b64_tr_b16 a[32:33], v248 offset:0\n"
"ds_read_b64_tr_b16 a[34:35], v248 offset:2048\n"
"v_mfma_f32_16x16x32_bf16 v[124:127], a[36:39], a[96:99], v[124:127]\n"
"buffer_load_dwordx4 a[80:83], v252, s[48:51], s78 offen offset:0 sc1\n"
"v_mfma_f32_16x16x32_bf16 v[132:135], a[36:39], a[112:115], v[132:135]\n"
"v_add_u32 v249, 0x10000, v249\n"
"v_mfma_f32_16x16x32_bf16 v[140:143], a[36:39], a[128:131], v[140:143]\n"
"ds_read_b64_tr_b16 a[36:37], v249 offset:0\n"
"ds_read_b64_tr_b16 a[38:39], v249 offset:2048\n"
"v_mfma_f32_16x16x32_bf16 v[120:123], a[40:43], a[100:103], v[120:123]\n"
"buffer_load_dwordx4 a[84:87], v252, s[48:51], s78 offen offset:1024 sc1\n"
"v_mfma_f32_16x16x32_bf16 v[128:131], a[40:43], a[116:119], v[128:131]\n"
"v_mfma_f32_16x16x32_bf16 v[136:139], a[40:43], a[132:135], v[136:139]\n"
"ds_read_b64_tr_b16 a[40:41], v248 offset:4096\n"
"ds_read_b64_tr_b16 a[42:43], v248 offset:6144\n"
"v_mfma_f32_16x16x32_bf16 v[124:127], a[44:47], a[100:103], v[124:127]\n"
"buffer_load_dwordx4 a[88:91], v252, s[48:51], s78 offen offset:2048 sc1\n"
"v_mfma_f32_16x16x32_bf16 v[132:135], a[44:47], a[116:119], v[132:135]\n"
"v_mfma_f32_16x16x32_bf16 v[140:143], a[44:47], a[132:135], v[140:143]\n"
"ds_read_b64_tr_b16 a[44:45], v249 offset:4096\n"
"ds_read_b64_tr_b16 a[46:47], v249 offset:6144\n"
"v_mfma_f32_16x16x32_bf16 v[120:123], a[48:51], a[104:107], v[120:123]\n"
"buffer_load_dwordx4 a[92:95], v252, s[48:51], s78 offen offset:3072 sc1\n"
"v_mfma_f32_16x16x32_bf16 v[128:131], a[48:51], a[120:123], v[128:131]\n"
"v_mfma_f32_16x16x32_bf16 v[136:139], a[48:51], a[136:139], v[136:139]\n"
"ds_read_b64_tr_b16 a[48:49], v248 offset:8192\n"
"ds_read_b64_tr_b16 a[50:51], v248 offset:10240\n"
"v_mfma_f32_16x16x32_bf16 v[124:127], a[52:55], a[104:107], v[124:127]\n"
"buffer_load_dwordx4 v188, s[40:43], s74 offen offset:0 lds\n"
"v_mfma_f32_16x16x32_bf16 v[132:135], a[52:55], a[120:123], v[132:135]\n"
"v_mfma_f32_16x16x32_bf16 v[140:143], a[52:55], a[136:139], v[140:143]\n"
"ds_read_b64_tr_b16 a[52:53], v249 offset:8192\n"
"ds_read_b64_tr_b16 a[54:55], v249 offset:10240\n"
"v_mfma_f32_16x16x32_bf16 v[120:123], a[56:59], a[108:111], v[120:123]\n"
"buffer_load_dwordx4 v189, s[40:43], s74 offen offset:1024 lds\n"
"s_add_u32 s74, s73, 0xb800\n"
"v_mfma_f32_16x16x32_bf16 v[128:131], a[56:59], a[124:127], v[128:131]\n"
"v_mfma_f32_16x16x32_bf16 v[136:139], a[56:59], a[140:143], v[136:139]\n"
"ds_read_b64_tr_b16 a[56:57], v248 offset:12288\n"
"ds_read_b64_tr_b16 a[58:59], v248 offset:14336\n"
"v_mfma_f32_16x16x32_bf16 v[124:127], a[60:63], a[108:111], v[124:127]\n"
"v_mfma_f32_16x16x32_bf16 v[132:135], a[60:63], a[124:127], v[132:135]\n"
"v_mfma_f32_16x16x32_bf16 v[140:143], a[60:63], a[140:143], v[140:143]\n"
"ds_read_b64_tr_b16 a[60:61], v249 offset:12288\n"
"ds_read_b64_tr_b16 a[62:63], v249 offset:14336\n"
"s_waitcnt lgkmcnt(15)\n"
"v_mfma_f32_16x16x32_bf16 v[208:211], a[192:195], v[48:51], 0\n"
"buffer_load_dwordx4 v188, s[40:43], s74 offen offset:2048 lds\n"
"v_mfma_f32_16x16x32_bf16 v[216:219], a[192:195], v[64:67], 0\n"
"v_add_u32 v246, 0xffff0000, v246\n"
"v_mfma_f32_16x16x32_bf16 v[224:227], a[192:195], v[80:83], 0\n"
"v_mfma_f32_16x16x32_bf16 v[212:215], a[196:199], v[48:51], 0\n"
"v_mfma_f32_16x16x32_bf16 v[220:223], a[196:199], v[64:67], 0\n"
"v_add_u32 v247, 0xffff0000, v247\n"
"v_mfma_f32_16x16x32_bf16 v[228:231], a[196:199], v[80:83], 0\n"
"buffer_load_dwordx4 v189, s[40:43], s74 offen offset:3072 lds\n"
"s_add_u32 s73, s94, 0x0\n"
"v_mfma_f32_16x16x32_bf16 v[208:211], a[200:203], v[52:55], v[208:211]\n"
"s_and_b32 s73, s73, 0x380\n"
"s_add_u32 s73, s73, s97\n"
"s_add_u32 m0, s68, 0x8000\n"
"v_mfma_f32_16x16x32_bf16 v[216:219], a[200:203], v[68:71], v[216:219]\n"
"s_add_u32 s74, s73, 0x0\n"
"s_waitcnt lgkmcnt(0)\n"
"s_barrier\n"
"v_mfma_f32_16x16x32_bf16 v[224:227], a[200:203], v[84:87], v[224:227]\n"
"ds_read_b64_tr_b16 a[200:201], v246 offset:4096\n"
"ds_read_b64_tr_b16 a[202:203], v246 offset:6144\n"
"v_mfma_f32_16x16x32_bf16 v[212:215], a[204:207], v[52:55], v[212:215]\n"
"ds_read_b64_tr_b16 a[192:193], v246 offset:0\n"
"ds_read_b64_tr_b16 a[194:195], v246 offset:2048\n"
"v_mfma_f32_16x16x32_bf16 v[220:223], a[204:207], v[68:71], v[220:223]\n"
"buffer_load_dwordx4 v188, s[40:43], s74 offen offset:0 lds\n"
"v_mfma_f32_16x16x32_bf16 v[228:231], a[204:207], v[84:87], v[228:231]\n"
"ds_read_b64_tr_b16 a[204:205], v247 offset:4096\n"
"ds_read_b64_tr_b16 a[206:207], v247 offset:6144\n"
"v_mfma_f32_16x16x32_bf16 v[208:211], a[208:211], v[56:59], v[208:211]\n"
"ds_read_b64_tr_b16 a[196:197], v247 offset:0\n"
"ds_read_b64_tr_b16 a[198:199], v247 offset:2048\n"
"v_mfma_f32_16x16x32_bf16 v[216:219], a[208:211], v[72:75], v[216:219]\n"
"buffer_load_dwordx4 v189, s[40:43], s74 offen offset:1024 lds\n"
"s_add_u32 s74, s73, 0x3800\n"
"v_mfma_f32_16x16x32_bf16 v[224:227], a[208:211], v[88:91], v[224:227]\n"
"ds_read_b64_tr_b16 a[208:209], v246 offset:8192\n"
"ds_read_b64_tr_b16 a[210:211], v246 offset:10240\n"
"v_mfma_f32_16x16x32_bf16 v[212:215], a[212:215], v[56:59], v[212:215]\n"
"s_waitcnt vmcnt(19)\n"
"v_cmp_ne_u32 s[22:23], s82, v242\n"
"s_nop 0\n"
"s_and_b32 s22, s22, 0xc000\n"
"s_cbranch_scc1 .Lk1_pw2\n"
".Lk1_pd2:\n"
"v_mfma_f32_16x16x32_bf16 v[220:223], a[212:215], v[72:75], v[220:223]\n"
"s_add_u32 s77, s77, 4096\n"
"buffer_load_dwordx4 a[224:227], v252, s[48:51], s77 offen offset:0 sc1\n"
"v_mfma_f32_16x16x32_bf16 v[228:231], a[212:215], v[88:91], v[228:231]\n"
"ds_read_b64_tr_b16 a[212:213], v247 offset:8192\n"
"ds_read_b64_tr_b16 a[214:215], v247 offset:10240\n"
"v_mfma_f32_16x16x32_bf16 v[208:211], a[216:219], v[60:63], v[208:211]\n"
"v_mfma_f32_16x16x32_bf16 v[216:219], a[216:219], v[76:79], v[216:219]\n"
"buffer_load_dwordx4 a[228:231], v252, s[48:51], s77 offen offset:1024 sc1\n"
"v_mov_b32 v144, 0\n"
"v_mfma_f32_16x16x32_bf16 v[224:227], a[216:219], v[92:95], v[224:227]\n"
"ds_read_b64_tr_b16 a[216:217], v246 offset:12288\n"
"ds_read_b64_tr_b16 a[218:219], v246 offset:14336\n"
"v_mfma_f32_16x16x32_bf16 v[212:215], a[220:223], v[60:63], v[212:215]\n"
"v_mov_b32 v145, 0\n"
"v_mov_b32 v146, 0\n"
"v_mfma_f32_16x16x32_bf16 v[220:223], a[220:223], v[76:79], v[220:223]\n"
"buffer_load_dwordx4 a[232:235], v252, s[48:51], s77 offen offset:2048 sc1\n"
"v_mov_b32 v147, 0\n"
"v_mfma_f32_16x16x32_bf16 v[228:231], a[220:223], v[92:95], v[228:231]\n"
"ds_read_b64_tr_b16 a[220:221], v247 offset:12288\n"
"ds_read_b64_tr_b16 a[222:223], v247 offset:14336\n"
"v_mfma_f32_16x16x32_bf16 v[208:211], a[32:35], a[96:99], v[208:211]\n"
"v_mov_b32 v148, 0\n"
"v_mov_b32 v149, 0\n"
"v_mfma_f32_16x16x32_bf16 v[216:219], a[32:35], a[112:115], v[216:219]\n"
"v_add_u32 v248, 0xffff0000, v248\n"
"v_mov_b32 v150, 0\n"
"v_mfma_f32_16x16x32_bf16 v[224:227], a[32:35], a[128:131], v[224:227]\n"
"ds_read_b64_tr_b16 a[32:33], v248 offset:0\n"
"ds_read_b64_tr_b16 a[34:35], v248 offset:2048\n"
"v_mfma_f32_16x16x32_bf16 v[212:215], a[36:39], a[96:99], v[212:215]\n"
"buffer_load_dwordx4 a[236:239], v252, s[48:51], s77 offen offset:3072 sc1\n"
"s_add_u32 s78, s78, 4096\n"
"v_mfma_f32_16x16x32_bf16 v[220:223], a[36:39], a[112:115], v[220:223]\n"
"v_add_u32 v249, 0xffff0000, v249\n"
"v_mov_b32 v151, 0\n"
"v_mfma_f32_16x16x32_bf16 v[228:231], a[36:39], a[128:131], v[228:231]\n"
"ds_read_b64_tr_b16 a[36:37], v249 offset:0\n"
"ds_read_b64_tr_b16 a[38:39], v249 offset:2048\n"
"v_mfma_f32_16x16x32_bf16 v[208:211], a[40:43], a[100:103], v[208:211]\n"
"buffer_load_dwordx4 a[240:243], v252, s[48:51], s78 offen offset:0 sc1\n"
"v_mov_b32 v152, 0\n"
"v_mfma_f32_16x16x32_bf16 v[216:219], a[40:43], a[116:119], v[216:219]\n"
"v_mov_b32 v153, 0\n"
"v_mov_b32 v154, 0\n"
"v_mfma_f32_16x16x32_bf16 v[224:227], a[40:43], a[132:135], v[224:227]\n"
"ds_read_b64_tr_b16 a[40:41], v248 offset:4096\n"
"ds_read_b64_tr_b16 a[42:43], v248 offset:6144\n"
"v_mfma_f32_16x16x32_bf16 v[212:215], a[44:47], a[100:103], v[212:215]\n"
"buffer_load_dwordx4 a[244:247], v252, s[48:51], s78 offen offset:1024 sc1\n"
"v_mov_b32 v155, 0\n"
"v_mfma_f32_16x16x32_bf16 v[220:223], a[44:47], a[116:119], v[220:223]\n"
"v_mov_b32 v156, 0\n"
"v_mov_b32 v157, 0\n"
"v_mfma_f32_16x16x32_bf16 v[228:231], a[44:47], a[132:135], v[228:231]\n"
"ds_read_b64_tr_b16 a[44:45], v249 offset:4096\n"
"ds_read_b64_tr_b16 a[46:47], v249 offset:6144\n"
"v_mfma_f32_16x16x32_bf16 v[208:211], a[48:51], a[104:107], v[208:211]\n"
"buffer_load_dwordx4 a[248:251], v252, s[48:51], s78 offen offset:2048 sc1\n"
"v_mov_b32 v158, 0\n"
"v_mfma_f32_16x16x32_bf16 v[216:219], a[48:51], a[120:123], v[216:219]\n"
"v_mov_b32 v159, 0\n"
"v_mov_b32 v160, 0\n"
"v_mfma_f32_16x16x32_bf16 v[224:227], a[48:51], a[136:139], v[224:227]\n"
"ds_read_b64_tr_b16 a[48:49], v248 offset:8192\n"
"ds_read_b64_tr_b16 a[50:51], v248 offset:10240\n"
"v_mfma_f32_16x16x32_bf16 v[212:215], a[52:55], a[104:107], v[212:215]\n"
"buffer_load_dwordx4 a[252:255], v252, s[48:51], s78 offen offset:3072 sc1\n"
"v_mov_b32 v161, 0\n"
"v_mfma_f32_16x16x32_bf16 v[220:223], a[52:55], a[120:123], v[220:223]\n"
"v_mov_b32 v162, 0\n"
"v_mov_b32 v163, 0\n"
"v_mfma_f32_16x16x32_bf16 v[228:231], a[52:55], a[136:139], v[228:231]\n"
"ds_read_b64_tr_b16 a[52:53], v249 offset:8192\n"
"ds_read_b64_tr_b16 a[54:55], v249 offset:10240\n"
"v_mfma_f32_16x16x32_bf16 v[208:211], a[56:59], a[108:111], v[208:211]\n"
"buffer_load_dwordx4 v188, s[40:43], s74 offen offset:2048 lds\n"
"v_mov_b32 v164, 0\n"
"v_mfma_f32_16x16x32_bf16 v[216:219], a[56:59], a[124:127], v[216:219]\n"
"v_mov_b32 v165, 0\n"
"v_mov_b32 v166, 0\n"
"v_mfma_f32_16x16x32_bf16 v[224:227], a[56:59], a[140:143], v[224:227]\n"
"ds_read_b64_tr_b16 a[56:57], v248 offset:12288\n"
"ds_read_b64_tr_b16 a[58:59], v248 offset:14336\n"
"v_mfma_f32_16x16x32_bf16 v[212:215], a[60:63], a[108:111], v[212:215]\n"
"buffer_load_dwordx4 v189, s[40:43], s74 offen offset:3072 lds\n"
"s_add_u32 m0, s68, 0x9000\n"
"v_mfma_f32_16x16x32_bf16 v[220:223], a[60:63], a[124:127], v[220:223]\n"
"s_add_u32 s74, s73, 0x8000\n"
"v_mov_b32 v167, 0\n"
"v_mfma_f32_16x16x32_bf16 v[228:231], a[60:63], a[140:143], v[228:231]\n"
"ds_read_b64_tr_b16 a[60:61], v249 offset:12288\n"
"ds_read_b64_tr_b16 a[62:63], v249 offset:14336\n"
"s_waitcnt lgkmcnt(15)\n"
"v_mfma_f32_16x16x32_bf16 v[0:3], a[192:195], v[48:51], 0\n"
"v_mfma_f32_16x16x32_bf16 v[8:11], a[192:195], v[64:67], 0\n"
"v_add_u32 v246, 0xffff0000, v246\n"
"v_mfma_f32_16x16x32_bf16 v[16:19], a[192:195], v[80:83], 0\n"
"buffer_load_dwordx4 v188, s[40:43], s74 offen offset:0 lds\n"
"v_mfma_f32_16x16x32_bf16 v[4:7], a[196:199], v[48:51], 0\n"
"v_mfma_f32_16x16x32_bf16 v[12:15], a[196:199], v[64:67], 0\n"
"v_add_u32 v247, 0xffff0000, v247\n"
"v_mfma_f32_16x16x32_bf16 v[20:23], a[196:199], v[80:83], 0\n"
"v_mfma_f32_16x16x32_bf16 v[0:3], a[200:203], v[52:55], v[0:3]\n"
"s_waitcnt vmcnt(13)\n"
"s_waitcnt lgkmcnt(0)\n"
"s_barrier\n"
"v_mfma_f32_16x16x32_bf16 v[8:11], a[200:203], v[68:71], v[8:11]\n"
"buffer_load_dwordx4 v189, s[40:43], s74 offen offset:1024 lds\n"
"s_add_u32 s74, s73, 0xb800\n"
"v_mfma_f32_16x16x32_bf16 v[16:19], a[200:203], v[84:87], v[16:19]\n"
"ds_read_b64_tr_b16 a[200:201], v246 offset:4096\n"
"ds_read_b64_tr_b16 a[202:203], v246 offset:6144\n"
"v_mfma_f32_16x16x32_bf16 v[4:7], a[204:207], v[52:55], v[4:7]\n"
"ds_read_b64_tr_b16 a[192:193], v246 offset:0\n"
"ds_read_b64_tr_b16 a[194:195], v246 offset:2048\n"
"v_mfma_f32_16x16x32_bf16 v[12:15], a[204:207], v[68:71], v[12:15]\n"
"v_mfma_f32_16x16x32_bf16 v[20:23], a[204:207], v[84:87], v[20:23]\n"
"ds_read_b64_tr_b16 a[204:205], v247 offset:4096\n"
"ds_read_b64_tr_b16 a[206:207], v247 offset:6144\n"
"v_mfma_f32_16x16x32_bf16 v[0:3], a[208:211], v[56:59], v[0:3]\n"
"ds_read_b64_tr_b16 a[196:197], v247 offset:0\n"
"ds_read_b64_tr_b16 a[198:199], v247 offset:2048\n"
"v_mfma_f32_16x16x32_bf16 v[8:11], a[208:211], v[72:75], v[8:11]\n"
"buffer_load_dwordx4 v188, s[40:43], s74 offen offset:2048 lds\n"
"v_mfma_f32_16x16x32_bf16 v[16:19], a[208:211], v[88:91], v[16:19]\n"
"ds_read_b64_tr_b16 a[208:209], v246 offset:8192\n"
"ds_read_b64_tr_b16 a[210:211], v246 offset:10240\n"
"v_mfma_f32_16x16x32_bf16 v[4:7], a[212:215], v[56:59], v[4:7]\n"
"v_mfma_f32_16x16x32_bf16 v[12:15], a[212:215], v[72:75], v[12:15]\n"
"buffer_load_dwordx4 v189, s[40:43], s74 offen offset:3072 lds\n"
"s_add_u32 s73, s94, 0x80\n"
"v_mfma_f32_16x16x32_bf16 v[20:23], a[212:215], v[88:91], v[20:23]\n"
"ds_read_b64_tr_b16 a[212:213], v247 offset:8192\n"
"ds_read_b64_tr_b16 a[214:215], v247 offset:10240\n"
"v_mfma_f32_16x16x32_bf16 v[0:3], a[216:219], v[60:63], v[0:3]\n"
"s_and_b32 s73, s73, 0x380\n"
"s_add_u32 s73, s73, s97\n"
"s_add_u32 m0, s68, 0x18000\n"
"v_mfma_f32_16x16x32_bf16 v[8:11], a[216:219], v[76:79], v[8:11]\n"
"s_add_u32 s74, s73, 0x0\n"
"v_mfma_f32_16x16x32_bf16 v[16:19], a[216:219], v[92:95], v[16:19]\n"
"ds_read_b64_tr_b16 a[216:217], v246 offset:12288\n"
"ds_read_b64_tr_b16 a[218:219], v246 offset:14336\n"
"v_mfma_f32_16x16x32_bf16 v[4:7], a[220:223], v[60:63], v[4:7]\n"
"v_mfma_f32_16x16x32_bf16 v[12:15], a[220:223], v[76:79], v[12:15]\n"
"buffer_load_dwordx4 v188, s[40:43], s74 offen offset:0 lds\n"
"v_mfma_f32_16x16x32_bf16 v[20:23], a[220:223], v[92:95], v[20:23]\n"
"ds_read_b64_tr_b16 a[220:221], v247 offset:12288\n"
"ds_read_b64_tr_b16 a[222:223], v247 offset:14336\n"
"v_mfma_f32_16x16x32_bf16 v[0:3], a[32:35], a[96:99], v[0:3]\n"
"v_mfma_f32_16x16x32_bf16 v[8:11], a[32:35], a[112:115], v[8:11]\n"
"v_add_u32 v248, 0xffff0000, v248\n"
"v_mfma_f32_16x16x32_bf16 v[16:19], a[32:35], a[128:131], v[16:19]\n"
"ds_read_b64_tr_b16 a[32:33], v248 offset:0\n"
"ds_read_b64_tr_b16 a[34:35], v248 offset:2048\n"
"v_mfma_f32_16x16x32_bf16 v[4:7], a[36:39], a[96:99], v[4:7]\n"
"buffer_load_dwordx4 v189, s[40:43], s74 offen offset:1024 lds\n"
"s_add_u32 s74, s73, 0x3800\n"
"v_mfma_f32_16x16x32_bf16 v[12:15], a[36:39], a[112:115], v[12:15]\n"
"v_add_u32 v249, 0xffff0000, v249\n"
"v_mfma_f32_16x16x32_bf16 v[20:23], a[36:39], a[128:131], v[20:23]\n"
"ds_read_b64_tr_b16 a[36:37], v249 offset:0\n"
"ds_read_b64_tr_b16 a[38:39], v249 offset:2048\n"
"v_mfma_f32_16x16x32_bf16 v[0:3], a[40:43], a[100:103], v[0:3]\n"
"v_mfma_f32_16x16x32_bf16 v[8:11], a[40:43], a[116:119], v[8:11]\n"
"v_mfma_f32_16x16x32_bf16 v[16:19], a[40:43], a[132:135], v[16:19]\n"
"ds_read_b64_tr_b16 a[40:41], v248 offset:4096\n"
"ds_read_b64_tr_b16 a[42:43], v248 offset:6144\n"
"v_mfma_f32_16x16x32_bf16 v[4:7], a[44:47], a[100:103], v[4:7]\n"
"buffer_load_dwordx4 v188, s[40:43], s74 offen offset:2048 lds\n"
"v_mfma_f32_16x16x32_bf16 v[12:15], a[44:47], a[116:119], v[12:15]\n"
"v_mfma_f32_16x16x32_bf16 v[20:23], a[44:47], a[132:135], v[20:23]\n"
"ds_read_b64_tr_b16 a[44:45], v249 offset:4096\n"
"ds_read_b64_tr_b16 a[46:47], v249 offset:6144\n"
"v_mfma_f32_16x16x32_bf16 v[0:3], a[48:51], a[104:107], v[0:3]\n"
"v_mfma_f32_16x16x32_bf16 v[8:11], a[48:51], a[120:123], v[8:11]\n"
"v_mfma_f32_16x16x32_bf16 v[16:19], a[48:51], a[136:139], v[16:19]\n"
"ds_read_b64_tr_b16 a[48:49], v248 offset:8192\n"
"ds_read_b64_tr_b16 a[50:51], v248 offset:10240\n"
"v_mfma_f32_16x16x32_bf16 v[4:7], a[52:55], a[104:107], v[4:7]\n"
"buffer_load_dwordx4 v189, s[40:43], s74 offen offset:3072 lds\n"
"s_add_u32 m0, s68, 0x19000\n"
"v_mfma_f32_16x16x32_bf16 v[12:15], a[52:55], a[120:123], v[12:15]\n"
"s_add_u32 s74, s73, 0x8000\n"
"v_mfma_f32_16x16x32_bf16 v[20:23], a[52:55], a[136:139], v[20:23]\n"
"ds_read_b64_tr_b16 a[52:53], v249 offset:8192\n"
"ds_read_b64_tr_b16 a[54:55], v249 offset:10240\n"
"v_mfma_f32_16x16x32_bf16 v[0:3], a[56:59], a[108:111], v[0:3]\n"
"v_mfma_f32_16x16x32_bf16 v[8:11], a[56:59], a[124:127], v[8:11]\n"
"v_mfma_f32_16x16x32_bf16 v[16:19], a[56:59], a[140:143], v[16:19]\n"
"ds_read_b64_tr_b16 a[56:57], v248 offset:12288\n"
"ds_read_b64_tr_b16 a[58:59], v248 offset:14336\n"
"v_mfma_f32_16x16x32_bf16 v[4:7], a[60:63], a[108:111], v[4:7]\n"
"buffer_load_dwordx4 v188, s[40:43], s74 offen offset:0 lds\n"
"v_mfma_f32_16x16x32_bf16 v[12:15], a[60:63], a[124:127], v[12:15]\n"
"v_mfma_f32_16x16x32_bf16 v[20:23], a[60:63], a[140:143], v[20:23]\n"
"ds_read_b64_tr_b16 a[60:61], v249 offset:12288\n"
"ds_read_b64_tr_b16 a[62:63], v249 offset:14336\n"
"s_waitcnt lgkmcnt(15)\n"
"v_mfma_f32_16x16x32_bf16 v[24:27], a[192:195], v[48:51], 0\n"
"v_mfma_f32_16x16x32_bf16 v[32:35], a[192:195], v[64:67], 0\n"
"buffer_load_dwordx4 v189, s[40:43], s74 offen offset:1024 lds\n"
"s_add_u32 s74, s73, 0xb800\n"
"v_mfma_f32_16x16x32_bf16 v[40:43], a[192:195], v[80:83], 0\n"
"v_mfma_f32_16x16x32_bf16 v[28:31], a[196:199], v[48:51], 0\n"
"v_mfma_f32_16x16x32_bf16 v[36:39], a[196:199], v[64:67], 0\n"
"v_mfma_f32_16x16x32_bf16 v[44:47], a[196:199], v[80:83], 0\n"
"v_mfma_f32_16x16x32_bf16 v[24:27], a[200:203], v[52:55], v[24:27]\n"
"buffer_load_dwordx4 v188, s[40:43], s74 offen offset:2048 lds\n"
"s_waitcnt vmcnt(7)\n"
"v_mfma_f32_16x16x32_bf16 v[32:35], a[200:203], v[68:71], v[32:35]\n"
"s_waitcnt lgkmcnt(0)\n"
"s_barrier\n"
"v_mfma_f32_16x16x32_bf16 v[40:43], a[200:203], v[84:87], v[40:43]\n"
"ds_read_b64_tr_b16 a[200:201], v246 offset:36864\n"
"ds_read_b64_tr_b16 a[202:203], v246 offset:38912\n"
"v_mfma_f32_16x16x32_bf16 v[28:31], a[204:207], v[52:55], v[28:31]\n"
"ds_read_b64_tr_b16 a[192:193], v246 offset:32768\n"
"ds_read_b64_tr_b16 a[194:195], v246 offset:34816\n"
"v_mfma_f32_16x16x32_bf16 v[36:39], a[204:207], v[68:71], v[36:39]\n"
"buffer_load_dwordx4 v189, s[40:43], s74 offen offset:3072 lds\n"
"s_add_u32 s73, s94, 0x100\n"
"v_mfma_f32_16x16x32_bf16 v[44:47], a[204:207], v[84:87], v[44:47]\n"
"ds_read_b64_tr_b16 a[204:205], v247 offset:36864\n"
"ds_read_b64_tr_b16 a[206:207], v247 offset:38912\n"
"v_mfma_f32_16x16x32_bf16 v[24:27], a[208:211], v[56:59], v[24:27]\n"
"ds_read_b64_tr_b16 a[196:197], v247 offset:32768\n"
"ds_read_b64_tr_b16 a[198:199], v247 offset:34816\n"
"v_mfma_f32_16x16x32_bf16 v[32:35], a[208:211], v[72:75], v[32:35]\n"
"s_and_b32 s73, s73, 0x380\n"
"s_add_u32 s73, s73, s97\n"
"s_add_u32 m0, s68, 0x20000\n"
"v_mfma_f32_16x16x32_bf16 v[40:43], a[208:211], v[88:91], v[40:43]\n"
"ds_read_b64_tr_b16 a[208:209], v246 offset:40960\n"
"ds_read_b64_tr_b16 a[210:211], v246 offset:43008\n"
"v_mfma_f32_16x16x32_bf16 v[28:31], a[212:215], v[56:59], v[28:31]\n"
"s_add_u32 s74, s73, 0x0\n"
"buffer_load_dwordx4 v188, s[40:43], s74 offen offset:0 lds\n"
"v_mfma_f32_16x16x32_bf16 v[36:39], a[212:215], v[72:75], v[36:39]\n"
"v_mfma_f32_16x16x32_bf16 v[44:47], a[212:215], v[88:91], v[44:47]\n"
"ds_read_b64_tr_b16 a[212:213], v247 offset:40960\n"
"ds_read_b64_tr_b16 a[214:215], v247 offset:43008\n"
"v_mfma_f32_16x16x32_bf16 v[24:27], a[216:219], v[60:63], v[24:27]\n"
"v_mfma_f32_16x16x32_bf16 v[32:35], a[216:219], v[76:79], v[32:35]\n"
"v_mfma_f32_16x16x32_bf16 v[40:43], a[216:219], v[92:95], v[40:43]\n"
"ds_read_b64_tr_b16 a[216:217], v246 offset:45056\n"
"ds_read_b64_tr_b16 a[218:219], v246 offset:47104\n"
"v_mfma_f32_16x16x32_bf16 v[28:31], a[220:223], v[60:63], v[28:31]\n"
"buffer_load_dwordx4 v189, s[40:43], s74 offen offset:1024 lds\n"
"s_add_u32 s74, s73, 0x3800\n"
"v_mfma_f32_16x16x32_bf16 v[36:39], a[220:223], v[76:79], v[36:39]\n"
"v_mfma_f32_16x16x32_bf16 v[44:47], a[220:223], v[92:95], v[44:47]\n"
"ds_read_b64_tr_b16 a[220:221], v247 offset:45056\n"
"ds_read_b64_tr_b16 a[222:223], v247 offset:47104\n"
"v_mfma_f32_16x16x32_bf16 v[24:27], a[32:35], a[96:99], v[24:27]\n"
"v_mfma_f32_16x16x32_bf16 v[32:35], a[32:35], a[112:115], v[32:35]\n"
"v_mfma_f32_16x16x32_bf16 v[40:43], a[32:35], a[128:131], v[40:43]\n"
"ds_read_b64_tr_b16 a[32:33], v248 offset:32768\n"
"ds_read_b64_tr_b16 a[34:35], v248 offset:34816\n"
"v_mfma_f32_16x16x32_bf16 v[28:31], a[36:39], a[96:99], v[28:31]\n"
"buffer_load_dwordx4 v188, s[40:43], s74 offen offset:2048 lds\n"
"v_mfma_f32_16x16x32_bf16 v[36:39], a[36:39], a[112:115], v[36:39]\n"
"v_mfma_f32_16x16x32_bf16 v[44:47], a[36:39], a[128:131], v[44:47]\n"
"ds_read_b64_tr_b16 a[36:37], v249 offset:32768\n"
"ds_read_b64_tr_b16 a[38:39], v249 offset:34816\n"
"v_mfma_f32_16x16x32_bf16 v[24:27], a[40:43], a[100:103], v[24:27]\n"
"v_mfma_f32_16x16x32_bf16 v[32:35], a[40:43], a[116:119], v[32:35]\n"
"buffer_load_dwordx4 v189, s[40:43], s74 offen offset:3072 lds\n"
"s_add_u32 m0, s68, 0x21000\n"
"v_mfma_f32_16x16x32_bf16 v[40:43], a[40:43], a[132:135], v[40:43]\n"
"ds_read_b64_tr_b16 a[40:41], v248 offset:36864\n"
"ds_read_b64_tr_b16 a[42:43], v248 offset:38912\n"
"v_mfma_f32_16x16x32_bf16 v[28:31], a[44:47], a[100:103], v[28:31]\n"
"s_add_u32 s74, s73, 0x8000\n"
"v_mfma_f32_16x16x32_bf16 v[36:39], a[44:47], a[116:119], v[36:39]\n"
"v_mfma_f32_16x16x32_bf16 v[44:47], a[44:47], a[132:135], v[44:47]\n"
"ds_read_b64_tr_b16 a[44:45], v249 offset:36864\n"
"ds_read_b64_tr_b16 a[46:47], v249 offset:38912\n"
"v_mfma_f32_16x16x32_bf16 v[24:27], a[48:51], a[104:107], v[24:27]\n"
"v_mfma_f32_16x16x32_bf16 v[32:35], a[48:51], a[120:123], v[32:35]\n"
"buffer_load_dwordx4 v188, s[40:43], s74 offen offset:0 lds\n"
"v_mfma_f32_16x16x32_bf16 v[40:43], a[48:51], a[136:139], v[40:43]\n"
"ds_read_b64_tr_b16 a[48:49], v248 offset:40960\n"
"ds_read_b64_tr_b16 a[50:51], v248 offset:43008\n"
"v_mfma_f32_16x16x32_bf16 v[28:31], a[52:55], a[104:107], v[28:31]\n"
"v_mfma_f32_16x16x32_bf16 v[36:39], a[52:55], a[120:123], v[36:39]\n"
"v_mfma_f32_16x16x32_bf16 v[44:47], a[52:55], a[136:139], v[44:47]\n"
"ds_read_b64_tr_b16 a[52:53], v249 offset:40960\n"
"ds_read_b64_tr_b16 a[54:55], v249 offset:43008\n"
"v_mfma_f32_16x16x32_bf16 v[24:27], a[56:59], a[108:111], v[24:27]\n"
"buffer_load_dwordx4 v189, s[40:43], s74 offen offset:1024 lds\n"
"s_add_u32 s74, s73, 0xb800\n"
"v_mfma_f32_16x16x32_bf16 v[32:35], a[56:59], a[124:127], v[32:35]\n"
"v_mfma_f32_16x16x32_bf16 v[40:43], a[56:59], a[140:143], v[40:43]\n"
"ds_read_b64_tr_b16 a[56:57], v248 offset:45056\n"
"ds_read_b64_tr_b16 a[58:59], v248 offset:47104\n"
"v_mfma_f32_16x16x32_bf16 v[28:31], a[60:63], a[108:111], v[28:31]\n"
"v_mfma_f32_16x16x32_bf16 v[36:39], a[60:63], a[124:127], v[36:39]\n"
"v_mfma_f32_16x16x32_bf16 v[44:47], a[60:63], a[140:143], v[44:47]\n"
"ds_read_b64_tr_b16 a[60:61], v249 offset:45056\n"
"ds_read_b64_tr_b16 a[62:63], v249 offset:47104\n"
"s_waitcnt lgkmcnt(15)\n"
"v_mfma_f32_16x16x32_bf16 v[96:99], a[192:195], a[0:3], v[96:99]\n"
"buffer_load_dwordx4 v188, s[40:43], s74 offen offset:2048 lds\n"
"v_mfma_f32_16x16x32_bf16 v[104:107], a[192:195], a[64:67], v[104:107]\n"
"v_add_u32 v246, 0x10000, v246\n"
"v_mfma_f32_16x16x32_bf16 v[112:115], a[192:195], a[224:227], v[112:115]\n"
"v_mfma_f32_16x16x32_bf16 v[100:103], a[196:199], a[0:3], v[100:103]\n"
"v_mfma_f32_16x16x32_bf16 v[108:111], a[196:199], a[64:67], v[108:111]\n"
"v_add_u32 v247, 0x10000, v247\n"
"v_mfma_f32_16x16x32_bf16 v[116:119], a[196:199], a[224:227], v[116:119]\n"
"buffer_load_dwordx4 v189, s[40:43], s74 offen offset:3072 lds\n"
"s_add_u32 s73, s94, 0x180\n"
"v_mfma_f32_16x16x32_bf16 v[96:99], a[200:203], a[4:7], v[96:99]\n"
"s_and_b32 s73, s73, 0x380\n"
"s_add_u32 s73, s73, s97\n"
"s_add_u32 m0, s68, 0x10000\n"
"v_mfma_f32_16x16x32_bf16 v[104:107], a[200:203], a[68:71], v[104:107]\n"
"s_add_u32 s74, s73, 0x0\n"
"s_waitcnt vmcnt(8)\n"
"s_waitcnt lgkmcnt(0)\n"
"s_barrier\n"
"v_mfma_f32_16x16x32_bf16 v[112:115], a[200:203], a[228:231], v[112:115]\n"
"ds_read_b64_tr_b16 a[200:201], v246 offset:36864\n"
"ds_read_b64_tr_b16 a[202:203], v246 offset:38912\n"
"v_mfma_f32_16x16x32_bf16 v[100:103], a[204:207], a[4:7], v[100:103]\n"
"ds_read_b64_tr_b16 a[192:193], v246 offset:32768\n"
"ds_read_b64_tr_b16 a[194:195], v246 offset:34816\n"
"v_mfma_f32_16x16x32_bf16 v[108:111], a[204:207], a[68:71], v[108:111]\n"
"buffer_load_dwordx4 v188, s[40:43], s74 offen offset:0 lds\n"
"v_mfma_f32_16x16x32_bf16 v[116:119], a[204:207], a[228:231], v[116:119]\n"
"ds_read_b64_tr_b16 a[204:205], v247 offset:36864\n"
"ds_read_b64_tr_b16 a[206:207], v247 offset:38912\n"
"v_mfma_f32_16x16x32_bf16 v[96:99], a[208:211], a[8:11], v[96:99]\n"
"ds_read_b64_tr_b16 a[196:197], v247 offset:32768\n"
"ds_read_b64_tr_b16 a[198:199], v247 offset:34816\n"
"v_mfma_f32_16x16x32_bf16 v[104:107], a[208:211], a[72:75], v[104:107]\n"
"buffer_load_dwordx4 v189, s[40:43], s74 offen offset:1024 lds\n"
"s_add_u32 s74, s73, 0x3800\n"
"v_mfma_f32_16x16x32_bf16 v[112:115], a[208:211], a[232:235], v[112:115]\n"
"ds_read_b64_tr_b16 a[208:209], v246 offset:40960\n"
"ds_read_b64_tr_b16 a[210:211], v246 offset:43008\n"
"v_mfma_f32_16x16x32_bf16 v[100:103], a[212:215], a[8:11], v[100:103]\n"
"v_mfma_f32_16x16x32_bf16 v[108:111], a[212:215], a[72:75], v[108:111]\n"
"v_mfma_f32_16x16x32_bf16 v[116:119], a[212:215], a[232:235], v[116:119]\n"
"ds_read_b64_tr_b16 a[212:213], v247 offset:40960\n"
"ds_read_b64_tr_b16 a[214:215], v247 offset:43008\n"
"v_mfma_f32_16x16x32_bf16 v[96:99], a[216:219], a[12:15], v[96:99]\n"
"v_mfma_f32_16x16x32_bf16 v[104:107], a[216:219], a[76:79], v[104:107]\n"
"buffer_load_dwordx4 v188, s[40:43], s74 offen offset:2048 lds\n"
"v_mfma_f32_16x16x32_bf16 v[112:115], a[216:219], a[236:239], v[112:115]\n"
"ds_read_b64_tr_b16 a[216:217], v246 offset:45056\n"
"ds_read_b64_tr_b16 a[218:219], v246 offset:47104\n"
"v_mfma_f32_16x16x32_bf16 v[100:103], a[220:223], a[12:15], v[100:103]\n"
"v_mfma_f32_16x16x32_bf16 v[108:111], a[220:223], a[76:79], v[108:111]\n"
"v_mfma_f32_16x16x32_bf16 v[116:119], a[220:223], a[236:239], v[116:119]\n"
"ds_read_b64_tr_b16 a[220:221], v247 offset:45056\n"
"ds_read_b64_tr_b16 a[222:223], v247 offset:47104\n"
"v_mfma_f32_16x16x32_bf16 v[96:99], a[32:35], a[16:19], v[96:99]\n"
"buffer_load_dwordx4 v189, s[40:43], s74 offen offset:3072 lds\n"
"s_add_u32 m0, s68, 0x11000\n"
"v_mfma_f32_16x16x32_bf16 v[104:107], a[32:35], a[80:83], v[104:107]\n"
"v_add_u32 v248, 0x10000, v248\n"
"s_add_u32 s74, s73, 0x8000\n"
"v_mfma_f32_16x16x32_bf16 v[112:115], a[32:35], a[240:243], v[112:115]\n"
"ds_read_b64_tr_b16 a[32:33], v248 offset:32768\n"
"ds_read_b64_tr_b16 a[34:35], v248 offset:34816\n"
"v_mfma_f32_16x16x32_bf16 v[100:103], a[36:39], a[16:19], v[100:103]\n"
"v_mfma_f32_16x16x32_bf16 v[108:111], a[36:39], a[80:83], v[108:111]\n"
"v_add_u32 v249, 0x10000, v249\n"
"v_mfma_f32_16x16x32_bf16 v[116:119], a[36:39], a[240:243], v[116:119]\n"
"ds_read_b64_tr_b16 a[36:37], v249 offset:32768\n"
"ds_read_b64_tr_b16 a[38:39], v249 offset:34816\n"
"v_mfma_f32_16x16x32_bf16 v[96:99], a[40:43], a[20:23], v[96:99]\n"
"buffer_load_dwordx4 v188, s[40:43], s74 offen offset:0 lds\n"
"v_mfma_f32_16x16x32_bf16 v[104:107], a[40:43], a[84:87], v[104:107]\n"
"v_mfma_f32_16x16x32_bf16 v[112:115], a[40:43], a[244:247], v[112:115]\n"
"ds_read_b64_tr_b16 a[40:41], v248 offset:36864\n"
"ds_read_b64_tr_b16 a[42:43], v248 offset:38912\n"
"v_mfma_f32_16x16x32_bf16 v[100:103], a[44:47], a[20:23], v[100:103]\n"
"v_mfma_f32_16x16x32_bf16 v[108:111], a[44:47], a[84:87], v[108:111]\n"
"v_mfma_f32_16x16x32_bf16 v[116:119], a[44:47], a[244:247], v[116:119]\n"
"ds_read_b64_tr_b16 a[44:45], v249 offset:36864\n"
"ds_read_b64_tr_b16 a[46:47], v249 offset:38912\n"
"v_mfma_f32_16x16x32_bf16 v[96:99], a[48:51], a[24:27], v[96:99]\n"
"buffer_load_dwordx4 v189, s[40:43], s74 offen offset:1024 lds\n"
"s_add_u32 s74, s73, 0xb800\n"
"v_mfma_f32_16x16x32_bf16 v[104:107], a[48:51], a[88:91], v[104:107]\n"
"v_mfma_f32_16x16x32_bf16 v[112:115], a[48:51], a[248:251], v[112:115]\n"
"ds_read_b64_tr_b16 a[48:49], v248 offset:40960\n"
"ds_read_b64_tr_b16 a[50:51], v248 offset:43008\n"
"v_mfma_f32_16x16x32_bf16 v[100:103], a[52:55], a[24:27], v[100:103]\n"
"v_mfma_f32_16x16x32_bf16 v[108:111], a[52:55], a[88:91], v[108:111]\n"
"v_mfma_f32_16x16x32_bf16 v[116:119], a[52:55], a[248:251], v[116:119]\n"
"ds_read_b64_tr_b16 a[52:53], v249 offset:40960\n"
"ds_read_b64_tr_b16 a[54:55], v249 offset:43008\n"
"v_mfma_f32_16x16x32_bf16 v[96:99], a[56:59], a[28:31], v[96:99]\n"
"buffer_load_dwordx4 v188, s[40:43], s74 offen offset:2048 lds\n"
"v_mfma_f32_16x16x32_bf16 v[104:107], a[56:59], a[92:95], v[104:107]\n"
"v_mfma_f32_16x16x32_bf16 v[112:115], a[56:59], a[252:255], v[112:115]\n"
"ds_read_b64_tr_b16 a[56:57], v248 offset:45056\n"
"ds_read_b64_tr_b16 a[58:59], v248 offset:47104\n"
"v_mfma_f32_16x16x32_bf16 v[100:103], a[60:63], a[28:31], v[100:103]\n"
"v_mfma_f32_16x16x32_bf16 v[108:111], a[60:63], a[92:95], v[108:111]\n"
"buffer_load_dwordx4 v189, s[40:43], s74 offen offset:3072 lds\n"
"s_add_u32 s73, s94, 0x200\n"
"v_mfma_f32_16x16x32_bf16 v[116:119], a[60:63], a[252:255], v[116:119]\n"
"ds_read_b64_tr_b16 a[60:61], v249 offset:45056\n"
"ds_read_b64_tr_b16 a[62:63], v249 offset:47104\n"
"s_waitcnt lgkmcnt(15)\n"
"v_mfma_f32_16x16x32_bf16 v[120:123], a[192:195], a[0:3], v[120:123]\n"
"s_and_b32 s73, s73, 0x380\n"
"s_add_u32 s73, s73, s97\n"
"s_add_u32 m0, s68, 0x0\n"
"v_mfma_f32_16x16x32_bf16 v[128:131], a[192:195], a[64:67], v[128:131]\n"
"v_add_u32 v246, 0x10000, v246\n"
"s_add_u32 s74, s73, 0x0\n"
"v_mfma_f32_16x16x32_bf16 v[136:139], a[192:195], a[224:227], v[136:139]\n"
"v_cvt_pk_bf16_f32 v96, v96, v97\n"
"v_cvt_pk_bf16_f32 v97, v98, v99\n"
"v_mfma_f32_16x16x32_bf16 v[124:127], a[196:199], a[0:3], v[124:127]\n"
"buffer_load_dwordx4 v188, s[40:43], s74 offen offset:0 lds\n"
"v_cvt_pk_bf16_f32 v104, v104, v105\n"
"v_mfma_f32_16x16x32_bf16 v[132:135], a[196:199], a[64:67], v[132:135]\n"
"v_add_u32 v247, 0x10000, v247\n"
"v_cvt_pk_bf16_f32 v105, v106, v107\n"
"v_mfma_f32_16x16x32_bf16 v[140:143], a[196:199], a[224:227], v[140:143]\n"
"v_cvt_pk_bf16_f32 v112, v112, v113\n"
"v_cvt_pk_bf16_f32 v113, v114, v115\n"
"v_mfma_f32_16x16x32_bf16 v[120:123], a[200:203], a[4:7], v[120:123]\n"
"v_cvt_pk_bf16_f32 v98, v100, v101\n"
"v_cvt_pk_bf16_f32 v99, v102, v103\n"
"v_mfma_f32_16x16x32_bf16 v[128:131], a[200:203], a[68:71], v[128:131]\n"
"v_cvt_pk_bf16_f32 v106, v108, v109\n"
"v_cvt_pk_bf16_f32 v107, v110, v111\n"
"v_cvt_pk_bf16_f32 v114, v116, v117\n"
"v_cvt_pk_bf16_f32 v115, v118, v119\n"
"s_add_u32 s56, s94, 0x0\n"
"s_and_b32 s56, s56, 0x3c0\n"
"s_add_u32 s56, s56, 0x0\n"
"s_mul_i32 s56, s56, 192\n"
"buffer_store_dwordx4 v[96:99], v252, s[48:51], s56 offen offset:0 sc1\n"
"s_waitcnt vmcnt(10)\n"
"s_waitcnt lgkmcnt(0)\n"
"s_barrier\n"
"v_mfma_f32_16x16x32_bf16 v[136:139], a[200:203], a[228:231], v[136:139]\n"
"ds_read_b64_tr_b16 a[200:201], v246 offset:4096\n"
"ds_read_b64_tr_b16 a[202:203], v246 offset:6144\n"
"v_mfma_f32_16x16x32_bf16 v[124:127], a[204:207], a[4:7], v[124:127]\n"
"ds_read_b64_tr_b16 a[192:193], v246 offset:0\n"
"ds_read_b64_tr_b16 a[194:195], v246 offset:2048\n"
"v_mfma_f32_16x16x32_bf16 v[132:135], a[204:207], a[68:71], v[132:135]\n"
"buffer_store_dwordx4 v[104:107], v252, s[48:51], s56 offen offset:1024 sc1\n"
"v_mfma_f32_16x16x32_bf16 v[140:143], a[204:207], a[228:231], v[140:143]\n"
"ds_read_b64_tr_b16 a[204:205], v247 offset:4096\n"
"ds_read_b64_tr_b16 a[206:207], v247 offset:6144\n"
"v_mfma_f32_16x16x32_bf16 v[120:123], a[208:211], a[8:11], v[120:123]\n"
"ds_read_b64_tr_b16 a[196:197], v247 offset:0\n"
"ds_read_b64_tr_b16 a[198:199], v247 offset:2048\n"
"v_mfma_f32_16x16x32_bf16 v[128:131], a[208:211], a[72:75], v[128:131]\n"
"buffer_load_dwordx4 v189, s[40:43], s74 offen offset:1024 lds\n"
"s_add_u32 s74, s73, 0x3800\n"
"v_mfma_f32_16x16x32_bf16 v[136:139], a[208:211], a[232:235], v[136:139]\n"
"ds_read_b64_tr_b16 a[208:209], v246 offset:8192\n"
"ds_read_b64_tr_b16 a[210:211], v246 offset:10240\n"
"v_mfma_f32_16x16x32_bf16 v[124:127], a[212:215], a[8:11], v[124:127]\n"
"v_mfma_f32_16x16x32_bf16 v[132:135], a[212:215], a[72:75], v[132:135]\n"
"buffer_load_dwordx4 v188, s[40:43], s74 offen offset:2048 lds\n"
"v_mfma_f32_16x16x32_bf16 v[140:143], a[212:215], a[232:235], v[140:143]\n"
"ds_read_b64_tr_b16 a[212:213], v247 offset:8192\n"
"ds_read_b64_tr_b16 a[214:215], v247 offset:10240\n"
"v_mfma_f32_16x16x32_bf16 v[120:123], a[216:219], a[12:15], v[120:123]\n"
"v_mfma_f32_16x16x32_bf16 v[128:131], a[216:219], a[76:79], v[128:131]\n"
"buffer_store_dwordx4 v[112:115], v252, s[48:51], s56 offen offset:2048 sc1\n"
"v_mfma_f32_16x16x32_bf16 v[136:139], a[216:219], a[236:239], v[136:139]\n"
"ds_read_b64_tr_b16 a[216:217], v246 offset:12288\n"
"ds_read_b64_tr_b16 a[218:219], v246 offset:14336\n"
"v_mfma_f32_16x16x32_bf16 v[124:127], a[220:223], a[12:15], v[124:127]\n"
"buffer_load_dwordx4 v189, s[40:43], s74 offen offset:3072 lds\n"
"s_add_u32 m0, s68, 0x1000\n"
"v_mfma_f32_16x16x32_bf16 v[132:135], a[220:223], a[76:79], v[132:135]\n"
"s_add_u32 s74, s73, 0x8000\n"
"v_mfma_f32_16x16x32_bf16 v[140:143], a[220:223], a[236:239], v[140:143]\n"
"ds_read_b64_tr_b16 a[220:221], v247 offset:12288\n"
"ds_read_b64_tr_b16 a[222:223], v247 offset:14336\n"
"v_mfma_f32_16x16x32_bf16 v[120:123], a[32:35], a[16:19], v[120:123]\n"
"v_mfma_f32_16x16x32_bf16 v[128:131], a[32:35], a[80:83], v[128:131]\n"
"v_add_u32 v248, 0x10000, v248\n"
"v_mfma_f32_16x16x32_bf16 v[136:139], a[32:35], a[240:243], v[136:139]\n"
"ds_read_b64_tr_b16 a[32:33], v248 offset:0\n"
"ds_read_b64_tr_b16 a[34:35], v248 offset:2048\n"
"v_mfma_f32_16x16x32_bf16 v[124:127], a[36:39], a[16:19], v[124:127]\n"
"buffer_load_dwordx4 v188, s[40:43], s74 offen offset:0 lds\n"
"v_mfma_f32_16x16x32_bf16 v[132:135], a[36:39], a[80:83], v[132:135]\n"
"v_add_u32 v249, 0x10000, v249\n"
"v_mfma_f32_16x16x32_bf16 v[140:143], a[36:39], a[240:243], v[140:143]\n"
"ds_read_b64_tr_b16 a[36:37], v249 offset:0\n"
"ds_read_b64_tr_b16 a[38:39], v249 offset:2048\n"
"v_mfma_f32_16x16x32_bf16 v[120:123], a[40:43], a[20:23], v[120:123]\n"
"v_mfma_f32_16x16x32_bf16 v[128:131], a[40:43], a[84:87], v[128:131]\n"
"v_mfma_f32_16x16x32_bf16 v[136:139], a[40:43], a[244:247], v[136:139]\n"
"ds_read_b64_tr_b16 a[40:41], v248 offset:4096\n"
"ds_read_b64_tr_b16 a[42:43], v248 offset:6144\n"
"v_mfma_f32_16x16x32_bf16 v[124:127], a[44:47], a[20:23], v[124:127]\n"
"buffer_load_dwordx4 v189, s[40:43], s74 offen offset:1024 lds\n"
"s_add_u32 s74, s73, 0xb800\n"
"v_mfma_f32_16x16x32_bf16 v[132:135], a[44:47], a[84:87], v[132:135]\n"
"v_mfma_f32_16x16x32_bf16 v[140:143], a[44:47], a[244:247], v[140:143]\n"
"ds_read_b64_tr_b16 a[44:45], v249 offset:4096\n"
"ds_read_b64_tr_b16 a[46:47], v249 offset:6144\n"
"v_mfma_f32_16x16x32_bf16 v[120:123], a[48:51], a[24:27], v[120:123]\n"
"v_mfma_f32_16x16x32_bf16 v[128:131], a[48:51], a[88:91], v[128:131]\n"
"buffer_load_dwordx4 v188, s[40:43], s74 offen offset:2048 lds\n"
"v_mfma_f32_16x16x32_bf16 v[136:139], a[48:51], a[248:251], v[136:139]\n"
"ds_read_b64_tr_b16 a[48:49], v248 offset:8192\n"
"ds_read_b64_tr_b16 a[50:51], v248 offset:10240\n"
"v_mfma_f32_16x16x32_bf16 v[124:127], a[52:55], a[24:27], v[124:127]\n"
"v_mfma_f32_16x16x32_bf16 v[132:135], a[52:55], a[88:91], v[132:135]\n"
"v_mfma_f32_16x16x32_bf16 v[140:143], a[52:55], a[248:251], v[140:143]\n"
"ds_read_b64_tr_b16 a[52:53], v249 offset:8192\n"
"ds_read_b64_tr_b16 a[54:55], v249 offset:10240\n"
"v_mfma_f32_16x16x32_bf16 v[120:123], a[56:59], a[28:31], v[120:123]\n"
"v_mfma_f32_16x16x32_bf16 v[128:131], a[56:59], a[92:95], v[128:131]\n"
"buffer_load_dwordx4 v189, s[40:43], s74 offen offset:3072 lds\n"
"s_add_u32 s73, s94, 0x280\n"
"v_mfma_f32_16x16x32_bf16 v[136:139], a[56:59], a[252:255], v[136:139]\n"
"ds_read_b64_tr_b16 a[56:57], v248 offset:12288\n"
"ds_read_b64_tr_b16 a[58:59], v248 offset:14336\n"
"v_mfma_f32_16x16x32_bf16 v[124:127], a[60:63], a[28:31], v[124:127]\n"
"s_and_b32 s73, s73, 0x380\n"
"s_add_u32 s73, s73, s96\n"
"s_add_u32 m0, s68, 0x8000\n"
"v_mfma_f32_16x16x32_bf16 v[132:135], a[60:63], a[92:95], v[132:135]\n"
"s_add_u32 s74, s73, 0x0\n"
"v_mfma_f32_16x16x32_bf16 v[140:143], a[60:63], a[252:255], v[140:143]\n"
"ds_read_b64_tr_b16 a[60:61], v249 offset:12288\n"
"ds_read_b64_tr_b16 a[62:63], v249 offset:14336\n"
"s_waitcnt lgkmcnt(15)\n"
"v_mfma_f32_16x16x32_bf16 v[208:211], a[192:195], a[0:3], v[208:211]\n"
"buffer_load_dwordx4 v188, s[40:43], s74 offen offset:0 lds\n"
"v_cvt_pk_bf16_f32 v120, v120, v121\n"
"v_mfma_f32_16x16x32_bf16 v[216:219], a[192:195], a[64:67], v[216:219]\n"
"v_add_u32 v246, 0xffff0000, v246\n"
"v_cvt_pk_bf16_f32 v121, v122, v123\n"
"v_mfma_f32_16x16x32_bf16 v[224:227], a[192:195], a[224:227], v[224:227]\n"
"v_cvt_pk_bf16_f32 v128, v128, v129\n"
"v_cvt_pk_bf16_f32 v129, v130, v131\n"
"v_mfma_f32_16x16x32_bf16 v[212:215], a[196:199], a[0:3], v[212:215]\n"
"v_cvt_pk_bf16_f32 v136, v136, v137\n"
"v_cvt_pk_bf16_f32 v137, v138, v139\n"
"v_mfma_f32_16x16x32_bf16 v[220:223], a[196:199], a[64:67], v[220:223]\n"
"v_add_u32 v247, 0xffff0000, v247\n"
"v_cvt_pk_bf16_f32 v122, v124, v125\n"
"v_mfma_f32_16x16x32_bf16 v[228:231], a[196:199], a[224:227], v[228:231]\n"
"v_cvt_pk_bf16_f32 v123, v126, v127\n"
"v_cvt_pk_bf16_f32 v130, v132, v133\n"
"v_mfma_f32_16x16x32_bf16 v[208:211], a[200:203], a[4:7], v[208:211]\n"
"buffer_load_dwordx4 v189, s[40:43], s74 offen offset:1024 lds\n"
"s_add_u32 s74, s73, 0x3800\n"
"v_mfma_f32_16x16x32_bf16 v[216:219], a[200:203], a[68:71], v[216:219]\n"
"v_cvt_pk_bf16_f32 v131, v134, v135\n"
"v_cvt_pk_bf16_f32 v138, v140, v141\n"
"v_cvt_pk_bf16_f32 v139, v142, v143\n"
"s_add_u32 s56, s94, 0x80\n"
"s_and_b32 s56, s56, 0x3c0\n"
"s_add_u32 s56, s56, 0x400\n"
"s_mul_i32 s56, s56, 192\n"
"buffer_store_dwordx4 v[120:123], v252, s[48:51], s56 offen offset:0 sc1\n"
"s_waitcnt vmcnt(14)\n"
"s_waitcnt lgkmcnt(0)\n"
"s_barrier\n"
"v_mfma_f32_16x16x32_bf16 v[224:227], a[200:203], a[228:231], v[224:227]\n"
"ds_read_b64_tr_b16 a[200:201], v246 offset:4096\n"
"ds_read_b64_tr_b16 a[202:203], v246 offset:6144\n"
"v_mfma_f32_16x16x32_bf16 v[212:215], a[204:207], a[4:7], v[212:215]\n"
"ds_read_b64_tr_b16 a[192:193], v246 offset:0\n"
"ds_read_b64_tr_b16 a[194:195], v246 offset:2048\n"
"v_mfma_f32_16x16x32_bf16 v[220:223], a[204:207], a[68:71], v[220:223]\n"
"buffer_store_dwordx4 v[128:131], v252, s[48:51], s56 offen offset:1024 sc1\n"
"v_mfma_f32_16x16x32_bf16 v[228:231], a[204:207], a[228:231], v[228:231]\n"
"ds_read_b64_tr_b16 a[204:205], v247 offset:4096\n"
"ds_read_b64_tr_b16 a[206:207], v247 offset:6144\n"
"v_mfma_f32_16x16x32_bf16 v[208:211], a[208:211], a[8:11], v[208:211]\n"
"ds_read_b64_tr_b16 a[196:197], v247 offset:0\n"
"ds_read_b64_tr_b16 a[198:199], v247 offset:2048\n"
"v_mfma_f32_16x16x32_bf16 v[216:219], a[208:211], a[72:75], v[216:219]\n"
"buffer_load_dwordx4 v188, s[40:43], s74 offen offset:2048 lds\n"
"v_mfma_f32_16x16x32_bf16 v[224:227], a[208:211], a[232:235], v[224:227]\n"
"ds_read_b64_tr_b16 a[208:209], v246 offset:8192\n"
"ds_read_b64_tr_b16 a[210:211], v246 offset:10240\n"
"v_mfma_f32_16x16x32_bf16 v[212:215], a[212:215], a[8:11], v[212:215]\n"
"v_mfma_f32_16x16x32_bf16 v[220:223], a[212:215], a[72:75], v[220:223]\n"
"buffer_load_dwordx4 v189, s[40:43], s74 offen offset:3072 lds\n"
"s_add_u32 m0, s68, 0x9000\n"
"v_mfma_f32_16x16x32_bf16 v[228:231], a[212:215], a[232:235], v[228:231]\n"
"ds_read_b64_tr_b16 a[212:213], v247 offset:8192\n"
"ds_read_b64_tr_b16 a[214:215], v247 offset:10240\n"
"v_mfma_f32_16x16x32_bf16 v[208:211], a[216:219], a[12:15], v[208:211]\n"
"s_add_u32 s74, s73, 0x8000\n"
"buffer_store_dwordx4 v[136:139], v252, s[48:51], s56 offen offset:2048 sc1\n"
"v_mfma_f32_16x16x32_bf16 v[216:219], a[216:219], a[76:79], v[216:219]\n"
"v_mfma_f32_16x16x32_bf16 v[224:227], a[216:219], a[236:239], v[224:227]\n"
"ds_read_b64_tr_b16 a[216:217], v246 offset:12288\n"
"ds_read_b64_tr_b16 a[218:219], v246 offset:14336\n"
"v_mfma_f32_16x16x32_bf16 v[212:215], a[220:223], a[12:15], v[212:215]\n"
"buffer_load_dwordx4 v188, s[40:43], s74 offen offset:0 lds\n"
"v_mfma_f32_16x16x32_bf16 v[220:223], a[220:223], a[76:79], v[220:223]\n"
"v_mfma_f32_16x16x32_bf16 v[228:231], a[220:223], a[236:239], v[228:231]\n"
"ds_read_b64_tr_b16 a[220:221], v247 offset:12288\n"
"ds_read_b64_tr_b16 a[222:223], v247 offset:14336\n"
"v_mfma_f32_16x16x32_bf16 v[208:211], a[32:35], a[16:19], v[208:211]\n"
"v_mfma_f32_16x16x32_bf16 v[216:219], a[32:35], a[80:83], v[216:219]\n"
"v_add_u32 v248, 0xffff0000, v248\n"
"v_mfma_f32_16x16x32_bf16 v[224:227], a[32:35], a[240:243], v[224:227]\n"
"ds_read_b64_tr_b16 a[32:33], v248 offset:0\n"
"ds_read_b64_tr_b16 a[34:35], v248 offset:2048\n"
"v_mfma_f32_16x16x32_bf16 v[212:215], a[36:39], a[16:19], v[212:215]\n"
"buffer_load_dwordx4 v189, s[40:43], s74 offen offset:1024 lds\n"
"s_add_u32 s74, s73, 0xb800\n"
"v_mfma_f32_16x16x32_bf16 v[220:223], a[36:39], a[80:83], v[220:223]\n"
"v_add_u32 v249, 0xffff0000, v249\n"
"v_mfma_f32_16x16x32_bf16 v[228:231], a[36:39], a[240:243], v[228:231]\n"
"ds_read_b64_tr_b16 a[36:37], v249 offset:0\n"
"ds_read_b64_tr_b16 a[38:39], v249 offset:2048\n"
"v_mfma_f32_16x16x32_bf16 v[208:211], a[40:43], a[20:23], v[208:211]\n"
"v_mfma_f32_16x16x32_bf16 v[216:219], a[40:43], a[84:87], v[216:219]\n"
"v_mfma_f32_16x16x32_bf16 v[224:227], a[40:43], a[244:247], v[224:227]\n"
"ds_read_b64_tr_b16 a[40:41], v248 offset:4096\n"
"ds_read_b64_tr_b16 a[42:43], v248 offset:6144\n"
"v_mfma_f32_16x16x32_bf16 v[212:215], a[44:47], a[20:23], v[212:215]\n"
"buffer_load_dwordx4 v188, s[40:43], s74 offen offset:2048 lds\n"
"v_mfma_f32_16x16x32_bf16 v[220:223], a[44:47], a[84:87], v[220:223]\n"
"v_mfma_f32_16x16x32_bf16 v[228:231], a[44:47], a[244:247], v[228:231]\n"
"ds_read_b64_tr_b16 a[44:45], v249 offset:4096\n"
"ds_read_b64_tr_b16 a[46:47], v249 offset:6144\n"
"v_mfma_f32_16x16x32_bf16 v[208:211], a[48:51], a[24:27], v[208:211]\n"
"v_mfma_f32_16x16x32_bf16 v[216:219], a[48:51], a[88:91], v[216:219]\n"
"buffer_load_dwordx4 v189, s[40:43], s74 offen offset:3072 lds\n"
"s_add_u32 s73, s94, 0x280\n"
"v_mfma_f32_16x16x32_bf16 v[224:227], a[48:51], a[248:251], v[224:227]\n"
"ds_read_b64_tr_b16 a[48:49], v248 offset:8192\n"
"ds_read_b64_tr_b16 a[50:51], v248 offset:10240\n"
"v_mfma_f32_16x16x32_bf16 v[212:215], a[52:55], a[24:27], v[212:215]\n"
"s_and_b32 s73, s73, 0x380\n"
"s_add_u32 s73, s73, s97\n"
"s_add_u32 m0, s68, 0x18000\n"
"v_mfma_f32_16x16x32_bf16 v[220:223], a[52:55], a[88:91], v[220:223]\n"
"s_add_u32 s74, s73, 0x0\n"
"v_mfma_f32_16x16x32_bf16 v[228:231], a[52:55], a[248:251], v[228:231]\n"
"ds_read_b64_tr_b16 a[52:53], v249 offset:8192\n"
"ds_read_b64_tr_b16 a[54:55], v249 offset:10240\n"
"v_mfma_f32_16x16x32_bf16 v[208:211], a[56:59], a[28:31], v[208:211]\n"
"v_mfma_f32_16x16x32_bf16 v[216:219], a[56:59], a[92:95], v[216:219]\n"
"buffer_load_dwordx4 v188, s[40:43], s74 offen offset:0 lds\n"
"v_mfma_f32_16x16x32_bf16 v[224:227], a[56:59], a[252:255], v[224:227]\n"
"ds_read_b64_tr_b16 a[56:57], v248 offset:12288\n"
"ds_read_b64_tr_b16 a[58:59], v248 offset:14336\n"
"v_mfma_f32_16x16x32_bf16 v[212:215], a[60:63], a[28:31], v[212:215]\n"
"v_mfma_f32_16x16x32_bf16 v[220:223], a[60:63], a[92:95], v[220:223]\n"
"s_waitcnt vmcnt(17)\n"
"s_mov_b64 exec, 0x1\n"
"buffer_store_dword v254, v253, s[52:55], 0 offen offset:8 sc1\n"
"s_mov_b64 exec, -1\n"
"v_mfma_f32_16x16x32_bf16 v[228:231], a[60:63], a[252:255], v[228:231]\n"
"ds_read_b64_tr_b16 a[60:61], v249 offset:12288\n"
"ds_read_b64_tr_b16 a[62:63], v249 offset:14336\n"
"s_waitcnt lgkmcnt(15)\n"
"v_mfma_f32_16x16x32_bf16 v[0:3], a[192:195], a[0:3], v[0:3]\n"
"buffer_load_dwordx4 v189, s[40:43], s74 offen offset:1024 lds\n"
"s_add_u32 s74, s73, 0x3800\n"
"v_mfma_f32_16x16x32_bf16 v[8:11], a[192:195], a[64:67], v[8:11]\n"
"v_add_u32 v246, 0xffff0000, v246\n"
"v_cvt_pk_bf16_f32 v208, v208, v209\n"
"v_mfma_f32_16x16x32_bf16 v[16:19], a[192:195], a[224:227], v[16:19]\n"
"v_cvt_pk_bf16_f32 v209, v210, v211\n"
"v_cvt_pk_bf16_f32 v216, v216, v217\n"
"v_mfma_f32_16x16x32_bf16 v[4:7], a[196:199], a[0:3], v[4:7]\n"
"v_cvt_pk_bf16_f32 v217, v218, v219\n"
"v_cvt_pk_bf16_f32 v224, v224, v225\n"
"v_mfma_f32_16x16x32_bf16 v[12:15], a[196:199], a[64:67], v[12:15]\n"
"v_add_u32 v247, 0xffff0000, v247\n"
"v_cvt_pk_bf16_f32 v225, v226, v227\n"
"v_mfma_f32_16x16x32_bf16 v[20:23], a[196:199], a[224:227], v[20:23]\n"
"buffer_load_dwordx4 v188, s[40:43], s74 offen offset:2048 lds\n"
"v_cvt_pk_bf16_f32 v210, v212, v213\n"
"v_mfma_f32_16x16x32_bf16 v[0:3], a[200:203], a[4:7], v[0:3]\n"
"v_cvt_pk_bf16_f32 v211, v214, v215\n"
"v_cvt_pk_bf16_f32 v218, v220, v221\n"
"v_mfma_f32_16x16x32_bf16 v[8:11], a[200:203], a[68:71], v[8:11]\n"
"v_cvt_pk_bf16_f32 v219, v222, v223\n"
"v_cvt_pk_bf16_f32 v226, v228, v229\n"
"v_cvt_pk_bf16_f32 v227, v230, v231\n"
"s_add_u32 s56, s94, 0x100\n"
"s_and_b32 s56, s56, 0x3c0\n"
"s_add_u32 s56, s56, 0x800\n"
"s_mul_i32 s56, s56, 192\n"
"buffer_store_dwordx4 v[208:211], v252, s[48:51], s56 offen offset:0 sc1\n"
"s_waitcnt vmcnt(16)\n"
"s_waitcnt lgkmcnt(0)\n"
"s_barrier\n"
"v_mfma_f32_16x16x32_bf16 v[16:19], a[200:203], a[228:231], v[16:19]\n"
"ds_read_b64_tr_b16 a[200:201], v246 offset:4096\n"
"ds_read_b64_tr_b16 a[202:203], v246 offset:6144\n"
"v_mfma_f32_16x16x32_bf16 v[4:7], a[204:207], a[4:7], v[4:7]\n"
"ds_read_b64_tr_b16 a[192:193], v246 offset:0\n"
"ds_read_b64_tr_b16 a[194:195], v246 offset:2048\n"
"v_mfma_f32_16x16x32_bf16 v[12:15], a[204:207], a[68:71], v[12:15]\n"
"buffer_store_dwordx4 v[216:219], v252, s[48:51], s56 offen offset:1024 sc1\n"
"v_mfma_f32_16x16x32_bf16 v[20:23], a[204:207], a[228:231], v[20:23]\n"
"ds_read_b64_tr_b16 a[204:205], v247 offset:4096\n"
"ds_read_b64_tr_b16 a[206:207], v247 offset:6144\n"
"v_mfma_f32_16x16x32_bf16 v[0:3], a[208:211], a[8:11], v[0:3]\n"
"ds_read_b64_tr_b16 a[196:197], v247 offset:0\n"
"ds_read_b64_tr_b16 a[198:199], v247 offset:2048\n"
"v_mfma_f32_16x16x32_bf16 v[8:11], a[208:211], a[72:75], v[8:11]\n"
"buffer_load_dwordx4 v189, s[40:43], s74 offen offset:3072 lds\n"
"s_add_u32 m0, s68, 0x19000\n"
"v_mfma_f32_16x16x32_bf16 v[16:19], a[208:211], a[232:235], v[16:19]\n"
"ds_read_b64_tr_b16 a[208:209], v246 offset:8192\n"
"ds_read_b64_tr_b16 a[210:211], v246 offset:10240\n"
"v_mfma_f32_16x16x32_bf16 v[4:7], a[212:215], a[8:11], v[4:7]\n"
"s_add_u32 s74, s73, 0x8000\n"
"v_mfma_f32_16x16x32_bf16 v[12:15], a[212:215], a[72:75], v[12:15]\n"
"buffer_load_dwordx4 v188, s[40:43], s74 offen offset:0 lds\n"
"v_mfma_f32_16x16x32_bf16 v[20:23], a[212:215], a[232:235], v[20:23]\n"
"ds_read_b64_tr_b16 a[212:213], v247 offset:8192\n"
"ds_read_b64_tr_b16 a[214:215], v247 offset:10240\n"
"v_mfma_f32_16x16x32_bf16 v[0:3], a[216:219], a[12:15], v[0:3]\n"
"v_mfma_f32_16x16x32_bf16 v[8:11], a[216:219], a[76:79], v[8:11]\n"
"buffer_store_dwordx4 v[224:227], v252, s[48:51], s56 offen offset:2048 sc1\n"
"v_mfma_f32_16x16x32_bf16 v[16:19], a[216:219], a[236:239], v[16:19]\n"
"ds_read_b64_tr_b16 a[216:217], v246 offset:12288\n"
"ds_read_b64_tr_b16 a[218:219], v246 offset:14336\n"
"v_mfma_f32_16x16x32_bf16 v[4:7], a[220:223], a[12:15], v[4:7]\n"
"buffer_load_dwordx4 v189, s[40:43], s74 offen offset:1024 lds\n"
"s_add_u32 s74, s73, 0xb800\n"
"v_mfma_f32_16x16x32_bf16 v[12:15], a[220:223], a[76:79], v[12:15]\n"
"v_mfma_f32_16x16x32_bf16 v[20:23], a[220:223], a[236:239], v[20:23]\n"
"ds_read_b64_tr_b16 a[220:221], v247 offset:12288\n"
"ds_read_b64_tr_b16 a[222:223], v247 offset:14336\n"
"v_mfma_f32_16x16x32_bf16 v[0:3], a[32:35], a[16:19], v[0:3]\n"
"v_mfma_f32_16x16x32_bf16 v[8:11], a[32:35], a[80:83], v[8:11]\n"
"v_add_u32 v248, 0xffff0000, v248\n"
"v_mfma_f32_16x16x32_bf16 v[16:19], a[32:35], a[240:243], v[16:19]\n"
"ds_read_b64_tr_b16 a[32:33], v248 offset:0\n"
"ds_read_b64_tr_b16 a[34:35], v248 offset:2048\n"
"v_mfma_f32_16x16x32_bf16 v[4:7], a[36:39], a[16:19], v[4:7]\n"
"buffer_load_dwordx4 v188, s[40:43], s74 offen offset:2048 lds\n"
"v_mfma_f32_16x16x32_bf16 v[12:15], a[36:39], a[80:83], v[12:15]\n"
"v_add_u32 v249, 0xffff0000, v249\n"
"v_mfma_f32_16x16x32_bf16 v[20:23], a[36:39], a[240:243], v[20:23]\n"
"ds_read_b64_tr_b16 a[36:37], v249 offset:0\n"
"ds_read_b64_tr_b16 a[38:39], v249 offset:2048\n"
"v_mfma_f32_16x16x32_bf16 v[0:3], a[40:43], a[20:23], v[0:3]\n"
"v_mfma_f32_16x16x32_bf16 v[8:11], a[40:43], a[84:87], v[8:11]\n"
"v_mfma_f32_16x16x32_bf16 v[16:19], a[40:43], a[244:247], v[16:19]\n"
"ds_read_b64_tr_b16 a[40:41], v248 offset:4096\n"
"ds_read_b64_tr_b16 a[42:43], v248 offset:6144\n"
"v_mfma_f32_16x16x32_bf16 v[4:7], a[44:47], a[20:23], v[4:7]\n"
"buffer_load_dwordx4 v189, s[40:43], s74 offen offset:3072 lds\n"
"s_add_u32 s73, s94, 0x300\n"
"v_mfma_f32_16x16x32_bf16 v[12:15], a[44:47], a[84:87], v[12:15]\n"
"s_and_b32 s73, s73, 0x380\n"
"s_add_u32 s73, s73, s96\n"
"s_add_u32 m0, s68, 0x20000\n"
"v_mfma_f32_16x16x32_bf16 v[20:23], a[44:47], a[244:247], v[20:23]\n"
"ds_read_b64_tr_b16 a[44:45], v249 offset:4096\n"
"ds_read_b64_tr_b16 a[46:47], v249 offset:6144\n"
"v_mfma_f32_16x16x32_bf16 v[0:3], a[48:51], a[24:27], v[0:3]\n"
"s_add_u32 s74, s73, 0x0\n"
"v_mfma_f32_16x16x32_bf16 v[8:11], a[48:51], a[88:91], v[8:11]\n"
"buffer_load_dwordx4 v188, s[40:43], s74 offen offset:0 lds\n"
"v_mfma_f32_16x16x32_bf16 v[16:19], a[48:51], a[248:251], v[16:19]\n"
"ds_read_b64_tr_b16 a[48:49], v248 offset:8192\n"
"ds_read_b64_tr_b16 a[50:51], v248 offset:10240\n"
"v_mfma_f32_16x16x32_bf16 v[4:7], a[52:55], a[24:27], v[4:7]\n"
"v_mfma_f32_16x16x32_bf16 v[12:15], a[52:55], a[88:91], v[12:15]\n"
"v_mfma_f32_16x16x32_bf16 v[20:23], a[52:55], a[248:251], v[20:23]\n"
"ds_read_b64_tr_b16 a[52:53], v249 offset:8192\n"
"ds_read_b64_tr_b16 a[54:55], v249 offset:10240\n"
"v_mfma_f32_16x16x32_bf16 v[0:3], a[56:59], a[28:31], v[0:3]\n"
"buffer_load_dword v243, v185, s[52:55], s86 offen sc1\n"
"v_mfma_f32_16x16x32_bf16 v[8:11], a[56:59], a[92:95], v[8:11]\n"
"buffer_load_dwordx4 v189, s[40:43], s74 offen offset:1024 lds\n"
"s_add_u32 s74, s73, 0x3800\n"
"v_mfma_f32_16x16x32_bf16 v[16:19], a[56:59], a[252:255], v[16:19]\n"
"ds_read_b64_tr_b16 a[56:57], v248 offset:12288\n"
"ds_read_b64_tr_b16 a[58:59], v248 offset:14336\n"
"v_mfma_f32_16x16x32_bf16 v[4:7], a[60:63], a[28:31], v[4:7]\n"
"s_waitcnt vmcnt(19)\n"
"s_mov_b64 exec, 0x2\n"
"buffer_store_dword v254, v253, s[52:55], 0 offen offset:8 sc1\n"
"s_mov_b64 exec, -1\n"
"v_mfma_f32_16x16x32_bf16 v[12:15], a[60:63], a[92:95], v[12:15]\n"
"v_mfma_f32_16x16x32_bf16 v[20:23], a[60:63], a[252:255], v[20:23]\n"
"ds_read_b64_tr_b16 a[60:61], v249 offset:12288\n"
"ds_read_b64_tr_b16 a[62:63], v249 offset:14336\n"
"s_waitcnt lgkmcnt(15)\n"
"v_mfma_f32_16x16x32_bf16 v[24:27], a[192:195], a[0:3], v[24:27]\n"
"buffer_load_dwordx4 v188, s[40:43], s74 offen offset:2048 lds\n"
"v_cvt_pk_bf16_f32 v0, v0, v1\n"
"v_mfma_f32_16x16x32_bf16 v[32:35], a[192:195], a[64:67], v[32:35]\n"
"v_cvt_pk_bf16_f32 v1, v2, v3\n"
"v_cvt_pk_bf16_f32 v8, v8, v9\n"
"v_mfma_f32_16x16x32_bf16 v[40:43], a[192:195], a[224:227], v[40:43]\n"
"v_cvt_pk_bf16_f32 v9, v10, v11\n"
"v_cvt_pk_bf16_f32 v16, v16, v17\n"
"v_mfma_f32_16x16x32_bf16 v[28:31], a[196:199], a[0:3], v[28:31]\n"
"v_cvt_pk_bf16_f32 v17, v18, v19\n"
"v_cvt_pk_bf16_f32 v2, v4, v5\n"
"v_mfma_f32_16x16x32_bf16 v[36:39], a[196:199], a[64:67], v[36:39]\n"
"v_cvt_pk_bf16_f32 v3, v6, v7\n"
"v_cvt_pk_bf16_f32 v10, v12, v13\n"
"v_mfma_f32_16x16x32_bf16 v[44:47], a[196:199], a[224:227], v[44:47]\n"
"buffer_load_dwordx4 v189, s[40:43], s74 offen offset:3072 lds\n"
"s_add_u32 m0, s68, 0x21000\n"
"v_mfma_f32_16x16x32_bf16 v[24:27], a[200:203], a[4:7], v[24:27]\n"
"s_add_u32 s74, s73, 0x8000\n"
"v_cvt_pk_bf16_f32 v11, v14, v15\n"
"v_cvt_pk_bf16_f32 v18, v20, v21\n"
"v_mfma_f32_16x16x32_bf16 v[32:35], a[200:203], a[68:71], v[32:35]\n"
"v_cvt_pk_bf16_f32 v19, v22, v23\n"
"s_add_u32 s56, s94, 0x180\n"
"s_and_b32 s56, s56, 0x3c0\n"
"s_add_u32 s56, s56, 0xc00\n"
"s_mul_i32 s56, s56, 192\n"
"buffer_store_dwordx4 v[0:3], v252, s[48:51], s56 offen offset:0 sc1\n"
"s_waitcnt vmcnt(19)\n"
"s_waitcnt lgkmcnt(0)\n"
"s_barrier\n"
"v_mfma_f32_16x16x32_bf16 v[40:43], a[200:203], a[228:231], v[40:43]\n"
"ds_read_b64_tr_b16 a[200:201], v246 offset:36864\n"
"ds_read_b64_tr_b16 a[202:203], v246 offset:38912\n"
"v_mfma_f32_16x16x32_bf16 v[28:31], a[204:207], a[4:7], v[28:31]\n"
"ds_read_b64_tr_b16 a[192:193], v246 offset:32768\n"
"ds_read_b64_tr_b16 a[194:195], v246 offset:34816\n"
"v_mfma_f32_16x16x32_bf16 v[36:39], a[204:207], a[68:71], v[36:39]\n"
"buffer_store_dwordx4 v[8:11], v252, s[48:51], s56 offen offset:1024 sc1\n"
"v_mfma_f32_16x16x32_bf16 v[44:47], a[204:207], a[228:231], v[44:47]\n"
"ds_read_b64_tr_b16 a[204:205], v247 offset:36864\n"
"ds_read_b64_tr_b16 a[206:207], v247 offset:38912\n"
"v_mfma_f32_16x16x32_bf16 v[24:27], a[208:211], a[8:11], v[24:27]\n"
"ds_read_b64_tr_b16 a[196:197], v247 offset:32768\n"
"ds_read_b64_tr_b16 a[198:199], v247 offset:34816\n"
"v_mfma_f32_16x16x32_bf16 v[32:35], a[208:211], a[72:75], v[32:35]\n"
"buffer_load_dwordx4 v188, s[40:43], s74 offen offset:0 lds\n"
"v_mfma_f32_16x16x32_bf16 v[40:43], a[208:211], a[232:235], v[40:43]\n"
"ds_read_b64_tr_b16 a[208:209], v246 offset:40960\n"
"ds_read_b64_tr_b16 a[210:211], v246 offset:43008\n"
"v_mfma_f32_16x16x32_bf16 v[28:31], a[212:215], a[8:11], v[28:31]\n"
"v_mfma_f32_16x16x32_bf16 v[36:39], a[212:215], a[72:75], v[36:39]\n"
"buffer_load_dwordx4 v189, s[40:43], s74 offen offset:1024 lds\n"
"s_add_u32 s74, s73, 0xb800\n"
"v_mfma_f32_16x16x32_bf16 v[44:47], a[212:215], a[232:235], v[44:47]\n"
"ds_read_b64_tr_b16 a[212:213], v247 offset:40960\n"
"ds_read_b64_tr_b16 a[214:215], v247 offset:43008\n"
"v_mfma_f32_16x16x32_bf16 v[24:27], a[216:219], a[12:15], v[24:27]\n"
"v_mfma_f32_16x16x32_bf16 v[32:35], a[216:219], a[76:79], v[32:35]\n"
"buffer_store_dwordx4 v[16:19], v252, s[48:51], s56 offen offset:2048 sc1\n"
"v_mfma_f32_16x16x32_bf16 v[40:43], a[216:219], a[236:239], v[40:43]\n"
"ds_read_b64_tr_b16 a[216:217], v246 offset:45056\n"
"ds_read_b64_tr_b16 a[218:219], v246 offset:47104\n"
"v_mfma_f32_16x16x32_bf16 v[28:31], a[220:223], a[12:15], v[28:31]\n"
"buffer_load_dwordx4 v188, s[40:43], s74 offen offset:2048 lds\n"
"v_mfma_f32_16x16x32_bf16 v[36:39], a[220:223], a[76:79], v[36:39]\n"
"v_mfma_f32_16x16x32_bf16 v[44:47], a[220:223], a[236:239], v[44:47]\n"
"ds_read_b64_tr_b16 a[220:221], v247 offset:45056\n"
"ds_read_b64_tr_b16 a[222:223], v247 offset:47104\n"
"v_mfma_f32_16x16x32_bf16 v[24:27], a[32:35], a[16:19], v[24:27]\n"
"v_mfma_f32_16x16x32_bf16 v[32:35], a[32:35], a[80:83], v[32:35]\n"
"v_mfma_f32_16x16x32_bf16 v[40:43], a[32:35], a[240:243], v[40:43]\n"
"ds_read_b64_tr_b16 a[32:33], v248 offset:32768\n"
"ds_read_b64_tr_b16 a[34:35], v248 offset:34816\n"
"v_mfma_f32_16x16x32_bf16 v[28:31], a[36:39], a[16:19], v[28:31]\n"
"buffer_load_dwordx4 v189, s[40:43], s74 offen offset:3072 lds\n"
"s_add_u32 s73, s94, 0x300\n"
"v_mfma_f32_16x16x32_bf16 v[36:39], a[36:39], a[80:83], v[36:39]\n"
"s_and_b32 s73, s73, 0x380\n"
"s_add_u32 s73, s73, s97\n"
"s_add_u32 m0, s68, 0x10000\n"
"v_mfma_f32_16x16x32_bf16 v[44:47], a[36:39], a[240:243], v[44:47]\n"
"ds_read_b64_tr_b16 a[36:37], v249 offset:32768\n"
"ds_read_b64_tr_b16 a[38:39], v249 offset:34816\n"
"v_mfma_f32_16x16x32_bf16 v[24:27], a[40:43], a[20:23], v[24:27]\n"
"s_add_u32 s74, s73, 0x0\n"
"v_mfma_f32_16x16x32_bf16 v[32:35], a[40:43], a[84:87], v[32:35]\n"
"v_mfma_f32_16x16x32_bf16 v[40:43], a[40:43], a[244:247], v[40:43]\n"
"ds_read_b64_tr_b16 a[40:41], v248 offset:36864\n"
"ds_read_b64_tr_b16 a[42:43], v248 offset:38912\n"
"v_mfma_f32_16x16x32_bf16 v[28:31], a[44:47], a[20:23], v[28:31]\n"
"buffer_load_dwordx4 v188, s[40:43], s74 offen offset:0 lds\n"
"v_mfma_f32_16x16x32_bf16 v[36:39], a[44:47], a[84:87], v[36:39]\n"
"v_mfma_f32_16x16x32_bf16 v[44:47], a[44:47], a[244:247], v[44:47]\n"
"ds_read_b64_tr_b16 a[44:45], v249 offset:36864\n"
"ds_read_b64_tr_b16 a[46:47], v249 offset:38912\n"
"v_mfma_f32_16x16x32_bf16 v[24:27], a[48:51], a[24:27], v[24:27]\n"
"v_mfma_f32_16x16x32_bf16 v[32:35], a[48:51], a[88:91], v[32:35]\n"
"buffer_load_dwordx4 v189, s[40:43], s74 offen offset:1024 lds\n"
"s_add_u32 s74, s73, 0x3800\n"
"v_mfma_f32_16x16x32_bf16 v[40:43], a[48:51], a[248:251], v[40:43]\n"
"ds_read_b64_tr_b16 a[48:49], v248 offset:40960\n"
"ds_read_b64_tr_b16 a[50:51], v248 offset:43008\n"
"v_mfma_f32_16x16x32_bf16 v[28:31], a[52:55], a[24:27], v[28:31]\n"
"v_mfma_f32_16x16x32_bf16 v[36:39], a[52:55], a[88:91], v[36:39]\n"
"v_mfma_f32_16x16x32_bf16 v[44:47], a[52:55], a[248:251], v[44:47]\n"
"ds_read_b64_tr_b16 a[52:53], v249 offset:40960\n"
"ds_read_b64_tr_b16 a[54:55], v249 offset:43008\n"
"v_mfma_f32_16x16x32_bf16 v[24:27], a[56:59], a[28:31], v[24:27]\n"
"buffer_load_dword v240, v185, s[52:55], s86 offen sc1\n"
"v_mfma_f32_16x16x32_bf16 v[32:35], a[56:59], a[92:95], v[32:35]\n"
"buffer_load_dwordx4 v188, s[40:43], s74 offen offset:2048 lds\n"
"v_mfma_f32_16x16x32_bf16 v[40:43], a[56:59], a[252:255], v[40:43]\n"
"ds_read_b64_tr_b16 a[56:57], v248 offset:45056\n"
"ds_read_b64_tr_b16 a[58:59], v248 offset:47104\n"
"v_mfma_f32_16x16x32_bf16 v[28:31], a[60:63], a[28:31], v[28:31]\n"
"v_mfma_f32_16x16x32_bf16 v[36:39], a[60:63], a[92:95], v[36:39]\n"
"s_waitcnt vmcnt(20)\n"
"s_mov_b64 exec, 0x4\n"
"buffer_store_dword v254, v253, s[52:55], 0 offen offset:8 sc1\n"
"s_mov_b64 exec, -1\n"
"v_mfma_f32_16x16x32_bf16 v[44:47], a[60:63], a[252:255], v[44:47]\n"
"ds_read_b64_tr_b16 a[60:61], v249 offset:45056\n"
"ds_read_b64_tr_b16 a[62:63], v249 offset:47104\n"
"s_waitcnt lgkmcnt(15)\n"
"v_mfma_f32_16x16x32_bf16 v[96:99], a[192:195], v[48:51], 0\n"
"buffer_load_dwordx4 v189, s[40:43], s74 offen offset:3072 lds\n"
"s_add_u32 m0, s68, 0x11000\n"
"v_mfma_f32_16x16x32_bf16 v[104:107], a[192:195], v[64:67], 0\n"
"v_add_u32 v246, 0x10000, v246\n"
"s_add_u32 s74, s73, 0x8000\n"
"v_mfma_f32_16x16x32_bf16 v[112:115], a[192:195], v[80:83], 0\n"
"v_cvt_pk_bf16_f32 v24, v24, v25\n"
"v_cvt_pk_bf16_f32 v25, v26, v27\n"
"v_mfma_f32_16x16x32_bf16 v[100:103], a[196:199], v[48:51], 0\n"
"v_cvt_pk_bf16_f32 v32, v32, v33\n"
"v_cvt_pk_bf16_f32 v33, v34, v35\n"
"v_mfma_f32_16x16x32_bf16 v[108:111], a[196:199], v[64:67], 0\n"
"v_add_u32 v247, 0x10000, v247\n"
"v_cvt_pk_bf16_f32 v40, v40, v41\n"
"v_mfma_f32_16x16x32_bf16 v[116:119], a[196:199], v[80:83], 0\n"
"buffer_load_dwordx4 v188, s[40:43], s74 offen offset:0 lds\n"
"v_cvt_pk_bf16_f32 v41, v42, v43\n"
"v_mfma_f32_16x16x32_bf16 v[96:99], a[200:203], v[52:55], v[96:99]\n"
"v_cvt_pk_bf16_f32 v26, v28, v29\n"
"v_cvt_pk_bf16_f32 v27, v30, v31\n"
"v_mfma_f32_16x16x32_bf16 v[104:107], a[200:203], v[68:71], v[104:107]\n"
"v_cvt_pk_bf16_f32 v34, v36, v37\n"
"v_cvt_pk_bf16_f32 v35, v38, v39\n"
"v_cvt_pk_bf16_f32 v42, v44, v45\n"
"v_cvt_pk_bf16_f32 v43, v46, v47\n"
"s_add_u32 s56, s94, 0x200\n"
"s_and_b32 s56, s56, 0x3c0\n"
"s_add_u32 s56, s56, 0x1000\n"
"s_mul_i32 s56, s56, 192\n"
"buffer_store_dwordx4 v[24:27], v252, s[48:51], s56 offen offset:0 sc1\n"
"s_waitcnt vmcnt(21)\n"
"s_waitcnt lgkmcnt(0)\n"
"s_barrier\n"
"v_mfma_f32_16x16x32_bf16 v[112:115], a[200:203], v[84:87], v[112:115]\n"
"ds_read_b64_tr_b16 a[200:201], v246 offset:36864\n"
"ds_read_b64_tr_b16 a[202:203], v246 offset:38912\n"
"v_mfma_f32_16x16x32_bf16 v[100:103], a[204:207], v[52:55], v[100:103]\n"
"ds_read_b64_tr_b16 a[192:193], v246 offset:32768\n"
"ds_read_b64_tr_b16 a[194:195], v246 offset:34816\n"
"v_mfma_f32_16x16x32_bf16 v[108:111], a[204:207], v[68:71], v[108:111]\n"
"buffer_store_dwordx4 v[32:35], v252, s[48:51], s56 offen offset:1024 sc1\n"
"v_mfma_f32_16x16x32_bf16 v[116:119], a[204:207], v[84:87], v[116:119]\n"
"ds_read_b64_tr_b16 a[204:205], v247 offset:36864\n"
"ds_read_b64_tr_b16 a[206:207], v247 offset:38912\n"
"v_mfma_f32_16x16x32_bf16 v[96:99], a[208:211], v[56:59], v[96:99]\n"
"ds_read_b64_tr_b16 a[196:197], v247 offset:32768\n"
"ds_read_b64_tr_b16 a[198:199], v247 offset:34816\n"
"v_mfma_f32_16x16x32_bf16 v[104:107], a[208:211], v[72:75], v[104:107]\n"
"s_waitcnt vmcnt(20)\n"
"v_cmp_ne_u32 s[22:23], s82, v243\n"
"s_nop 0\n"
"s_and_b32 s22, s22, 0x4\n"
"s_cbranch_scc1 .Lk1_pw3\n"
".Lk1_pd3:\n"
"v_mfma_f32_16x16x32_bf16 v[112:115], a[208:211], v[88:91], v[112:115]\n"
"ds_read_b64_tr_b16 a[208:209], v246 offset:40960\n"
"ds_read_b64_tr_b16 a[210:211], v246 offset:43008\n"
"v_mfma_f32_16x16x32_bf16 v[100:103], a[212:215], v[56:59], v[100:103]\n"
"s_add_u32 s71, s57, 0\n"
"buffer_load_dwordx4 v189, s[40:43], s74 offen offset:1024 lds\n"
"s_add_u32 s74, s73, 0xb800\n"
"v_mfma_f32_16x16x32_bf16 v[108:111], a[212:215], v[72:75], v[108:111]\n"
"v_mfma_f32_16x16x32_bf16 v[116:119], a[212:215], v[88:91], v[116:119]\n"
"ds_read_b64_tr_b16 a[212:213], v247 offset:40960\n"
"ds_read_b64_tr_b16 a[214:215], v247 offset:43008\n"
"v_mfma_f32_16x16x32_bf16 v[96:99], a[216:219], v[60:63], v[96:99]\n"
"buffer_store_dwordx4 v[40:43], v252, s[48:51], s56 offen offset:2048 sc1\n"
"v_mfma_f32_16x16x32_bf16 v[104:107], a[216:219], v[76:79], v[104:107]\n"
"v_mfma_f32_16x16x32_bf16 v[112:115], a[216:219], v[92:95], v[112:115]\n"
"ds_read_b64_tr_b16 a[216:217], v246 offset:45056\n"
"ds_read_b64_tr_b16 a[218:219], v246 offset:47104\n"
"v_mfma_f32_16x16x32_bf16 v[100:103], a[220:223], v[60:63], v[100:103]\n"
"buffer_load_dwordx4 v188, s[40:43], s74 offen offset:2048 lds\n"
"v_mfma_f32_16x16x32_bf16 v[108:111], a[220:223], v[76:79], v[108:111]\n"
"v_mfma_f32_16x16x32_bf16 v[116:119], a[220:223], v[92:95], v[116:119]\n"
"ds_read_b64_tr_b16 a[220:221], v247 offset:45056\n"
"ds_read_b64_tr_b16 a[222:223], v247 offset:47104\n"
"v_mfma_f32_16x16x32_bf16 v[96:99], a[32:35], a[96:99], v[96:99]\n"
"buffer_load_dwordx4 v189, s[40:43], s74 offen offset:3072 lds\n"
"s_add_u32 s73, s94, 0x380\n"
"v_mfma_f32_16x16x32_bf16 v[104:107], a[32:35], a[112:115], v[104:107]\n"
"v_add_u32 v248, 0x10000, v248\n"
"s_and_b32 s73, s73, 0x380\n"
"v_mfma_f32_16x16x32_bf16 v[112:115], a[32:35], a[128:131], v[112:115]\n"
"ds_read_b64_tr_b16 a[32:33], v248 offset:32768\n"
"ds_read_b64_tr_b16 a[34:35], v248 offset:34816\n"
"v_mfma_f32_16x16x32_bf16 v[100:103], a[36:39], a[96:99], v[100:103]\n"
"s_add_u32 s73, s73, s96\n"
"s_add_u32 m0, s68, 0x0\n"
"s_add_u32 s74, s73, 0x0\n"
"v_mfma_f32_16x16x32_bf16 v[108:111], a[36:39], a[112:115], v[108:111]\n"
"v_add_u32 v249, 0x10000, v249\n"
"v_mfma_f32_16x16x32_bf16 v[116:119], a[36:39], a[128:131], v[116:119]\n"
"ds_read_b64_tr_b16 a[36:37], v249 offset:32768\n"
"ds_read_b64_tr_b16 a[38:39], v249 offset:34816\n"
"v_mfma_f32_16x16x32_bf16 v[96:99], a[40:43], a[100:103], v[96:99]\n"
"buffer_load_dwordx4 a[144:147], v252, s[48:51], s71 offen offset:0 sc1 nt\n"
"v_mfma_f32_16x16x32_bf16 v[104:107], a[40:43], a[116:119], v[104:107]\n"
"v_mfma_f32_16x16x32_bf16 v[112:115], a[40:43], a[132:135], v[112:115]\n"
"ds_read_b64_tr_b16 a[40:41], v248 offset:36864\n"
"ds_read_b64_tr_b16 a[42:43], v248 offset:38912\n"
"v_mfma_f32_16x16x32_bf16 v[100:103], a[44:47], a[100:103], v[100:103]\n"
"buffer_load_dwordx4 v188, s[40:43], s74 offen offset:0 lds\n"
"v_mfma_f32_16x16x32_bf16 v[108:111], a[44:47], a[116:119], v[108:111]\n"
"v_mfma_f32_16x16x32_bf16 v[116:119], a[44:47], a[132:135], v[116:119]\n"
"ds_read_b64_tr_b16 a[44:45], v249 offset:36864\n"
"ds_read_b64_tr_b16 a[46:47], v249 offset:38912\n"
"v_mfma_f32_16x16x32_bf16 v[96:99], a[48:51], a[104:107], v[96:99]\n"
"buffer_load_dwordx4 a[148:151], v252, s[48:51], s71 offen offset:1024 sc1 nt\n"
"v_mfma_f32_16x16x32_bf16 v[104:107], a[48:51], a[120:123], v[104:107]\n"
"v_mfma_f32_16x16x32_bf16 v[112:115], a[48:51], a[136:139], v[112:115]\n"
"ds_read_b64_tr_b16 a[48:49], v248 offset:40960\n"
"ds_read_b64_tr_b16 a[50:51], v248 offset:43008\n"
"v_mfma_f32_16x16x32_bf16 v[100:103], a[52:55], a[104:107], v[100:103]\n"
"buffer_load_dwordx4 v189, s[40:43], s74 offen offset:1024 lds\n"
"s_add_u32 s74, s73, 0x3800\n"
"v_mfma_f32_16x16x32_bf16 v[108:111], a[52:55], a[120:123], v[108:111]\n"
"v_mfma_f32_16x16x32_bf16 v[116:119], a[52:55], a[136:139], v[116:119]\n"
"ds_read_b64_tr_b16 a[52:53], v249 offset:40960\n"
"ds_read_b64_tr_b16 a[54:55], v249 offset:43008\n"
"v_mfma_f32_16x16x32_bf16 v[96:99], a[56:59], a[108:111], v[96:99]\n"
"buffer_load_dword v241, v185, s[52:55], s86 offen sc1\n"
"v_mfma_f32_16x16x32_bf16 v[104:107], a[56:59], a[124:127], v[104:107]\n"
"buffer_load_dwordx4 a[152:155], v252, s[48:51], s71 offen offset:2048 sc1 nt\n"
"v_mfma_f32_16x16x32_bf16 v[112:115], a[56:59], a[140:143], v[112:115]\n"
"ds_read_b64_tr_b16 a[56:57], v248 offset:45056\n"
"ds_read_b64_tr_b16 a[58:59], v248 offset:47104\n"
"v_mfma_f32_16x16x32_bf16 v[100:103], a[60:63], a[108:111], v[100:103]\n"
"s_waitcnt vmcnt(21)\n"
"s_mov_b64 exec, 0x8\n"
"buffer_store_dword v254, v253, s[52:55], 0 offen offset:8 sc1\n"
"s_mov_b64 exec, -1\n"
"v_mfma_f32_16x16x32_bf16 v[108:111], a[60:63], a[124:127], v[108:111]\n"
"buffer_load_dwordx4 v188, s[40:43], s74 offen offset:2048 lds\n"
"v_mfma_f32_16x16x32_bf16 v[116:119], a[60:63], a[140:143], v[116:119]\n"
"ds_read_b64_tr_b16 a[60:61], v249 offset:45056\n"
"ds_read_b64_tr_b16 a[62:63], v249 offset:47104\n"
"s_waitcnt lgkmcnt(15)\n"
"v_mfma_f32_16x16x32_bf16 v[96:99], a[192:195], a[0:3], v[96:99]\n"
"v_mfma_f32_16x16x32_bf16 v[104:107], a[192:195], a[64:67], v[104:107]\n"
"v_add_u32 v246, 0x10000, v246\n"
"v_mfma_f32_16x16x32_bf16 v[112:115], a[192:195], a[224:227], v[112:115]\n"
"buffer_load_dwordx4 v189, s[40:43], s74 offen offset:3072 lds\n"
"s_add_u32 m0, s68, 0x1000\n"
"v_mfma_f32_16x16x32_bf16 v[100:103], a[196:199], a[0:3], v[100:103]\n"
"s_add_u32 s74, s73, 0x8000\n"
"v_mfma_f32_16x16x32_bf16 v[108:111], a[196:199], a[64:67], v[108:111]\n"
"v_add_u32 v247, 0x10000, v247\n"
"v_mfma_f32_16x16x32_bf16 v[116:119], a[196:199], a[224:227], v[116:119]\n"
"v_mfma_f32_16x16x32_bf16 v[96:99], a[200:203], a[4:7], v[96:99]\n"
"buffer_load_dwordx4 v188, s[40:43], s74 offen offset:0 lds\n"
"s_waitcnt vmcnt(23)\n"
"v_mfma_f32_16x16x32_bf16 v[104:107], a[200:203], a[68:71], v[104:107]\n"
"s_waitcnt lgkmcnt(0)\n"
"s_barrier\n"
"v_mfma_f32_16x16x32_bf16 v[112:115], a[200:203], a[228:231], v[112:115]\n"
"ds_read_b64_tr_b16 a[200:201], v246 offset:4096\n"
"ds_read_b64_tr_b16 a[202:203], v246 offset:6144\n"
"v_mfma_f32_16x16x32_bf16 v[100:103], a[204:207], a[4:7], v[100:103]\n"
"ds_read_b64_tr_b16 a[192:193], v246 offset:0\n"
"ds_read_b64_tr_b16 a[194:195], v246 offset:2048\n"
"v_mfma_f32_16x16x32_bf16 v[108:111], a[204:207], a[68:71], v[108:111]\n"
"buffer_load_dwordx4 v189, s[40:43], s74 offen offset:1024 lds\n"
"s_add_u32 s74, s73, 0xb800\n"
"v_mfma_f32_16x16x32_bf16 v[116:119], a[204:207], a[228:231], v[116:119]\n"
"ds_read_b64_tr_b16 a[204:205], v247 offset:4096\n"
"ds_read_b64_tr_b16 a[206:207], v247 offset:6144\n"
"v_mfma_f32_16x16x32_bf16 v[96:99], a[208:211], a[8:11], v[96:99]\n"
"ds_read_b64_tr_b16 a[196:197], v247 offset:0\n"
"ds_read_b64_tr_b16 a[198:199], v247 offset:2048\n"
"v_mfma_f32_16x16x32_bf16 v[104:107], a[208:211], a[72:75], v[104:107]\n"
"v_mfma_f32_16x16x32_bf16 v[112:115], a[208:211], a[232:235], v[112:115]\n"
"ds_read_b64_tr_b16 a[208:209], v246 offset:8192\n"
"ds_read_b64_tr_b16 a[210:211], v246 offset:10240\n"
"v_mfma_f32_16x16x32_bf16 v[100:103], a[212:215], a[8:11], v[100:103]\n"
"buffer_load_dwordx4 v188, s[40:43], s74 offen offset:2048 lds\n"
"s_waitcnt vmcnt(22)\n"
"v_cmp_ne_u32 s[22:23], s82, v240\n"
"s_nop 0\n"
"s_and_b32 s22, s22, 0x8\n"
"s_cbranch_scc1 .Lk1_pw4\n"
".Lk1_pd4:\n"
"v_mfma_f32_16x16x32_bf16 v[108:111], a[212:215], a[72:75], v[108:111]\n"
"s_add_u32 s71, s57, 196608\n"
"buffer_load_dwordx4 a[156:159], v252, s[48:51], s71 offen offset:0 sc1 nt\n"
"v_mfma_f32_16x16x32_bf16 v[116:119], a[212:215], a[232:235], v[116:119]\n"
"ds_read_b64_tr_b16 a[212:213], v247 offset:8192\n"
"ds_read_b64_tr_b16 a[214:215], v247 offset:10240\n"
"v_mfma_f32_16x16x32_bf16 v[96:99], a[216:219], a[12:15], v[96:99]\n"
"v_mfma_f32_16x16x32_bf16 v[104:107], a[216:219], a[76:79], v[104:107]\n"
"buffer_load_dwordx4 a[160:163], v252, s[48:51], s71 offen offset:1024 sc1 nt\n"
"v_mfma_f32_16x16x32_bf16 v[112:115], a[216:219], a[236:239], v[112:115]\n"
"ds_read_b64_tr_b16 a[216:217], v246 offset:12288\n"
"ds_read_b64_tr_b16 a[218:219], v246 offset:14336\n"
"v_mfma_f32_16x16x32_bf16 v[100:103], a[220:223], a[12:15], v[100:103]\n"
"v_mfma_f32_16x16x32_bf16 v[108:111], a[220:223], a[76:79], v[108:111]\n"
"buffer_load_dwordx4 a[164:167], v252, s[48:51], s71 offen offset:2048 sc1 nt\n"
"v_mfma_f32_16x16x32_bf16 v[116:119], a[220:223], a[236:239], v[116:119]\n"
"ds_read_b64_tr_b16 a[220:221], v247 offset:12288\n"
"ds_read_b64_tr_b16 a[222:223], v247 offset:14336\n"
"v_mfma_f32_16x16x32_bf16 v[96:99], a[32:35], a[16:19], v[96:99]\n"
"v_mfma_f32_16x16x32_bf16 v[104:107], a[32:35], a[80:83], v[104:107]\n"
"v_add_u32 v248, 0x10000, v248\n"
"v_mfma_f32_16x16x32_bf16 v[112:115], a[32:35], a[240:243], v[112:115]\n"
"ds_read_b64_tr_b16 a[32:33], v248 offset:0\n"
"ds_read_b64_tr_b16 a[34:35], v248 offset:2048\n"
"v_mfma_f32_16x16x32_bf16 v[100:103], a[36:39], a[16:19], v[100:103]\n"
"buffer_load_dwordx4 v189, s[40:43], s74 offen offset:3072 lds\n"
"s_add_u32 s73, s94, 0x380\n"
"v_mfma_f32_16x16x32_bf16 v[108:111], a[36:39], a[80:83], v[108:111]\n"
"v_add_u32 v249, 0x10000, v249\n"
"s_and_b32 s73, s73, 0x380\n"
"v_mfma_f32_16x16x32_bf16 v[116:119], a[36:39], a[240:243], v[116:119]\n"
"ds_read_b64_tr_b16 a[36:37], v249 offset:0\n"
"ds_read_b64_tr_b16 a[38:39], v249 offset:2048\n"
"v_mfma_f32_16x16x32_bf16 v[96:99], a[40:43], a[20:23], v[96:99]\n"
"s_add_u32 s73, s73, s97\n"
"s_add_u32 m0, s68, 0x8000\n"
"s_add_u32 s74, s73, 0x0\n"
"v_mfma_f32_16x16x32_bf16 v[104:107], a[40:43], a[84:87], v[104:107]\n"
"buffer_load_dwordx4 v188, s[40:43], s74 offen offset:0 lds\n"
"v_mfma_f32_16x16x32_bf16 v[112:115], a[40:43], a[244:247], v[112:115]\n"
"ds_read_b64_tr_b16 a[40:41], v248 offset:4096\n"
"ds_read_b64_tr_b16 a[42:43], v248 offset:6144\n"
"v_mfma_f32_16x16x32_bf16 v[100:103], a[44:47], a[20:23], v[100:103]\n"
"v_mfma_f32_16x16x32_bf16 v[108:111], a[44:47], a[84:87], v[108:111]\n"
"v_mfma_f32_16x16x32_bf16 v[116:119], a[44:47], a[244:247], v[116:119]\n"
"ds_read_b64_tr_b16 a[44:45], v249 offset:4096\n"
"ds_read_b64_tr_b16 a[46:47], v249 offset:6144\n"
"v_mfma_f32_16x16x32_bf16 v[96:99], a[48:51], a[24:27], v[96:99]\n"
"buffer_load_dwordx4 v189, s[40:43], s74 offen offset:1024 lds\n"
"s_add_u32 s74, s73, 0x3800\n"
"v_mfma_f32_16x16x32_bf16 v[104:107], a[48:51], a[88:91], v[104:107]\n"
"v_mfma_f32_16x16x32_bf16 v[112:115], a[48:51], a[248:251], v[112:115]\n"
"ds_read_b64_tr_b16 a[48:49], v248 offset:8192\n"
"ds_read_b64_tr_b16 a[50:51], v248 offset:10240\n"
"v_mfma_f32_16x16x32_bf16 v[100:103], a[52:55], a[24:27], v[100:103]\n"
"v_mfma_f32_16x16x32_bf16 v[108:111], a[52:55], a[88:91], v[108:111]\n"
"v_mfma_f32_16x16x32_bf16 v[116:119], a[52:55], a[248:251], v[116:119]\n"
"ds_read_b64_tr_b16 a[52:53], v249 offset:8192\n"
"ds_read_b64_tr_b16 a[54:55], v249 offset:10240\n"
"v_mfma_f32_16x16x32_bf16 v[96:99], a[56:59], a[28:31], v[96:99]\n"
"buffer_load_dwordx4 v188, s[40:43], s74 offen offset:2048 lds\n"
"v_mfma_f32_16x16x32_bf16 v[104:107], a[56:59], a[92:95], v[104:107]\n"
"v_mfma_f32_16x16x32_bf16 v[112:115], a[56:59], a[252:255], v[112:115]\n"
"ds_read_b64_tr_b16 a[56:57], v248 offset:12288\n"
"ds_read_b64_tr_b16 a[58:59], v248 offset:14336\n"
"v_mfma_f32_16x16x32_bf16 v[100:103], a[60:63], a[28:31], v[100:103]\n"
"buffer_load_dword v242, v185, s[52:55], s86 offen sc1\n"
"v_mfma_f32_16x16x32_bf16 v[108:111], a[60:63], a[92:95], v[108:111]\n"
"v_mfma_f32_16x16x32_bf16 v[116:119], a[60:63], a[252:255], v[116:119]\n"
"ds_read_b64_tr_b16 a[60:61], v249 offset:12288\n"
"ds_read_b64_tr_b16 a[62:63], v249 offset:14336\n"
"s_waitcnt lgkmcnt(15)\n"
"v_mfma_f32_16x16x32_bf16 v[120:123], a[192:195], v[48:51], 0\n"
"s_waitcnt vmcnt(22)\n"
"s_mov_b64 exec, 0x10\n"
"buffer_store_dword v254, v253, s[52:55], 0 offen offset:8 sc1\n"
"s_mov_b64 exec, -1\n"
"v_mfma_f32_16x16x32_bf16 v[128:131], a[192:195], v[64:67], 0\n"
"v_add_u32 v246, 0xffff0000, v246\n"
"v_cvt_pk_bf16_f32 v96, v96, v97\n"
"v_mfma_f32_16x16x32_bf16 v[136:139], a[192:195], v[80:83], 0\n"
"buffer_load_dwordx4 v189, s[40:43], s74 offen offset:3072 lds\n"
"s_add_u32 m0, s68, 0x9000\n"
"v_mfma_f32_16x16x32_bf16 v[124:127], a[196:199], v[48:51], 0\n"
"s_add_u32 s74, s73, 0x8000\n"
"v_cvt_pk_bf16_f32 v97, v98, v99\n"
"v_cvt_pk_bf16_f32 v104, v104, v105\n"
"v_mfma_f32_16x16x32_bf16 v[132:135], a[196:199], v[64:67], 0\n"
"v_add_u32 v247, 0xffff0000, v247\n"
"v_cvt_pk_bf16_f32 v105, v106, v107\n"
"v_mfma_f32_16x16x32_bf16 v[140:143], a[196:199], v[80:83], 0\n"
"buffer_load_dwordx4 v188, s[40:43], s74 offen offset:0 lds\n"
"v_cvt_pk_bf16_f32 v112, v112, v113\n"
"v_mfma_f32_16x16x32_bf16 v[120:123], a[200:203], v[52:55], v[120:123]\n"
"v_cvt_pk_bf16_f32 v113, v114, v115\n"
"v_cvt_pk_bf16_f32 v98, v100, v101\n"
"v_mfma_f32_16x16x32_bf16 v[128:131], a[200:203], v[68:71], v[128:131]\n"
"v_cvt_pk_bf16_f32 v99, v102, v103\n"
"v_cvt_pk_bf16_f32 v106, v108, v109\n"
"v_cvt_pk_bf16_f32 v107, v110, v111\n"
"v_cvt_pk_bf16_f32 v114, v116, v117\n"
"v_cvt_pk_bf16_f32 v115, v118, v119\n"
"s_add_u32 s56, s94, 0x280\n"
"s_and_b32 s56, s56, 0x3c0\n"
"s_add_u32 s56, s56, 0x1400\n"
"s_mul_i32 s56, s56, 192\n"
"buffer_store_dwordx4 v[96:99], v252, s[48:51], s56 offen offset:0 sc1\n"
"s_waitcnt vmcnt(24)\n"
"s_waitcnt lgkmcnt(0)\n"
"s_barrier\n"
"v_mfma_f32_16x16x32_bf16 v[136:139], a[200:203], v[84:87], v[136:139]\n"
"ds_read_b64_tr_b16 a[200:201], v246 offset:4096\n"
"ds_read_b64_tr_b16 a[202:203], v246 offset:6144\n"
"v_mfma_f32_16x16x32_bf16 v[124:127], a[204:207], v[52:55], v[124:127]\n"
"ds_read_b64_tr_b16 a[192:193], v246 offset:0\n"
"ds_read_b64_tr_b16 a[194:195], v246 offset:2048\n"
"v_mfma_f32_16x16x32_bf16 v[132:135], a[204:207], v[68:71], v[132:135]\n"
"buffer_store_dwordx4 v[104:107], v252, s[48:51], s56 offen offset:1024 sc1\n"
"v_mfma_f32_16x16x32_bf16 v[140:143], a[204:207], v[84:87], v[140:143]\n"
"ds_read_b64_tr_b16 a[204:205], v247 offset:4096\n"
"ds_read_b64_tr_b16 a[206:207], v247 offset:6144\n"
"v_mfma_f32_16x16x32_bf16 v[120:123], a[208:211], v[56:59], v[120:123]\n"
"ds_read_b64_tr_b16 a[196:197], v247 offset:0\n"
"ds_read_b64_tr_b16 a[198:199], v247 offset:2048\n"
"v_mfma_f32_16x16x32_bf16 v[128:131], a[208:211], v[72:75], v[128:131]\n"
"buffer_load_dwordx4 v189, s[40:43], s74 offen offset:1024 lds\n"
"s_add_u32 s74, s73, 0xb800\n"
"v_mfma_f32_16x16x32_bf16 v[136:139], a[208:211], v[88:91], v[136:139]\n"
"ds_read_b64_tr_b16 a[208:209], v246 offset:8192\n"
"ds_read_b64_tr_b16 a[210:211], v246 offset:10240\n"
"v_mfma_f32_16x16x32_bf16 v[124:127], a[212:215], v[56:59], v[124:127]\n"
"v_mfma_f32_16x16x32_bf16 v[132:135], a[212:215], v[72:75], v[132:135]\n"
"s_waitcnt vmcnt(21)\n"
"v_cmp_ne_u32 s[22:23], s82, v241\n"
"s_nop 0\n"
"s_and_b32 s22, s22, 0x10\n"
"s_cbranch_scc1 .Lk1_pw5\n"
".Lk1_pd5:\n"
"v_mfma_f32_16x16x32_bf16 v[140:143], a[212:215], v[88:91], v[140:143]\n"
"ds_read_b64_tr_b16 a[212:213], v247 offset:8192\n"
"ds_read_b64_tr_b16 a[214:215], v247 offset:10240\n"
"v_mfma_f32_16x16x32_bf16 v[120:123], a[216:219], v[60:63], v[120:123]\n"
"buffer_store_dwordx4 v[112:115], v252, s[48:51], s56 offen offset:2048 sc1\n"
"v_mfma_f32_16x16x32_bf16 v[128:131], a[216:219], v[76:79], v[128:131]\n"
"s_add_u32 s71, s57, 393216\n"
"v_mfma_f32_16x16x32_bf16 v[136:139], a[216:219], v[92:95], v[136:139]\n"
"ds_read_b64_tr_b16 a[216:217], v246 offset:12288\n"
"ds_read_b64_tr_b16 a[218:219], v246 offset:14336\n"
"v_mfma_f32_16x16x32_bf16 v[124:127], a[220:223], v[60:63], v[124:127]\n"
"buffer_load_dwordx4 v188, s[40:43], s74 offen offset:2048 lds\n"
"v_mfma_f32_16x16x32_bf16 v[132:135], a[220:223], v[76:79], v[132:135]\n"
"v_mfma_f32_16x16x32_bf16 v[140:143], a[220:223], v[92:95], v[140:143]\n"
"ds_read_b64_tr_b16 a[220:221], v247 offset:12288\n"
"ds_read_b64_tr_b16 a[222:223], v247 offset:14336\n"
"v_mfma_f32_16x16x32_bf16 v[120:123], a[32:35], a[96:99], v[120:123]\n"
"buffer_load_dwordx4 v189, s[40:43], s74 offen offset:3072 lds\n"
"v_mfma_f32_16x16x32_bf16 v[128:131], a[32:35], a[112:115], v[128:131]\n"
"v_add_u32 v248, 0xffff0000, v248\n"
"v_mfma_f32_16x16x32_bf16 v[136:139], a[32:35], a[128:131], v[136:139]\n"
"ds_read_b64_tr_b16 a[32:33], v248 offset:0\n"
"ds_read_b64_tr_b16 a[34:35], v248 offset:2048\n"
"v_mfma_f32_16x16x32_bf16 v[124:127], a[36:39], a[96:99], v[124:127]\n"
"v_mfma_f32_16x16x32_bf16 v[132:135], a[36:39], a[112:115], v[132:135]\n"
"v_add_u32 v249, 0xffff0000, v249\n"
"s_waitcnt vmcnt(23)\n"
"v_mfma_f32_16x16x32_bf16 v[144:147], v[168:171], a[144:147], v[144:147]\n"
"v_mfma_f32_16x16x32_bf16 v[148:151], v[170:173], a[144:147], v[148:151]\n"
"v_mfma_f32_16x16x32_bf16 v[152:155], v[168:171], a[148:151], v[152:155]\n"
"v_mfma_f32_16x16x32_bf16 v[156:159], v[170:173], a[148:151], v[156:159]\n"
"v_mfma_f32_16x16x32_bf16 v[160:163], v[168:171], a[152:155], v[160:163]\n"
"v_mfma_f32_16x16x32_bf16 v[164:167], v[170:173], a[152:155], v[164:167]\n"
"buffer_load_dwordx4 a[168:171], v252, s[48:51], s71 offen offset:0 sc1 nt\n"
"v_mfma_f32_16x16x32_bf16 v[140:143], a[36:39], a[128:131], v[140:143]\n"
"ds_read_b64_tr_b16 a[36:37], v249 offset:0\n"
"ds_read_b64_tr_b16 a[38:39], v249 offset:2048\n"
"v_mfma_f32_16x16x32_bf16 v[120:123], a[40:43], a[100:103], v[120:123]\n"
"v_mfma_f32_16x16x32_bf16 v[128:131], a[40:43], a[116:119], v[128:131]\n"
"buffer_load_dwordx4 a[172:175], v252, s[48:51], s71 offen offset:1024 sc1 nt\n"
"v_mfma_f32_16x16x32_bf16 v[136:139], a[40:43], a[132:135], v[136:139]\n"
"ds_read_b64_tr_b16 a[40:41], v248 offset:4096\n"
"ds_read_b64_tr_b16 a[42:43], v248 offset:6144\n"
"v_mfma_f32_16x16x32_bf16 v[124:127], a[44:47], a[100:103], v[124:127]\n"
"v_mfma_f32_16x16x32_bf16 v[132:135], a[44:47], a[116:119], v[132:135]\n"
"v_mfma_f32_16x16x32_bf16 v[140:143], a[44:47], a[132:135], v[140:143]\n"
"ds_read_b64_tr_b16 a[44:45], v249 offset:4096\n"
"ds_read_b64_tr_b16 a[46:47], v249 offset:6144\n"
"v_mfma_f32_16x16x32_bf16 v[120:123], a[48:51], a[104:107], v[120:123]\n"
"buffer_load_dwordx4 a[176:179], v252, s[48:51], s71 offen offset:2048 sc1 nt\n"
"v_mfma_f32_16x16x32_bf16 v[128:131], a[48:51], a[120:123], v[128:131]\n"
"v_mfma_f32_16x16x32_bf16 v[136:139], a[48:51], a[136:139], v[136:139]\n"
"ds_read_b64_tr_b16 a[48:49], v248 offset:8192\n"
"ds_read_b64_tr_b16 a[50:51], v248 offset:10240\n"
"v_mfma_f32_16x16x32_bf16 v[124:127], a[52:55], a[104:107], v[124:127]\n"
"buffer_load_dword v243, v185, s[52:55], s86 offen sc1\n"
"v_mfma_f32_16x16x32_bf16 v[132:135], a[52:55], a[120:123], v[132:135]\n"
"v_mfma_f32_16x16x32_bf16 v[140:143], a[52:55], a[136:139], v[140:143]\n"
"ds_read_b64_tr_b16 a[52:53], v249 offset:8192\n"
"ds_read_b64_tr_b16 a[54:55], v249 offset:10240\n"
"v_mfma_f32_16x16x32_bf16 v[120:123], a[56:59], a[108:111], v[120:123]\n"
"v_mfma_f32_16x16x32_bf16 v[128:131], a[56:59], a[124:127], v[128:131]\n"
"v_mfma_f32_16x16x32_bf16 v[136:139], a[56:59], a[140:143], v[136:139]\n"
"ds_read_b64_tr_b16 a[56:57], v248 offset:12288\n"
"ds_read_b64_tr_b16 a[58:59], v248 offset:14336\n"
"v_mfma_f32_16x16x32_bf16 v[124:127], a[60:63], a[108:111], v[124:127]\n"
"v_mfma_f32_16x16x32_bf16 v[132:135], a[60:63], a[124:127], v[132:135]\n"
"v_mfma_f32_16x16x32_bf16 v[140:143], a[60:63], a[140:143], v[140:143]\n"
"ds_read_b64_tr_b16 a[60:61], v249 offset:12288\n"
"ds_read_b64_tr_b16 a[62:63], v249 offset:14336\n"
"s_waitcnt lgkmcnt(15)\n"
"v_mfma_f32_16x16x32_bf16 v[120:123], a[192:195], a[0:3], v[120:123]\n"
"v_mfma_f32_16x16x32_bf16 v[128:131], a[192:195], a[64:67], v[128:131]\n"
"v_add_u32 v246, 0xffff0000, v246\n"
"v_mfma_f32_16x16x32_bf16 v[136:139], a[192:195], a[224:227], v[136:139]\n"
"v_mfma_f32_16x16x32_bf16 v[124:127], a[196:199], a[0:3], v[124:127]\n"
"v_mfma_f32_16x16x32_bf16 v[132:135], a[196:199], a[64:67], v[132:135]\n"
"v_add_u32 v247, 0xffff0000, v247\n"
"v_mfma_f32_16x16x32_bf16 v[140:143], a[196:199], a[224:227], v[140:143]\n"
"s_waitcnt vmcnt(18)\n"
"v_mfma_f32_16x16x32_bf16 v[144:147], v[168:171], a[156:159], v[144:147]\n"
"v_mfma_f32_16x16x32_bf16 v[148:151], v[170:173], a[156:159], v[148:151]\n"
"v_mfma_f32_16x16x32_bf16 v[152:155], v[168:171], a[160:163], v[152:155]\n"
"v_mfma_f32_16x16x32_bf16 v[156:159], v[170:173], a[160:163], v[156:159]\n"
"v_mfma_f32_16x16x32_bf16 v[160:163], v[168:171], a[164:167], v[160:163]\n"
"v_mfma_f32_16x16x32_bf16 v[164:167], v[170:173], a[164:167], v[164:167]\n"
"v_mfma_f32_16x16x32_bf16 v[120:123], a[200:203], a[4:7], v[120:123]\n"
"s_waitcnt vmcnt(17)\n"
"s_waitcnt lgkmcnt(0)\n"
"s_barrier\n"
"v_mfma_f32_16x16x32_bf16 v[128:131], a[200:203], a[68:71], v[128:131]\n"
"s_waitcnt vmcnt(13)\n"
"v_cmp_ne_u32 s[22:23], s82, v242\n"
"s_nop 0\n"
"s_and_b32 s22, s22, 0x20\n"
"s_cbranch_scc1 .Lk1_pw6\n"
".Lk1_pd6:\n"
"v_mfma_f32_16x16x32_bf16 v[136:139], a[200:203], a[228:231], v[136:139]\n"
"ds_read_b64_tr_b16 a[200:201], v246 offset:4096\n"
"ds_read_b64_tr_b16 a[202:203], v246 offset:6144\n"
"v_mfma_f32_16x16x32_bf16 v[124:127], a[204:207], a[4:7], v[124:127]\n"
"ds_read_b64_tr_b16 a[192:193], v246 offset:0\n"
"ds_read_b64_tr_b16 a[194:195], v246 offset:2048\n"
"v_mfma_f32_16x16x32_bf16 v[132:135], a[204:207], a[68:71], v[132:135]\n"
"s_add_u32 s71, s57, 589824\n"
"buffer_load_dwordx4 a[180:183], v252, s[48:51], s71 offen offset:0 sc1 nt\n"
"v_mfma_f32_16x16x32_bf16 v[140:143], a[204:207], a[228:231], v[140:143]\n"
"ds_read_b64_tr_b16 a[204:205], v247 offset:4096\n"
"ds_read_b64_tr_b16 a[206:207], v247 offset:6144\n"
"v_mfma_f32_16x16x32_bf16 v[120:123], a[208:211], a[8:11], v[120:123]\n"
"ds_read_b64_tr_b16 a[196:197], v247 offset:0\n"
"ds_read_b64_tr_b16 a[198:199], v247 offset:2048\n"
"v_mfma_f32_16x16x32_bf16 v[128:131], a[208:211], a[72:75], v[128:131]\n"
"buffer_load_dwordx4 a[184:187], v252, s[48:51], s71 offen offset:1024 sc1 nt\n"
"v_mfma_f32_16x16x32_bf16 v[136:139], a[208:211], a[232:235], v[136:139]\n"
"ds_read_b64_tr_b16 a[208:209], v246 offset:8192\n"
"ds_read_b64_tr_b16 a[210:211], v246 offset:10240\n"
"v_mfma_f32_16x16x32_bf16 v[124:127], a[212:215], a[8:11], v[124:127]\n"
"v_mfma_f32_16x16x32_bf16 v[132:135], a[212:215], a[72:75], v[132:135]\n"
"buffer_load_dwordx4 a[188:191], v252, s[48:51], s71 offen offset:2048 sc1 nt\n"
"v_mfma_f32_16x16x32_bf16 v[140:143], a[212:215], a[232:235], v[140:143]\n"
"ds_read_b64_tr_b16 a[212:213], v247 offset:8192\n"
"ds_read_b64_tr_b16 a[214:215], v247 offset:10240\n"
"v_mfma_f32_16x16x32_bf16 v[120:123], a[216:219], a[12:15], v[120:123]\n"
"v_mfma_f32_16x16x32_bf16 v[128:131], a[216:219], a[76:79], v[128:131]\n"
"v_mfma_f32_16x16x32_bf16 v[136:139], a[216:219], a[236:239], v[136:139]\n"
"ds_read_b64_tr_b16 a[216:217], v246 offset:12288\n"
"ds_read_b64_tr_b16 a[218:219], v246 offset:14336\n"
"v_mfma_f32_16x16x32_bf16 v[124:127], a[220:223], a[12:15], v[124:127]\n"
"v_mfma_f32_16x16x32_bf16 v[132:135], a[220:223], a[76:79], v[132:135]\n"
"v_mfma_f32_16x16x32_bf16 v[140:143], a[220:223], a[236:239], v[140:143]\n"
"ds_read_b64_tr_b16 a[220:221], v247 offset:12288\n"
"ds_read_b64_tr_b16 a[222:223], v247 offset:14336\n"
"v_mfma_f32_16x16x32_bf16 v[120:123], a[32:35], a[16:19], v[120:123]\n"
"v_mfma_f32_16x16x32_bf16 v[128:131], a[32:35], a[80:83], v[128:131]\n"
"v_add_u32 v248, 0xffff0000, v248\n"
"v_mfma_f32_16x16x32_bf16 v[136:139], a[32:35], a[240:243], v[136:139]\n"
"ds_read_b64_tr_b16 a[32:33], v248 offset:0\n"
"ds_read_b64_tr_b16 a[34:35], v248 offset:2048\n"
"v_mfma_f32_16x16x32_bf16 v[124:127], a[36:39], a[16:19], v[124:127]\n"
"v_mfma_f32_16x16x32_bf16 v[132:135], a[36:39], a[80:83], v[132:135]\n"
"v_add_u32 v249, 0xffff0000, v249\n"
"v_mfma_f32_16x16x32_bf16 v[140:143], a[36:39], a[240:243], v[140:143]\n"
"ds_read_b64_tr_b16 a[36:37], v249 offset:0\n"
"ds_read_b64_tr_b16 a[38:39], v249 offset:2048\n"
"v_mfma_f32_16x16x32_bf16 v[120:123], a[40:43], a[20:23], v[120:123]\n"
"v_mfma_f32_16x16x32_bf16 v[128:131], a[40:43], a[84:87], v[128:131]\n"
"v_mfma_f32_16x16x32_bf16 v[136:139], a[40:43], a[244:247], v[136:139]\n"
"ds_read_b64_tr_b16 a[40:41], v248 offset:4096\n"
"ds_read_b64_tr_b16 a[42:43], v248 offset:6144\n"
"v_mfma_f32_16x16x32_bf16 v[124:127], a[44:47], a[20:23], v[124:127]\n"
"v_mfma_f32_16x16x32_bf16 v[132:135], a[44:47], a[84:87], v[132:135]\n"
"v_mfma_f32_16x16x32_bf16 v[140:143], a[44:47], a[244:247], v[140:143]\n"
"ds_read_b64_tr_b16 a[44:45], v249 offset:4096\n"
"ds_read_b64_tr_b16 a[46:47], v249 offset:6144\n"
"v_mfma_f32_16x16x32_bf16 v[120:123], a[48:51], a[24:27], v[120:123]\n"
"v_mfma_f32_16x16x32_bf16 v[128:131], a[48:51], a[88:91], v[128:131]\n"
"s_waitcnt vmcnt(9)\n"
"s_mov_b64 exec, 0x20\n"
"buffer_store_dword v254, v253, s[52:55], 0 offen offset:8 sc1\n"
"s_mov_b64 exec, -1\n"
"v_mfma_f32_16x16x32_bf16 v[136:139], a[48:51], a[248:251], v[136:139]\n"
"ds_read_b64_tr_b16 a[48:49], v248 offset:8192\n"
"ds_read_b64_tr_b16 a[50:51], v248 offset:10240\n"
"v_mfma_f32_16x16x32_bf16 v[124:127], a[52:55], a[24:27], v[124:127]\n"
"v_mfma_f32_16x16x32_bf16 v[132:135], a[52:55], a[88:91], v[132:135]\n"
"v_mfma_f32_16x16x32_bf16 v[140:143], a[52:55], a[248:251], v[140:143]\n"
"ds_read_b64_tr_b16 a[52:53], v249 offset:8192\n"
"ds_read_b64_tr_b16 a[54:55], v249 offset:10240\n"
"v_mfma_f32_16x16x32_bf16 v[120:123], a[56:59], a[28:31], v[120:123]\n"
"v_mfma_f32_16x16x32_bf16 v[128:131], a[56:59], a[92:95], v[128:131]\n"
"v_mfma_f32_16x16x32_bf16 v[136:139], a[56:59], a[252:255], v[136:139]\n"
"ds_read_b64_tr_b16 a[56:57], v248 offset:12288\n"
"ds_read_b64_tr_b16 a[58:59], v248 offset:14336\n"
"v_mfma_f32_16x16x32_bf16 v[124:127], a[60:63], a[28:31], v[124:127]\n"
"v_mfma_f32_16x16x32_bf16 v[132:135], a[60:63], a[92:95], v[132:135]\n"
"v_mfma_f32_16x16x32_bf16 v[140:143], a[60:63], a[252:255], v[140:143]\n"
"ds_read_b64_tr_b16 a[60:61], v249 offset:12288\n"
"ds_read_b64_tr_b16 a[62:63], v249 offset:14336\n"
"s_waitcnt lgkmcnt(15)\n"
"v_mfma_f32_16x16x32_bf16 v[144:147], a[192:195], v[48:51], v[144:147]\n"
"v_cvt_pk_bf16_f32 v120, v120, v121\n"
"v_cvt_pk_bf16_f32 v121, v122, v123\n"
"v_mfma_f32_16x16x32_bf16 v[152:155], a[192:195], v[64:67], v[152:155]\n"
"v_cvt_pk_bf16_f32 v128, v128, v129\n"
"v_cvt_pk_bf16_f32 v129, v130, v131\n"
"v_mfma_f32_16x16x32_bf16 v[160:163], a[192:195], v[80:83], v[160:163]\n"
"v_cvt_pk_bf16_f32 v136, v136, v137\n"
"v_cvt_pk_bf16_f32 v137, v138, v139\n"
"v_mfma_f32_16x16x32_bf16 v[148:151], a[196:199], v[48:51], v[148:151]\n"
"v_cvt_pk_bf16_f32 v122, v124, v125\n"
"v_cvt_pk_bf16_f32 v123, v126, v127\n"
"v_mfma_f32_16x16x32_bf16 v[156:159], a[196:199], v[64:67], v[156:159]\n"
"v_cvt_pk_bf16_f32 v130, v132, v133\n"
"v_cvt_pk_bf16_f32 v131, v134, v135\n"
"v_mfma_f32_16x16x32_bf16 v[164:167], a[196:199], v[80:83], v[164:167]\n"
"v_cvt_pk_bf16_f32 v138, v140, v141\n"
"v_cvt_pk_bf16_f32 v139, v142, v143\n"
"v_mfma_f32_16x16x32_bf16 v[144:147], a[200:203], v[52:55], v[144:147]\n"
"s_add_u32 s56, s94, 0x300\n"
"s_and_b32 s56, s56, 0x3c0\n"
"s_add_u32 s56, s56, 0x1800\n"
"v_mfma_f32_16x16x32_bf16 v[152:155], a[200:203], v[68:71], v[152:155]\n"
"s_mul_i32 s56, s56, 192\n"
"buffer_store_dwordx4 v[120:123], v252, s[48:51], s56 offen offset:0 sc1\n"
"s_waitcnt vmcnt(9)\n"
"s_waitcnt lgkmcnt(0)\n"
"s_barrier\n"
"v_mfma_f32_16x16x32_bf16 v[160:163], a[200:203], v[84:87], v[160:163]\n"
"ds_read_b64_tr_b16 a[200:201], v246 offset:36864\n"
"ds_read_b64_tr_b16 a[202:203], v246 offset:38912\n"
"v_mfma_f32_16x16x32_bf16 v[148:151], a[204:207], v[52:55], v[148:151]\n"
"ds_read_b64_tr_b16 a[192:193], v246 offset:32768\n"
"ds_read_b64_tr_b16 a[194:195], v246 offset:34816\n"
"v_mfma_f32_16x16x32_bf16 v[156:159], a[204:207], v[68:71], v[156:159]\n"
"buffer_store_dwordx4 v[128:131], v252, s[48:51], s56 offen offset:1024 sc1\n"
"v_mfma_f32_16x16x32_bf16 v[164:167], a[204:207], v[84:87], v[164:167]\n"
"ds_read_b64_tr_b16 a[204:205], v247 offset:36864\n"
"ds_read_b64_tr_b16 a[206:207], v247 offset:38912\n"
"v_mfma_f32_16x16x32_bf16 v[144:147], a[208:211], v[56:59], v[144:147]\n"
"ds_read_b64_tr_b16 a[196:197], v247 offset:32768\n"
"ds_read_b64_tr_b16 a[198:199], v247 offset:34816\n"
"v_mfma_f32_16x16x32_bf16 v[152:155], a[208:211], v[72:75], v[152:155]\n"
"buffer_store_dwordx4 v[136:139], v252, s[48:51], s56 offen offset:2048 sc1\n"
"v_mfma_f32_16x16x32_bf16 v[160:163], a[208:211], v[88:91], v[160:163]\n"
"ds_read_b64_tr_b16 a[208:209], v246 offset:40960\n"
"ds_read_b64_tr_b16 a[210:211], v246 offset:43008\n"
"v_mfma_f32_16x16x32_bf16 v[148:151], a[212:215], v[56:59], v[148:151]\n"
"s_waitcnt vmcnt(7)\n"
"v_cmp_ne_u32 s[22:23], s82, v243\n"
"s_nop 0\n"
"s_and_b32 s22, s22, 0x40\n"
"s_cbranch_scc1 .Lk1_pw7\n"
".Lk1_pd7:\n"
"v_mfma_f32_16x16x32_bf16 v[156:159], a[212:215], v[72:75], v[156:159]\n"
"s_add_u32 s71, s57, 786432\n"
"buffer_load_dwordx4 a[144:147], v252, s[48:51], s71 offen offset:0 sc1 nt\n"
"v_mfma_f32_16x16x32_bf16 v[164:167], a[212:215], v[88:91], v[164:167]\n"
"ds_read_b64_tr_b16 a[212:213], v247 offset:40960\n"
"ds_read_b64_tr_b16 a[214:215], v247 offset:43008\n"
"v_mfma_f32_16x16x32_bf16 v[144:147], a[216:219], v[60:63], v[144:147]\n"
"v_mfma_f32_16x16x32_bf16 v[144:147], v[168:171], a[168:171], v[144:147]\n"
"v_mfma_f32_16x16x32_bf16 v[148:151], v[170:173], a[168:171], v[148:151]\n"
"v_mfma_f32_16x16x32_bf16 v[152:155], v[168:171], a[172:175], v[152:155]\n"
"v_mfma_f32_16x16x32_bf16 v[156:159], v[170:173], a[172:175], v[156:159]\n"
"v_mfma_f32_16x16x32_bf16 v[160:163], v[168:171], a[176:179], v[160:163]\n"
"v_mfma_f32_16x16x32_bf16 v[164:167], v[170:173], a[176:179], v[164:167]\n"
"buffer_load_dwordx4 a[148:151], v252, s[48:51], s71 offen offset:1024 sc1 nt\n"
"v_mfma_f32_16x16x32_bf16 v[152:155], a[216:219], v[76:79], v[152:155]\n"
"v_mfma_f32_16x16x32_bf16 v[160:163], a[216:219], v[92:95], v[160:163]\n"
"ds_read_b64_tr_b16 a[216:217], v246 offset:45056\n"
"ds_read_b64_tr_b16 a[218:219], v246 offset:47104\n"
"v_mfma_f32_16x16x32_bf16 v[148:151], a[220:223], v[60:63], v[148:151]\n"
"buffer_load_dwordx4 a[152:155], v252, s[48:51], s71 offen offset:2048 sc1 nt\n"
"v_mfma_f32_16x16x32_bf16 v[156:159], a[220:223], v[76:79], v[156:159]\n"
"v_mfma_f32_16x16x32_bf16 v[164:167], a[220:223], v[92:95], v[164:167]\n"
"ds_read_b64_tr_b16 a[220:221], v247 offset:45056\n"
"ds_read_b64_tr_b16 a[222:223], v247 offset:47104\n"
"v_mfma_f32_16x16x32_bf16 v[144:147], a[32:35], a[96:99], v[144:147]\n"
"v_mfma_f32_16x16x32_bf16 v[152:155], a[32:35], a[112:115], v[152:155]\n"
"v_mfma_f32_16x16x32_bf16 v[160:163], a[32:35], a[128:131], v[160:163]\n"
"ds_read_b64_tr_b16 a[32:33], v248 offset:32768\n"
"ds_read_b64_tr_b16 a[34:35], v248 offset:34816\n"
"v_mfma_f32_16x16x32_bf16 v[148:151], a[36:39], a[96:99], v[148:151]\n"
"v_mfma_f32_16x16x32_bf16 v[156:159], a[36:39], a[112:115], v[156:159]\n"
"buffer_load_dword v240, v185, s[52:55], s86 offen sc1\n"
"v_mfma_f32_16x16x32_bf16 v[164:167], a[36:39], a[128:131], v[164:167]\n"
"ds_read_b64_tr_b16 a[36:37], v249 offset:32768\n"
"ds_read_b64_tr_b16 a[38:39], v249 offset:34816\n"
"v_mfma_f32_16x16x32_bf16 v[144:147], a[40:43], a[100:103], v[144:147]\n"
"v_mfma_f32_16x16x32_bf16 v[152:155], a[40:43], a[116:119], v[152:155]\n"
"v_mfma_f32_16x16x32_bf16 v[160:163], a[40:43], a[132:135], v[160:163]\n"
"ds_read_b64_tr_b16 a[40:41], v248 offset:36864\n"
"ds_read_b64_tr_b16 a[42:43], v248 offset:38912\n"
"v_mfma_f32_16x16x32_bf16 v[148:151], a[44:47], a[100:103], v[148:151]\n"
"v_mfma_f32_16x16x32_bf16 v[156:159], a[44:47], a[116:119], v[156:159]\n"
"v_mfma_f32_16x16x32_bf16 v[164:167], a[44:47], a[132:135], v[164:167]\n"
"ds_read_b64_tr_b16 a[44:45], v249 offset:36864\n"
"ds_read_b64_tr_b16 a[46:47], v249 offset:38912\n"
"v_mfma_f32_16x16x32_bf16 v[144:147], a[48:51], a[104:107], v[144:147]\n"
"v_mfma_f32_16x16x32_bf16 v[152:155], a[48:51], a[120:123], v[152:155]\n"
"v_mfma_f32_16x16x32_bf16 v[160:163], a[48:51], a[136:139], v[160:163]\n"
"ds_read_b64_tr_b16 a[48:49], v248 offset:40960\n"
"ds_read_b64_tr_b16 a[50:51], v248 offset:43008\n"
"v_mfma_f32_16x16x32_bf16 v[148:151], a[52:55], a[104:107], v[148:151]\n"
"v_mfma_f32_16x16x32_bf16 v[156:159], a[52:55], a[120:123], v[156:159]\n"
"v_mfma_f32_16x16x32_bf16 v[164:167], a[52:55], a[136:139], v[164:167]\n"
"ds_read_b64_tr_b16 a[52:53], v249 offset:40960\n"
"ds_read_b64_tr_b16 a[54:55], v249 offset:43008\n"
"v_mfma_f32_16x16x32_bf16 v[144:147], a[56:59], a[108:111], v[144:147]\n"
"v_mfma_f32_16x16x32_bf16 v[152:155], a[56:59], a[124:127], v[152:155]\n"
"v_mfma_f32_16x16x32_bf16 v[160:163], a[56:59], a[140:143], v[160:163]\n"
"ds_read_b64_tr_b16 a[56:57], v248 offset:45056\n"
"ds_read_b64_tr_b16 a[58:59], v248 offset:47104\n"
"v_mfma_f32_16x16x32_bf16 v[148:151], a[60:63], a[108:111], v[148:151]\n"
"v_mfma_f32_16x16x32_bf16 v[156:159], a[60:63], a[124:127], v[156:159]\n"
"v_mfma_f32_16x16x32_bf16 v[164:167], a[60:63], a[140:143], v[164:167]\n"
"ds_read_b64_tr_b16 a[60:61], v249 offset:45056\n"
"ds_read_b64_tr_b16 a[62:63], v249 offset:47104\n"
"s_waitcnt lgkmcnt(15)\n"
"v_mfma_f32_16x16x32_bf16 v[144:147], a[192:195], a[0:3], v[144:147]\n"
"v_mfma_f32_16x16x32_bf16 v[152:155], a[192:195], a[64:67], v[152:155]\n"
"v_mfma_f32_16x16x32_bf16 v[160:163], a[192:195], a[224:227], v[160:163]\n"
"s_waitcnt vmcnt(8)\n"
"v_mfma_f32_16x16x32_bf16 v[144:147], v[168:171], a[180:183], v[144:147]\n"
"v_mfma_f32_16x16x32_bf16 v[148:151], v[170:173], a[180:183], v[148:151]\n"
"v_mfma_f32_16x16x32_bf16 v[152:155], v[168:171], a[184:187], v[152:155]\n"
"v_mfma_f32_16x16x32_bf16 v[156:159], v[170:173], a[184:187], v[156:159]\n"
"v_mfma_f32_16x16x32_bf16 v[160:163], v[168:171], a[188:191], v[160:163]\n"
"v_mfma_f32_16x16x32_bf16 v[164:167], v[170:173], a[188:191], v[164:167]\n"
"v_mfma_f32_16x16x32_bf16 v[148:151], a[196:199], a[0:3], v[148:151]\n"
"v_mfma_f32_16x16x32_bf16 v[156:159], a[196:199], a[64:67], v[156:159]\n"
"v_mfma_f32_16x16x32_bf16 v[164:167], a[196:199], a[224:227], v[164:167]\n"
"v_mfma_f32_16x16x32_bf16 v[144:147], a[200:203], a[4:7], v[144:147]\n"
"v_mfma_f32_16x16x32_bf16 v[152:155], a[200:203], a[68:71], v[152:155]\n"
"v_mfma_f32_16x16x32_bf16 v[160:163], a[200:203], a[228:231], v[160:163]\n"
"v_mfma_f32_16x16x32_bf16 v[148:151], a[204:207], a[4:7], v[148:151]\n"
"v_mfma_f32_16x16x32_bf16 v[156:159], a[204:207], a[68:71], v[156:159]\n"
"v_mfma_f32_16x16x32_bf16 v[164:167], a[204:207], a[228:231], v[164:167]\n"
"v_mfma_f32_16x16x32_bf16 v[144:147], a[208:211], a[8:11], v[144:147]\n"
"v_mfma_f32_16x16x32_bf16 v[152:155], a[208:211], a[72:75], v[152:155]\n"
"v_mfma_f32_16x16x32_bf16 v[160:163], a[208:211], a[232:235], v[160:163]\n"
"v_mfma_f32_16x16x32_bf16 v[148:151], a[212:215], a[8:11], v[148:151]\n"
"v_mfma_f32_16x16x32_bf16 v[156:159], a[212:215], a[72:75], v[156:159]\n"
"v_mfma_f32_16x16x32_bf16 v[164:167], a[212:215], a[232:235], v[164:167]\n"
"v_mfma_f32_16x16x32_bf16 v[144:147], a[216:219], a[12:15], v[144:147]\n"
"v_mfma_f32_16x16x32_bf16 v[152:155], a[216:219], a[76:79], v[152:155]\n"
"v_mfma_f32_16x16x32_bf16 v[160:163], a[216:219], a[236:239], v[160:163]\n"
"v_mfma_f32_16x16x32_bf16 v[148:151], a[220:223], a[12:15], v[148:151]\n"
"v_mfma_f32_16x16x32_bf16 v[156:159], a[220:223], a[76:79], v[156:159]\n"
"v_mfma_f32_16x16x32_bf16 v[164:167], a[220:223], a[236:239], v[164:167]\n"
"s_waitcnt lgkmcnt(14)\n"
"v_mfma_f32_16x16x32_bf16 v[144:147], a[32:35], a[16:19], v[144:147]\n"
"v_mfma_f32_16x16x32_bf16 v[152:155], a[32:35], a[80:83], v[152:155]\n"
"v_mfma_f32_16x16x32_bf16 v[160:163], a[32:35], a[240:243], v[160:163]\n"
"s_waitcnt lgkmcnt(12)\n"
"v_mfma_f32_16x16x32_bf16 v[148:151], a[36:39], a[16:19], v[148:151]\n"
"v_mfma_f32_16x16x32_bf16 v[156:159], a[36:39], a[80:83], v[156:159]\n"
"v_mfma_f32_16x16x32_bf16 v[164:167], a[36:39], a[240:243], v[164:167]\n"
"s_waitcnt lgkmcnt(10)\n"
"v_mfma_f32_16x16x32_bf16 v[144:147], a[40:43], a[20:23], v[144:147]\n"
"v_mfma_f32_16x16x32_bf16 v[152:155], a[40:43], a[84:87], v[152:155]\n"
"v_mfma_f32_16x16x32_bf16 v[160:163], a[40:43], a[244:247], v[160:163]\n"
"s_waitcnt lgkmcnt(8)\n"
"v_mfma_f32_16x16x32_bf16 v[148:151], a[44:47], a[20:23], v[148:151]\n"
"v_mfma_f32_16x16x32_bf16 v[156:159], a[44:47], a[84:87], v[156:159]\n"
"v_mfma_f32_16x16x32_bf16 v[164:167], a[44:47], a[244:247], v[164:167]\n"
"s_waitcnt vmcnt(4)\n"
"s_mov_b64 exec, 0x40\n"
"buffer_store_dword v254, v253, s[52:55], 0 offen offset:8 sc1\n"
"s_mov_b64 exec, -1\n"
"s_waitcnt lgkmcnt(6)\n"
"v_mfma_f32_16x16x32_bf16 v[144:147], a[48:51], a[24:27], v[144:147]\n"
"v_mfma_f32_16x16x32_bf16 v[152:155], a[48:51], a[88:91], v[152:155]\n"
"v_mfma_f32_16x16x32_bf16 v[160:163], a[48:51], a[248:251], v[160:163]\n"
"s_waitcnt lgkmcnt(4)\n"
"v_mfma_f32_16x16x32_bf16 v[148:151], a[52:55], a[24:27], v[148:151]\n"
"v_mfma_f32_16x16x32_bf16 v[156:159], a[52:55], a[88:91], v[156:159]\n"
"v_mfma_f32_16x16x32_bf16 v[164:167], a[52:55], a[248:251], v[164:167]\n"
"s_waitcnt lgkmcnt(2)\n"
"v_mfma_f32_16x16x32_bf16 v[144:147], a[56:59], a[28:31], v[144:147]\n"
"v_mfma_f32_16x16x32_bf16 v[152:155], a[56:59], a[92:95], v[152:155]\n"
"v_mfma_f32_16x16x32_bf16 v[160:163], a[56:59], a[252:255], v[160:163]\n"
"s_waitcnt lgkmcnt(0)\n"
"v_mfma_f32_16x16x32_bf16 v[148:151], a[60:63], a[28:31], v[148:151]\n"
"v_mfma_f32_16x16x32_bf16 v[156:159], a[60:63], a[92:95], v[156:159]\n"
"v_mfma_f32_16x16x32_bf16 v[164:167], a[60:63], a[252:255], v[164:167]\n"
"s_waitcnt vmcnt(1)\n"
"v_cmp_ne_u32 s[22:23], s82, v240\n"
"s_nop 0\n"
"s_and_b32 s22, s22, 0x80\n"
"s_cbranch_scc1 .Lk1_pw8\n"
".Lk1_pd8:\n"
"s_add_u32 s71, s57, 983040\n"
"buffer_load_dwordx4 a[156:159], v252, s[48:51], s71 offen offset:0 sc1 nt\n"
"buffer_load_dwordx4 a[160:163], v252, s[48:51], s71 offen offset:1024 sc1 nt\n"
"buffer_load_dwordx4 a[164:167], v252, s[48:51], s71 offen offset:2048 sc1 nt\n"
"buffer_load_dword v241, v185, s[52:55], s86 offen sc1\n"
"v_mfma_f32_16x16x32_bf16 v[144:147], v[168:171], a[144:147], v[144:147]\n"
"v_mfma_f32_16x16x32_bf16 v[148:151], v[170:173], a[144:147], v[148:151]\n"
"v_mfma_f32_16x16x32_bf16 v[152:155], v[168:171], a[148:151], v[152:155]\n"
"v_mfma_f32_16x16x32_bf16 v[156:159], v[170:173], a[148:151], v[156:159]\n"
"v_mfma_f32_16x16x32_bf16 v[160:163], v[168:171], a[152:155], v[160:163]\n"
"v_mfma_f32_16x16x32_bf16 v[164:167], v[170:173], a[152:155], v[164:167]\n"
"s_waitcnt vmcnt(0)\n"
"v_cmp_ne_u32 s[22:23], s82, v241\n"
"s_nop 0\n"
"s_and_b32 s22, s22, 0x100\n"
"s_cbranch_scc1 .Lk1_pw9\n"
".Lk1_pd9:\n"
"s_add_u32 s71, s57, 1179648\n"
"buffer_load_dwordx4 a[168:171], v252, s[48:51], s71 offen offset:0 sc1 nt\n"
"buffer_load_dwordx4 a[172:175], v252, s[48:51], s71 offen offset:1024 sc1 nt\n"
"buffer_load_dwordx4 a[176:179], v252, s[48:51], s71 offen offset:2048 sc1 nt\n"
"v_mfma_f32_16x16x32_bf16 v[144:147], v[168:171], a[156:159], v[144:147]\n"
"v_mfma_f32_16x16x32_bf16 v[148:151], v[170:173], a[156:159], v[148:151]\n"
"v_mfma_f32_16x16x32_bf16 v[152:155], v[168:171], a[160:163], v[152:155]\n"
"v_mfma_f32_16x16x32_bf16 v[156:159], v[170:173], a[160:163], v[156:159]\n"
"v_mfma_f32_16x16x32_bf16 v[160:163], v[168:171], a[164:167], v[160:163]\n"
"v_mfma_f32_16x16x32_bf16 v[164:167], v[170:173], a[164:167], v[164:167]\n"
"s_waitcnt vmcnt(0)\n"
"v_mfma_f32_16x16x32_bf16 v[144:147], v[168:171], a[168:171], v[144:147]\n"
"v_mfma_f32_16x16x32_bf16 v[148:151], v[170:173], a[168:171], v[148:151]\n"
"v_mfma_f32_16x16x32_bf16 v[152:155], v[168:171], a[172:175], v[152:155]\n"
"v_mfma_f32_16x16x32_bf16 v[156:159], v[170:173], a[172:175], v[156:159]\n"
"v_mfma_f32_16x16x32_bf16 v[160:163], v[168:171], a[176:179], v[160:163]\n"
"v_mfma_f32_16x16x32_bf16 v[164:167], v[170:173], a[176:179], v[164:167]\n"
"v_cvt_pk_bf16_f32 v0, v144, v145\n"
"v_cvt_pk_bf16_f32 v2, v148, v149\n"
"v_cvt_pk_bf16_f32 v1, v146, v147\n"
"v_cvt_pk_bf16_f32 v3, v150, v151\n"
"s_mov_b32 s71, 0x0\n"
"v_permlane16_swap_b32 v0, v2\n"
"v_permlane16_swap_b32 v1, v3\n"
"buffer_store_dwordx4 v[0:3], v187, s[28:31], s71 offen sc1\n"
"v_cvt_pk_bf16_f32 v4, v152, v153\n"
"v_cvt_pk_bf16_f32 v6, v156, v157\n"
"v_cvt_pk_bf16_f32 v5, v154, v155\n"
"v_cvt_pk_bf16_f32 v7, v158, v159\n"
"s_mov_b32 s71, 0x8000\n"
"v_permlane16_swap_b32 v4, v6\n"
"v_permlane16_swap_b32 v5, v7\n"
"buffer_store_dwordx4 v[4:7], v187, s[28:31], s71 offen sc1\n"
"v_cvt_pk_bf16_f32 v8, v160, v161\n"
"v_cvt_pk_bf16_f32 v10, v164, v165\n"
"v_cvt_pk_bf16_f32 v9, v162, v163\n"
"v_cvt_pk_bf16_f32 v11, v166, v167\n"
"s_mov_b32 s71, 0x10000\n"
"v_permlane16_swap_b32 v8, v10\n"
"v_permlane16_swap_b32 v9, v11\n"
"buffer_store_dwordx4 v[8:11], v187, s[28:31], s71 offen sc1\n"
"s_endpgm\n"
".Lk1_cold:\n"
".Lk1_tr1:\n"
"s_getpc_b64 s[62:63]\n"
"s_branch .Lk1_rare2\n"
"s_branch .Lk1_ct1\n"
".Lk1_tr2:\n"
"s_getpc_b64 s[62:63]\n"
"s_branch .Lk1_rare0\n"
"s_branch .Lk1_ct2\n"
".Lk1_tr3:\n"
"s_getpc_b64 s[62:63]\n"
"s_branch .Lk1_rare1\n"
"s_branch .Lk1_ct3\n"
".Lk1_tr5:\n"
"s_getpc_b64 s[62:63]\n"
"s_branch .Lk1_rare2\n"
"s_branch .Lk1_ct5\n"
".Lk1_tr6:\n"
"s_getpc_b64 s[62:63]\n"
"s_branch .Lk1_rare0\n"
"s_branch .Lk1_ct6\n"
".Lk1_tr7:\n"
"s_getpc_b64 s[62:63]\n"
"s_branch .Lk1_rare1\n"
"s_branch .Lk1_ct7\n"
".Lk1_tr9:\n"
"s_getpc_b64 s[62:63]\n"
"s_branch .Lk1_rare2\n"
"s_branch .Lk1_ct9\n"
".Lk1_tr10:\n"
"s_getpc_b64 s[62:63]\n"
"s_branch .Lk1_rare0\n"
"s_branch .Lk1_ct10\n"
".Lk1_tr11:\n"
"s_getpc_b64 s[62:63]\n"
"s_branch .Lk1_rare1\n"
"s_branch .Lk1_ct11\n"
".Lk1_tr13:\n"
"s_getpc_b64 s[62:63]\n"
"s_branch .Lk1_rare2\n"
"s_branch .Lk1_ct13\n"
".Lk1_tr14:\n"
"s_getpc_b64 s[62:63]\n"
"s_branch .Lk1_rare0\n"
"s_branch .Lk1_ct14\n"
".Lk1_tr15:\n"
"s_getpc_b64 s[62:63]\n"
"s_branch .Lk1_rare1\n"
"s_branch .Lk1_ct15\n"
".Lk1_tr105:\n"
"s_getpc_b64 s[62:63]\n"
"s_branch .Lk1_rare2\n"
"s_branch .Lk1_ct105\n"
".Lk1_tr106:\n"
"s_getpc_b64 s[62:63]\n"
"s_branch .Lk1_rare0\n"
"s_branch .Lk1_ct106\n"
".Lk1_tr107:\n"
"s_getpc_b64 s[62:63]\n"
"s_branch .Lk1_rare1\n"
"s_branch .Lk1_ct107\n"
".Lk1_tr109:\n"
"s_getpc_b64 s[62:63]\n"
"s_branch .Lk1_rare2\n"
"s_branch .Lk1_ct109\n"
".Lk1_tr110:\n"
"s_getpc_b64 s[62:63]\n"
"s_branch .Lk1_rare0\n"
"s_branch .Lk1_ct110\n"
".Lk1_tr111:\n"
"s_getpc_b64 s[62:63]\n"
"s_branch .Lk1_rare1\n"
"s_branch .Lk1_ct111\n"
".Lk1_tr112:\n"
"s_getpc_b64 s[62:63]\n"
"s_branch .Lk1_rare2\n"
"s_branch .Lk1_ct112\n"
".Lk1_tr113:\n"
"s_getpc_b64 s[62:63]\n"
"s_branch .Lk1_rare0\n"
"s_branch .Lk1_ct113\n"
".Lk1_tr114:\n"
"s_getpc_b64 s[62:63]\n"
"s_branch .Lk1_rare1\n"
"s_branch .Lk1_ct114\n"
".Lk1_tr115:\n"
"s_getpc_b64 s[62:63]\n"
"s_branch .Lk1_rare2\n"
"s_branch .Lk1_ct115\n"
".Lk1_tr116:\n"
"s_getpc_b64 s[62:63]\n"
"s_branch .Lk1_rare0\n"
"s_branch .Lk1_ct116\n"
".Lk1_tr117:\n"
"s_getpc_b64 s[62:63]\n"
"s_branch .Lk1_rare1\n"
"s_branch .Lk1_ct117\n"
".Lk1_tr118:\n"
"s_getpc_b64 s[62:63]\n"
"s_branch .Lk1_rare2\n"
"s_branch .Lk1_ct118\n"
".Lk1_tr119:\n"
"s_getpc_b64 s[62:63]\n"
"s_branch .Lk1_rare0\n"
"s_branch .Lk1_ct119\n"
".Lk1_tr120:\n"
"s_getpc_b64 s[62:63]\n"
"s_branch .Lk1_rare1\n"
"s_branch .Lk1_ct120\n"
".Lk1_tr121:\n"
"s_getpc_b64 s[62:63]\n"
"s_branch .Lk1_rare2\n"
"s_branch .Lk1_ct121\n"
".Lk1_tr0:\n"
"s_sleep 1\n"
"s_memrealtime s[22:23]\n"
"s_waitcnt lgkmcnt(0)\n"
"s_sub_u32 s25, s22, s24\n"
"s_cmp_lt_i32 s25, 0\n"
"s_cbranch_scc1 .Lk1_tr0\n"
"s_mov_b32 s22, s24\n"
"s_branch .Lk1_ct0\n"
".Lk1_tr4:\n"
"s_sleep 1\n"
"s_memrealtime s[22:23]\n"
"s_waitcnt lgkmcnt(0)\n"
"s_sub_u32 s25, s22, s24\n"
"s_cmp_lt_i32 s25, 0\n"
"s_cbranch_scc1 .Lk1_tr4\n"
"s_mov_b32 s22, s24\n"
"s_branch .Lk1_ct4\n"
".Lk1_tr8:\n"
"s_sleep 1\n"
"s_memrealtime s[22:23]\n"
"s_waitcnt lgkmcnt(0)\n"
"s_sub_u32 s25, s22, s24\n"
"s_cmp_lt_i32 s25, 0\n"
"s_cbranch_scc1 .Lk1_tr8\n"
"s_mov_b32 s22, s24\n"
"s_branch .Lk1_ct8\n"
".Lk1_tr12:\n"
"s_sleep 1\n"
"s_memrealtime s[22:23]\n"
"s_waitcnt lgkmcnt(0)\n"
"s_sub_u32 s25, s22, s24\n"
"s_cmp_lt_i32 s25, 0\n"
"s_cbranch_scc1 .Lk1_tr12\n"
"s_mov_b32 s22, s24\n"
"s_branch .Lk1_ct12\n"
".Lk1_tr104:\n"
"s_sleep 1\n"
"s_memrealtime s[22:23]\n"
"s_waitcnt lgkmcnt(0)\n"
"s_sub_u32 s25, s22, s24\n"
"s_cmp_lt_i32 s25, 0\n"
"s_cbranch_scc1 .Lk1_tr104\n"
"s_mov_b32 s22, s24\n"
"s_branch .Lk1_ct104\n"
".Lk1_tr108:\n"
"s_sleep 1\n"
"s_memrealtime s[22:23]\n"
"s_waitcnt lgkmcnt(0)\n"
"s_sub_u32 s25, s22, s24\n"
"s_cmp_lt_i32 s25, 0\n"
"s_cbranch_scc1 .Lk1_tr108\n"
"s_mov_b32 s22, s24\n"
"s_branch .Lk1_ct108\n"
".Lk1_hw:\n"
"s_mov_b32 s95, 0x8000\n"
".Lk1_hx:\n"
"buffer_load_dword v254, v245, s[52:55], 0 offen sc1\n"
"s_waitcnt vmcnt(0)\n"
"s_mov_b64 exec, 0x37f\n"
"buffer_store_dword v254, v253, s[52:55], 0 offen offset:8 sc1\n"
"s_mov_b64 exec, -1\n"
"buffer_load_dword v186, v185, s[52:55], s86 offen sc1\n"
"s_waitcnt vmcnt(0)\n"
"v_subrev_u32 v186, s82, v186\n"
"v_cmp_lt_u32 vcc, 1, v186\n"
"s_nop 1\n"
"s_and_b32 s33, vcc_lo, 0xdfc\n"
"s_cbranch_scc0 .Lk1_hd\n"
"s_sleep 1\n"
"s_sub_u32 s95, s95, 1\n"
"s_cbranch_scc0 .Lk1_hx\n"
"s_branch .Lk1_hd\n"
".Lk1_pw0:\n"
"s_mov_b32 s95, 0x10000\n"
".Lk1_py0:\n"
"buffer_load_dword v240, v185, s[52:55], s86 offen sc1\n"
"s_waitcnt vmcnt(0)\n"
"v_cmp_ne_u32 s[22:23], s82, v240\n"
"s_nop 0\n"
"s_and_b32 s22, s22, 0xc00\n"
"s_cbranch_scc0 .Lk1_pd0\n"
"s_sub_u32 s95, s95, 1\n"
"s_cbranch_scc0 .Lk1_py0\n"
"s_branch .Lk1_pd0\n"
".Lk1_pw1:\n"
"s_mov_b32 s95, 0x10000\n"
".Lk1_py1:\n"
"buffer_load_dword v241, v185, s[52:55], s86 offen sc1\n"
"s_waitcnt vmcnt(0)\n"
"v_cmp_ne_u32 s[22:23], s82, v241\n"
"s_nop 0\n"
"s_and_b32 s22, s22, 0x3000\n"
"s_cbranch_scc0 .Lk1_pd1\n"
"s_sub_u32 s95, s95, 1\n"
"s_cbranch_scc0 .Lk1_py1\n"
"s_branch .Lk1_pd1\n"
".Lk1_pw2:\n"
"s_mov_b32 s95, 0x10000\n"
".Lk1_py2:\n"
"buffer_load_dword v242, v185, s[52:55], s86 offen sc1\n"
"s_waitcnt vmcnt(0)\n"
"v_cmp_ne_u32 s[22:23], s82, v242\n"
"s_nop 0\n"
"s_and_b32 s22, s22, 0xc000\n"
"s_cbranch_scc0 .Lk1_pd2\n"
"s_sub_u32 s95, s95, 1\n"
"s_cbranch_scc0 .Lk1_py2\n"
"s_branch .Lk1_pd2\n"
".Lk1_pw3:\n"
"s_mov_b32 s95, 0x10000\n"
".Lk1_py3:\n"
"buffer_load_dword v243, v185, s[52:55], s86 offen sc1\n"
"s_waitcnt vmcnt(0)\n"
"v_cmp_ne_u32 s[22:23], s82, v243\n"
"s_nop 0\n"
"s_and_b32 s22, s22, 0x4\n"
"s_cbranch_scc0 .Lk1_pd3\n"
"s_sub_u32 s95, s95, 1\n"
"s_cbranch_scc0 .Lk1_py3\n"
"s_branch .Lk1_pd3\n"
".Lk1_pw4:\n"
"s_mov_b32 s95, 0x10000\n"
".Lk1_py4:\n"
"buffer_load_dword v240, v185, s[52:55], s86 offen sc1\n"
"s_waitcnt vmcnt(0)\n"
"v_cmp_ne_u32 s[22:23], s82, v240\n"
"s_nop 0\n"
"s_and_b32 s22, s22, 0x8\n"
"s_cbranch_scc0 .Lk1_pd4\n"
"s_sub_u32 s95, s95, 1\n"
"s_cbranch_scc0 .Lk1_py4\n"
"s_branch .Lk1_pd4\n"
".Lk1_pw5:\n"
"s_mov_b32 s95, 0x10000\n"
".Lk1_py5:\n"
"buffer_load_dword v241, v185, s[52:55], s86 offen sc1\n"
"s_waitcnt vmcnt(0)\n"
"v_cmp_ne_u32 s[22:23], s82, v241\n"
"s_nop 0\n"
"s_and_b32 s22, s22, 0x10\n"
"s_cbranch_scc0 .Lk1_pd5\n"
"s_sub_u32 s95, s95, 1\n"
"s_cbranch_scc0 .Lk1_py5\n"
"s_branch .Lk1_pd5\n"
".Lk1_pw6:\n"
"s_mov_b32 s95, 0x10000\n"
".Lk1_py6:\n"
"buffer_load_dword v242, v185, s[52:55], s86 offen sc1\n"
"s_waitcnt vmcnt(0)\n"
"v_cmp_ne_u32 s[22:23], s82, v242\n"
"s_nop 0\n"
"s_and_b32 s22, s22, 0x20\n"
"s_cbranch_scc0 .Lk1_pd6\n"
"s_sub_u32 s95, s95, 1\n"
"s_cbranch_scc0 .Lk1_py6\n"
"s_branch .Lk1_pd6\n"
".Lk1_pw7:\n"
"s_mov_b32 s95, 0x10000\n"
".Lk1_py7:\n"
"buffer_load_dword v243, v185, s[52:55], s86 offen sc1\n"
"s_waitcnt vmcnt(0)\n"
"v_cmp_ne_u32 s[22:23], s82, v243\n"
"s_nop 0\n"
"s_and_b32 s22, s22, 0x40\n"
"s_cbranch_scc0 .Lk1_pd7\n"
"s_sub_u32 s95, s95, 1\n"
"s_cbranch_scc0 .Lk1_py7\n"
"s_branch .Lk1_pd7\n"
".Lk1_pw8:\n"
"s_mov_b32 s95, 0x10000\n"
".Lk1_py8:\n"
"buffer_load_dword v240, v185, s[52:55], s86 offen sc1\n"
"s_waitcnt vmcnt(0)\n"
"v_cmp_ne_u32 s[22:23], s82, v240\n"
"s_nop 0\n"
"s_and_b32 s22, s22, 0x80\n"
"s_cbranch_scc0 .Lk1_pd8\n"
"s_sub_u32 s95, s95, 1\n"
"s_cbranch_scc0 .Lk1_py8\n"
"s_branch .Lk1_pd8\n"
".Lk1_pw9:\n"
"s_mov_b32 s95, 0x10000\n"
".Lk1_py9:\n"
"buffer_load_dword v241, v185, s[52:55], s86 offen sc1\n"
"s_waitcnt vmcnt(0)\n"
"v_cmp_ne_u32 s[22:23], s82, v241\n"
"s_nop 0\n"
"s_and_b32 s22, s22, 0x100\n"
"s_cbranch_scc0 .Lk1_pd9\n"
"s_sub_u32 s95, s95, 1\n"
"s_cbranch_scc0 .Lk1_py9\n"
"s_branch .Lk1_pd9\n"
".Lk1_rare0:\n"
"s_nop 15\n"
"v_max3_f32 v176, v192, v193, v194\n"
"v_max3_f32 v177, v200, v201, v202\n"
"v_max3_f32 v176, v176, v195, v196\n"
"v_max3_f32 v177, v177, v203, v204\n"
"v_max3_f32 v176, v176, v197, v198\n"
"v_max3_f32 v177, v177, v205, v206\n"
"v_max_f32 v176, v176, v199\n"
"v_max_f32 v177, v177, v207\n"
"s_nop 1\n"
"v_permlane16_swap_b32 v176, v177\n"
"v_max_f32 v176, v176, v177\n"
"v_mov_b32 v177, v176\n"
"s_nop 1\n"
"v_permlane32_swap_b32 v176, v177\n"
"v_max_f32 v176, v176, v177\n"
"v_mov_b32 v177, v176\n"
"s_nop 1\n"
"v_permlane16_swap_b32 v176, v177\n"
"v_add_f32 v176, 0x41a00000, v176\n"
"v_max_f32 v176, 0, v176\n"
"v_exp_f32 v178, -v176\n"
"v_sub_f32 v96, v96, v176\n"
"v_sub_f32 v97, v97, v176\n"
"v_sub_f32 v98, v98, v176\n"
"v_sub_f32 v99, v99, v176\n"
"v_mul_f32 v144, v144, v178\n"
"v_accvgpr_read_b32 v180, a0\n"
"v_mul_f32 v180, v180, v178\n"
"v_accvgpr_write_b32 a0, v180\n"
"v_accvgpr_read_b32 v181, a1\n"
"v_mul_f32 v181, v181, v178\n"
"v_accvgpr_write_b32 a1, v181\n"
"v_accvgpr_read_b32 v180, a2\n"
"v_mul_f32 v180, v180, v178\n"
"v_accvgpr_write_b32 a2, v180\n"
"v_accvgpr_read_b32 v181, a3\n"
"v_mul_f32 v181, v181, v178\n"
"v_accvgpr_write_b32 a3, v181\n"
"v_accvgpr_read_b32 v180, a4\n"
"v_mul_f32 v180, v180, v178\n"
"v_accvgpr_write_b32 a4, v180\n"
"v_accvgpr_read_b32 v181, a5\n"
"v_mul_f32 v181, v181, v178\n"
"v_accvgpr_write_b32 a5, v181\n"
"v_accvgpr_read_b32 v180, a6\n"
"v_mul_f32 v180, v180, v178\n"
"v_accvgpr_write_b32 a6, v180\n"
"v_accvgpr_read_b32 v181, a7\n"
"v_mul_f32 v181, v181, v178\n"
"v_accvgpr_write_b32 a7, v181\n"
"v_accvgpr_read_b32 v180, a8\n"
"v_mul_f32 v180, v180, v178\n"
"v_accvgpr_write_b32 a8, v180\n"
"v_accvgpr_read_b32 v181, a9\n"
"v_mul_f32 v181, v181, v178\n"
"v_accvgpr_write_b32 a9, v181\n"
"v_accvgpr_read_b32 v180, a10\n"
"v_mul_f32 v180, v180, v178\n"
"v_accvgpr_write_b32 a10, v180\n"
"v_accvgpr_read_b32 v181, a11\n"
"v_mul_f32 v181, v181, v178\n"
"v_accvgpr_write_b32 a11, v181\n"
"v_accvgpr_read_b32 v180, a12\n"
"v_mul_f32 v180, v180, v178\n"
"v_accvgpr_write_b32 a12, v180\n"
"v_accvgpr_read_b32 v181, a13\n"
"v_mul_f32 v181, v181, v178\n"
"v_accvgpr_write_b32 a13, v181\n"
"v_accvgpr_read_b32 v180, a14\n"
"v_mul_f32 v180, v180, v178\n"
"v_accvgpr_write_b32 a14, v180\n"
"v_accvgpr_read_b32 v181, a15\n"
"v_mul_f32 v181, v181, v178\n"
"v_accvgpr_write_b32 a15, v181\n"
"v_accvgpr_read_b32 v180, a16\n"
"v_mul_f32 v180, v180, v178\n"
"v_accvgpr_write_b32 a16, v180\n"
"v_accvgpr_read_b32 v181, a17\n"
"v_mul_f32 v181, v181, v178\n"
"v_accvgpr_write_b32 a17, v181\n"
"v_accvgpr_read_b32 v180, a18\n"
"v_mul_f32 v180, v180, v178\n"
"v_accvgpr_write_b32 a18, v180\n"
"v_accvgpr_read_b32 v181, a19\n"
"v_mul_f32 v181, v181, v178\n"
"v_accvgpr_write_b32 a19, v181\n"
"v_accvgpr_read_b32 v180, a20\n"
"v_mul_f32 v180, v180, v178\n"
"v_accvgpr_write_b32 a20, v180\n"
"v_accvgpr_read_b32 v181, a21\n"
"v_mul_f32 v181, v181, v178\n"
"v_accvgpr_write_b32 a21, v181\n"
"v_accvgpr_read_b32 v180, a22\n"
"v_mul_f32 v180, v180, v178\n"
"v_accvgpr_write_b32 a22, v180\n"
"v_accvgpr_read_b32 v181, a23\n"
"v_mul_f32 v181, v181, v178\n"
"v_accvgpr_write_b32 a23, v181\n"
"v_accvgpr_read_b32 v180, a24\n"
"v_mul_f32 v180, v180, v178\n"
"v_accvgpr_write_b32 a24, v180\n"
"v_accvgpr_read_b32 v181, a25\n"
"v_mul_f32 v181, v181, v178\n"
"v_accvgpr_write_b32 a25, v181\n"
"v_accvgpr_read_b32 v180, a26\n"
"v_mul_f32 v180, v180, v178\n"
"v_accvgpr_write_b32 a26, v180\n"
"v_accvgpr_read_b32 v181, a27\n"
"v_mul_f32 v181, v181, v178\n"
"v_accvgpr_write_b32 a27, v181\n"
"v_accvgpr_read_b32 v180, a28\n"
"v_mul_f32 v180, v180, v178\n"
"v_accvgpr_write_b32 a28, v180\n"
"v_accvgpr_read_b32 v181, a29\n"
"v_mul_f32 v181, v181, v178\n"
"v_accvgpr_write_b32 a29, v181\n"
"v_accvgpr_read_b32 v180, a30\n"
"v_mul_f32 v180, v180, v178\n"
"v_accvgpr_write_b32 a30, v180\n"
"v_accvgpr_read_b32 v181, a31\n"
"v_mul_f32 v181, v181, v178\n"
"v_accvgpr_write_b32 a31, v181\n"
"v_sub_f32 v180, v192, v176\n"
"v_sub_f32 v181, v193, v176\n"
"v_exp_f32 v180, v180\n"
"v_exp_f32 v181, v181\n"
"s_nop 0\n"
"v_cvt_pk_bf16_f32 v120, v180, v181\n"
"v_sub_f32 v180, v194, v176\n"
"v_sub_f32 v181, v195, v176\n"
"v_exp_f32 v180, v180\n"
"v_exp_f32 v181, v181\n"
"s_nop 0\n"
"v_cvt_pk_bf16_f32 v121, v180, v181\n"
"v_sub_f32 v180, v196, v176\n"
"v_sub_f32 v181, v197, v176\n"
"v_exp_f32 v180, v180\n"
"v_exp_f32 v181, v181\n"
"s_nop 0\n"
"v_cvt_pk_bf16_f32 v122, v180, v181\n"
"v_sub_f32 v180, v198, v176\n"
"v_sub_f32 v181, v199, v176\n"
"v_exp_f32 v180, v180\n"
"v_exp_f32 v181, v181\n"
"s_nop 0\n"
"v_cvt_pk_bf16_f32 v123, v180, v181\n"
"v_add_f32 v177, 0x41a00000, v177\n"
"v_max_f32 v177, 0, v177\n"
"v_exp_f32 v179, -v177\n"
"v_sub_f32 v100, v100, v177\n"
"v_sub_f32 v101, v101, v177\n"
"v_sub_f32 v102, v102, v177\n"
"v_sub_f32 v103, v103, v177\n"
"v_mul_f32 v148, v148, v179\n"
"v_accvgpr_read_b32 v180, a32\n"
"v_mul_f32 v180, v180, v179\n"
"v_accvgpr_write_b32 a32, v180\n"
"v_accvgpr_read_b32 v181, a33\n"
"v_mul_f32 v181, v181, v179\n"
"v_accvgpr_write_b32 a33, v181\n"
"v_accvgpr_read_b32 v180, a34\n"
"v_mul_f32 v180, v180, v179\n"
"v_accvgpr_write_b32 a34, v180\n"
"v_accvgpr_read_b32 v181, a35\n"
"v_mul_f32 v181, v181, v179\n"
"v_accvgpr_write_b32 a35, v181\n"
"v_accvgpr_read_b32 v180, a36\n"
"v_mul_f32 v180, v180, v179\n"
"v_accvgpr_write_b32 a36, v180\n"
"v_accvgpr_read_b32 v181, a37\n"
"v_mul_f32 v181, v181, v179\n"
"v_accvgpr_write_b32 a37, v181\n"
"v_accvgpr_read_b32 v180, a38\n"
"v_mul_f32 v180, v180, v179\n"
"v_accvgpr_write_b32 a38, v180\n"
"v_accvgpr_read_b32 v181, a39\n"
"v_mul_f32 v181, v181, v179\n"
"v_accvgpr_write_b32 a39, v181\n"
"v_accvgpr_read_b32 v180, a40\n"
"v_mul_f32 v180, v180, v179\n"
"v_accvgpr_write_b32 a40, v180\n"
"v_accvgpr_read_b32 v181, a41\n"
"v_mul_f32 v181, v181, v179\n"
"v_accvgpr_write_b32 a41, v181\n"
"v_accvgpr_read_b32 v180, a42\n"
"v_mul_f32 v180, v180, v179\n"
"v_accvgpr_write_b32 a42, v180\n"
"v_accvgpr_read_b32 v181, a43\n"
"v_mul_f32 v181, v181, v179\n"
"v_accvgpr_write_b32 a43, v181\n"
"v_accvgpr_read_b32 v180, a44\n"
"v_mul_f32 v180, v180, v179\n"
"v_accvgpr_write_b32 a44, v180\n"
"v_accvgpr_read_b32 v181, a45\n"
"v_mul_f32 v181, v181, v179\n"
"v_accvgpr_write_b32 a45, v181\n"
"v_accvgpr_read_b32 v180, a46\n"
"v_mul_f32 v180, v180, v179\n"
"v_accvgpr_write_b32 a46, v180\n"
"v_accvgpr_read_b32 v181, a47\n"
"v_mul_f32 v181, v181, v179\n"
"v_accvgpr_write_b32 a47, v181\n"
"v_accvgpr_read_b32 v180, a48\n"
"v_mul_f32 v180, v180, v179\n"
"v_accvgpr_write_b32 a48, v180\n"
"v_accvgpr_read_b32 v181, a49\n"
"v_mul_f32 v181, v181, v179\n"
"v_accvgpr_write_b32 a49, v181\n"
"v_accvgpr_read_b32 v180, a50\n"
"v_mul_f32 v180, v180, v179\n"
"v_accvgpr_write_b32 a50, v180\n"
"v_accvgpr_read_b32 v181, a51\n"
"v_mul_f32 v181, v181, v179\n"
"v_accvgpr_write_b32 a51, v181\n"
"v_accvgpr_read_b32 v180, a52\n"
"v_mul_f32 v180, v180, v179\n"
"v_accvgpr_write_b32 a52, v180\n"
"v_accvgpr_read_b32 v181, a53\n"
"v_mul_f32 v181, v181, v179\n"
"v_accvgpr_write_b32 a53, v181\n"
"v_accvgpr_read_b32 v180, a54\n"
"v_mul_f32 v180, v180, v179\n"
"v_accvgpr_write_b32 a54, v180\n"
"v_accvgpr_read_b32 v181, a55\n"
"v_mul_f32 v181, v181, v179\n"
"v_accvgpr_write_b32 a55, v181\n"
"v_accvgpr_read_b32 v180, a56\n"
"v_mul_f32 v180, v180, v179\n"
"v_accvgpr_write_b32 a56, v180\n"
"v_accvgpr_read_b32 v181, a57\n"
"v_mul_f32 v181, v181, v179\n"
"v_accvgpr_write_b32 a57, v181\n"
"v_accvgpr_read_b32 v180, a58\n"
"v_mul_f32 v180, v180, v179\n"
"v_accvgpr_write_b32 a58, v180\n"
"v_accvgpr_read_b32 v181, a59\n"
"v_mul_f32 v181, v181, v179\n"
"v_accvgpr_write_b32 a59, v181\n"
"v_accvgpr_read_b32 v180, a60\n"
"v_mul_f32 v180, v180, v179\n"
"v_accvgpr_write_b32 a60, v180\n"
"v_accvgpr_read_b32 v181, a61\n"
"v_mul_f32 v181, v181, v179\n"
"v_accvgpr_write_b32 a61, v181\n"
"v_accvgpr_read_b32 v180, a62\n"
"v_mul_f32 v180, v180, v179\n"
"v_accvgpr_write_b32 a62, v180\n"
"v_accvgpr_read_b32 v181, a63\n"
"v_mul_f32 v181, v181, v179\n"
"v_accvgpr_write_b32 a63, v181\n"
"v_sub_f32 v180, v200, v177\n"
"v_sub_f32 v181, v201, v177\n"
"v_exp_f32 v180, v180\n"
"v_exp_f32 v181, v181\n"
"s_nop 0\n"
"v_cvt_pk_bf16_f32 v124, v180, v181\n"
"v_sub_f32 v180, v202, v177\n"
"v_sub_f32 v181, v203, v177\n"
"v_exp_f32 v180, v180\n"
"v_exp_f32 v181, v181\n"
"s_nop 0\n"
"v_cvt_pk_bf16_f32 v125, v180, v181\n"
"v_sub_f32 v180, v204, v177\n"
"v_sub_f32 v181, v205, v177\n"
"v_exp_f32 v180, v180\n"
"v_exp_f32 v181, v181\n"
"s_nop 0\n"
"v_cvt_pk_bf16_f32 v126, v180, v181\n"
"v_sub_f32 v180, v206, v177\n"
"v_sub_f32 v181, v207, v177\n"
"v_exp_f32 v180, v180\n"
"v_exp_f32 v181, v181\n"
"s_nop 0\n"
"v_cvt_pk_bf16_f32 v127, v180, v181\n"
"s_add_u32 s62, s62, 4\n"
"s_addc_u32 s63, s63, 0\n"
"s_nop 1\n"
"s_setpc_b64 s[62:63]\n"
".Lk1_rare1:\n"
"s_nop 15\n"
"v_max3_f32 v176, v208, v209, v210\n"
"v_max3_f32 v177, v216, v217, v218\n"
"v_max3_f32 v176, v176, v211, v212\n"
"v_max3_f32 v177, v177, v219, v220\n"
"v_max3_f32 v176, v176, v213, v214\n"
"v_max3_f32 v177, v177, v221, v222\n"
"v_max_f32 v176, v176, v215\n"
"v_max_f32 v177, v177, v223\n"
"s_nop 1\n"
"v_permlane16_swap_b32 v176, v177\n"
"v_max_f32 v176, v176, v177\n"
"v_mov_b32 v177, v176\n"
"s_nop 1\n"
"v_permlane32_swap_b32 v176, v177\n"
"v_max_f32 v176, v176, v177\n"
"v_mov_b32 v177, v176\n"
"s_nop 1\n"
"v_permlane16_swap_b32 v176, v177\n"
"v_add_f32 v176, 0x41a00000, v176\n"
"v_max_f32 v176, 0, v176\n"
"v_exp_f32 v178, -v176\n"
"v_sub_f32 v104, v104, v176\n"
"v_sub_f32 v105, v105, v176\n"
"v_sub_f32 v106, v106, v176\n"
"v_sub_f32 v107, v107, v176\n"
"v_mul_f32 v152, v152, v178\n"
"v_accvgpr_read_b32 v180, a64\n"
"v_mul_f32 v180, v180, v178\n"
"v_accvgpr_write_b32 a64, v180\n"
"v_accvgpr_read_b32 v181, a65\n"
"v_mul_f32 v181, v181, v178\n"
"v_accvgpr_write_b32 a65, v181\n"
"v_accvgpr_read_b32 v180, a66\n"
"v_mul_f32 v180, v180, v178\n"
"v_accvgpr_write_b32 a66, v180\n"
"v_accvgpr_read_b32 v181, a67\n"
"v_mul_f32 v181, v181, v178\n"
"v_accvgpr_write_b32 a67, v181\n"
"v_accvgpr_read_b32 v180, a68\n"
"v_mul_f32 v180, v180, v178\n"
"v_accvgpr_write_b32 a68, v180\n"
"v_accvgpr_read_b32 v181, a69\n"
"v_mul_f32 v181, v181, v178\n"
"v_accvgpr_write_b32 a69, v181\n"
"v_accvgpr_read_b32 v180, a70\n"
"v_mul_f32 v180, v180, v178\n"
"v_accvgpr_write_b32 a70, v180\n"
"v_accvgpr_read_b32 v181, a71\n"
"v_mul_f32 v181, v181, v178\n"
"v_accvgpr_write_b32 a71, v181\n"
"v_accvgpr_read_b32 v180, a72\n"
"v_mul_f32 v180, v180, v178\n"
"v_accvgpr_write_b32 a72, v180\n"
"v_accvgpr_read_b32 v181, a73\n"
"v_mul_f32 v181, v181, v178\n"
"v_accvgpr_write_b32 a73, v181\n"
"v_accvgpr_read_b32 v180, a74\n"
"v_mul_f32 v180, v180, v178\n"
"v_accvgpr_write_b32 a74, v180\n"
"v_accvgpr_read_b32 v181, a75\n"
"v_mul_f32 v181, v181, v178\n"
"v_accvgpr_write_b32 a75, v181\n"
"v_accvgpr_read_b32 v180, a76\n"
"v_mul_f32 v180, v180, v178\n"
"v_accvgpr_write_b32 a76, v180\n"
"v_accvgpr_read_b32 v181, a77\n"
"v_mul_f32 v181, v181, v178\n"
"v_accvgpr_write_b32 a77, v181\n"
"v_accvgpr_read_b32 v180, a78\n"
"v_mul_f32 v180, v180, v178\n"
"v_accvgpr_write_b32 a78, v180\n"
"v_accvgpr_read_b32 v181, a79\n"
"v_mul_f32 v181, v181, v178\n"
"v_accvgpr_write_b32 a79, v181\n"
"v_accvgpr_read_b32 v180, a80\n"
"v_mul_f32 v180, v180, v178\n"
"v_accvgpr_write_b32 a80, v180\n"
"v_accvgpr_read_b32 v181, a81\n"
"v_mul_f32 v181, v181, v178\n"
"v_accvgpr_write_b32 a81, v181\n"
"v_accvgpr_read_b32 v180, a82\n"
"v_mul_f32 v180, v180, v178\n"
"v_accvgpr_write_b32 a82, v180\n"
"v_accvgpr_read_b32 v181, a83\n"
"v_mul_f32 v181, v181, v178\n"
"v_accvgpr_write_b32 a83, v181\n"
"v_accvgpr_read_b32 v180, a84\n"
"v_mul_f32 v180, v180, v178\n"
"v_accvgpr_write_b32 a84, v180\n"
"v_accvgpr_read_b32 v181, a85\n"
"v_mul_f32 v181, v181, v178\n"
"v_accvgpr_write_b32 a85, v181\n"
"v_accvgpr_read_b32 v180, a86\n"
"v_mul_f32 v180, v180, v178\n"
"v_accvgpr_write_b32 a86, v180\n"
"v_accvgpr_read_b32 v181, a87\n"
"v_mul_f32 v181, v181, v178\n"
"v_accvgpr_write_b32 a87, v181\n"
"v_accvgpr_read_b32 v180, a88\n"
"v_mul_f32 v180, v180, v178\n"
"v_accvgpr_write_b32 a88, v180\n"
"v_accvgpr_read_b32 v181, a89\n"
"v_mul_f32 v181, v181, v178\n"
"v_accvgpr_write_b32 a89, v181\n"
"v_accvgpr_read_b32 v180, a90\n"
"v_mul_f32 v180, v180, v178\n"
"v_accvgpr_write_b32 a90, v180\n"
"v_accvgpr_read_b32 v181, a91\n"
"v_mul_f32 v181, v181, v178\n"
"v_accvgpr_write_b32 a91, v181\n"
"v_accvgpr_read_b32 v180, a92\n"
"v_mul_f32 v180, v180, v178\n"
"v_accvgpr_write_b32 a92, v180\n"
"v_accvgpr_read_b32 v181, a93\n"
"v_mul_f32 v181, v181, v178\n"
"v_accvgpr_write_b32 a93, v181\n"
"v_accvgpr_read_b32 v180, a94\n"
"v_mul_f32 v180, v180, v178\n"
"v_accvgpr_write_b32 a94, v180\n"
"v_accvgpr_read_b32 v181, a95\n"
"v_mul_f32 v181, v181, v178\n"
"v_accvgpr_write_b32 a95, v181\n"
"v_sub_f32 v180, v208, v176\n"
"v_sub_f32 v181, v209, v176\n"
"v_exp_f32 v180, v180\n"
"v_exp_f32 v181, v181\n"
"s_nop 0\n"
"v_cvt_pk_bf16_f32 v128, v180, v181\n"
"v_sub_f32 v180, v210, v176\n"
"v_sub_f32 v181, v211, v176\n"
"v_exp_f32 v180, v180\n"
"v_exp_f32 v181, v181\n"
"s_nop 0\n"
"v_cvt_pk_bf16_f32 v129, v180, v181\n"
"v_sub_f32 v180, v212, v176\n"
"v_sub_f32 v181, v213, v176\n"
"v_exp_f32 v180, v180\n"
"v_exp_f32 v181, v181\n"
"s_nop 0\n"
"v_cvt_pk_bf16_f32 v130, v180, v181\n"
"v_sub_f32 v180, v214, v176\n"
"v_sub_f32 v181, v215, v176\n"
"v_exp_f32 v180, v180\n"
"v_exp_f32 v181, v181\n"
"s_nop 0\n"
"v_cvt_pk_bf16_f32 v131, v180, v181\n"
"v_add_f32 v177, 0x41a00000, v177\n"
"v_max_f32 v177, 0, v177\n"
"v_exp_f32 v179, -v177\n"
"v_sub_f32 v108, v108, v177\n"
"v_sub_f32 v109, v109, v177\n"
"v_sub_f32 v110, v110, v177\n"
"v_sub_f32 v111, v111, v177\n"
"v_mul_f32 v156, v156, v179\n"
"v_accvgpr_read_b32 v180, a96\n"
"v_mul_f32 v180, v180, v179\n"
"v_accvgpr_write_b32 a96, v180\n"
"v_accvgpr_read_b32 v181, a97\n"
"v_mul_f32 v181, v181, v179\n"
"v_accvgpr_write_b32 a97, v181\n"
"v_accvgpr_read_b32 v180, a98\n"
"v_mul_f32 v180, v180, v179\n"
"v_accvgpr_write_b32 a98, v180\n"
"v_accvgpr_read_b32 v181, a99\n"
"v_mul_f32 v181, v181, v179\n"
"v_accvgpr_write_b32 a99, v181\n"
"v_accvgpr_read_b32 v180, a100\n"
"v_mul_f32 v180, v180, v179\n"
"v_accvgpr_write_b32 a100, v180\n"
"v_accvgpr_read_b32 v181, a101\n"
"v_mul_f32 v181, v181, v179\n"
"v_accvgpr_write_b32 a101, v181\n"
"v_accvgpr_read_b32 v180, a102\n"
"v_mul_f32 v180, v180, v179\n"
"v_accvgpr_write_b32 a102, v180\n"
"v_accvgpr_read_b32 v181, a103\n"
"v_mul_f32 v181, v181, v179\n"
"v_accvgpr_write_b32 a103, v181\n"
"v_accvgpr_read_b32 v180, a104\n"
"v_mul_f32 v180, v180, v179\n"
"v_accvgpr_write_b32 a104, v180\n"
"v_accvgpr_read_b32 v181, a105\n"
"v_mul_f32 v181, v181, v179\n"
"v_accvgpr_write_b32 a105, v181\n"
"v_accvgpr_read_b32 v180, a106\n"
"v_mul_f32 v180, v180, v179\n"
"v_accvgpr_write_b32 a106, v180\n"
"v_accvgpr_read_b32 v181, a107\n"
"v_mul_f32 v181, v181, v179\n"
"v_accvgpr_write_b32 a107, v181\n"
"v_accvgpr_read_b32 v180, a108\n"
"v_mul_f32 v180, v180, v179\n"
"v_accvgpr_write_b32 a108, v180\n"
"v_accvgpr_read_b32 v181, a109\n"
"v_mul_f32 v181, v181, v179\n"
"v_accvgpr_write_b32 a109, v181\n"
"v_accvgpr_read_b32 v180, a110\n"
"v_mul_f32 v180, v180, v179\n"
"v_accvgpr_write_b32 a110, v180\n"
"v_accvgpr_read_b32 v181, a111\n"
"v_mul_f32 v181, v181, v179\n"
"v_accvgpr_write_b32 a111, v181\n"
"v_accvgpr_read_b32 v180, a112\n"
"v_mul_f32 v180, v180, v179\n"
"v_accvgpr_write_b32 a112, v180\n"
"v_accvgpr_read_b32 v181, a113\n"
"v_mul_f32 v181, v181, v179\n"
"v_accvgpr_write_b32 a113, v181\n"
"v_accvgpr_read_b32 v180, a114\n"
"v_mul_f32 v180, v180, v179\n"
"v_accvgpr_write_b32 a114, v180\n"
"v_accvgpr_read_b32 v181, a115\n"
"v_mul_f32 v181, v181, v179\n"
"v_accvgpr_write_b32 a115, v181\n"
"v_accvgpr_read_b32 v180, a116\n"
"v_mul_f32 v180, v180, v179\n"
"v_accvgpr_write_b32 a116, v180\n"
"v_accvgpr_read_b32 v181, a117\n"
"v_mul_f32 v181, v181, v179\n"
"v_accvgpr_write_b32 a117, v181\n"
"v_accvgpr_read_b32 v180, a118\n"
"v_mul_f32 v180, v180, v179\n"
"v_accvgpr_write_b32 a118, v180\n"
"v_accvgpr_read_b32 v181, a119\n"
"v_mul_f32 v181, v181, v179\n"
"v_accvgpr_write_b32 a119, v181\n"
"v_accvgpr_read_b32 v180, a120\n"
"v_mul_f32 v180, v180, v179\n"
"v_accvgpr_write_b32 a120, v180\n"
"v_accvgpr_read_b32 v181, a121\n"
"v_mul_f32 v181, v181, v179\n"
"v_accvgpr_write_b32 a121, v181\n"
"v_accvgpr_read_b32 v180, a122\n"
"v_mul_f32 v180, v180, v179\n"
"v_accvgpr_write_b32 a122, v180\n"
"v_accvgpr_read_b32 v181, a123\n"
"v_mul_f32 v181, v181, v179\n"
"v_accvgpr_write_b32 a123, v181\n"
"v_accvgpr_read_b32 v180, a124\n"
"v_mul_f32 v180, v180, v179\n"
"v_accvgpr_write_b32 a124, v180\n"
"v_accvgpr_read_b32 v181, a125\n"
"v_mul_f32 v181, v181, v179\n"
"v_accvgpr_write_b32 a125, v181\n"
"v_accvgpr_read_b32 v180, a126\n"
"v_mul_f32 v180, v180, v179\n"
"v_accvgpr_write_b32 a126, v180\n"
"v_accvgpr_read_b32 v181, a127\n"
"v_mul_f32 v181, v181, v179\n"
"v_accvgpr_write_b32 a127, v181\n"
"v_sub_f32 v180, v216, v177\n"
"v_sub_f32 v181, v217, v177\n"
"v_exp_f32 v180, v180\n"
"v_exp_f32 v181, v181\n"
"s_nop 0\n"
"v_cvt_pk_bf16_f32 v132, v180, v181\n"
"v_sub_f32 v180, v218, v177\n"
"v_sub_f32 v181, v219, v177\n"
"v_exp_f32 v180, v180\n"
"v_exp_f32 v181, v181\n"
"s_nop 0\n"
"v_cvt_pk_bf16_f32 v133, v180, v181\n"
"v_sub_f32 v180, v220, v177\n"
"v_sub_f32 v181, v221, v177\n"
"v_exp_f32 v180, v180\n"
"v_exp_f32 v181, v181\n"
"s_nop 0\n"
"v_cvt_pk_bf16_f32 v134, v180, v181\n"
"v_sub_f32 v180, v222, v177\n"
"v_sub_f32 v181, v223, v177\n"
"v_exp_f32 v180, v180\n"
"v_exp_f32 v181, v181\n"
"s_nop 0\n"
"v_cvt_pk_bf16_f32 v135, v180, v181\n"
"s_add_u32 s62, s62, 4\n"
"s_addc_u32 s63, s63, 0\n"
"s_nop 1\n"
"s_setpc_b64 s[62:63]\n"
".Lk1_rare2:\n"
"s_nop 15\n"
"v_max3_f32 v176, v224, v225, v226\n"
"v_max3_f32 v177, v232, v233, v234\n"
"v_max3_f32 v176, v176, v227, v228\n"
"v_max3_f32 v177, v177, v235, v236\n"
"v_max3_f32 v176, v176, v229, v230\n"
"v_max3_f32 v177, v177, v237, v238\n"
"v_max_f32 v176, v176, v231\n"
"v_max_f32 v177, v177, v239\n"
"s_nop 1\n"
"v_permlane16_swap_b32 v176, v177\n"
"v_max_f32 v176, v176, v177\n"
"v_mov_b32 v177, v176\n"
"s_nop 1\n"
"v_permlane32_swap_b32 v176, v177\n"
"v_max_f32 v176, v176, v177\n"
"v_mov_b32 v177, v176\n"
"s_nop 1\n"
"v_permlane16_swap_b32 v176, v177\n"
"v_add_f32 v176, 0x41a00000, v176\n"
"v_max_f32 v176, 0, v176\n"
"v_exp_f32 v178, -v176\n"
"v_sub_f32 v112, v112, v176\n"
"v_sub_f32 v113, v113, v176\n"
"v_sub_f32 v114, v114, v176\n"
"v_sub_f32 v115, v115, v176\n"
"v_mul_f32 v160, v160, v178\n"
"v_accvgpr_read_b32 v180, a128\n"
"v_mul_f32 v180, v180, v178\n"
"v_accvgpr_write_b32 a128, v180\n"
"v_accvgpr_read_b32 v181, a129\n"
"v_mul_f32 v181, v181, v178\n"
"v_accvgpr_write_b32 a129, v181\n"
"v_accvgpr_read_b32 v180, a130\n"
"v_mul_f32 v180, v180, v178\n"
"v_accvgpr_write_b32 a130, v180\n"
"v_accvgpr_read_b32 v181, a131\n"
"v_mul_f32 v181, v181, v178\n"
"v_accvgpr_write_b32 a131, v181\n"
"v_accvgpr_read_b32 v180, a132\n"
"v_mul_f32 v180, v180, v178\n"
"v_accvgpr_write_b32 a132, v180\n"
"v_accvgpr_read_b32 v181, a133\n"
"v_mul_f32 v181, v181, v178\n"
"v_accvgpr_write_b32 a133, v181\n"
"v_accvgpr_read_b32 v180, a134\n"
"v_mul_f32 v180, v180, v178\n"
"v_accvgpr_write_b32 a134, v180\n"
"v_accvgpr_read_b32 v181, a135\n"
"v_mul_f32 v181, v181, v178\n"
"v_accvgpr_write_b32 a135, v181\n"
"v_accvgpr_read_b32 v180, a136\n"
"v_mul_f32 v180, v180, v178\n"
"v_accvgpr_write_b32 a136, v180\n"
"v_accvgpr_read_b32 v181, a137\n"
"v_mul_f32 v181, v181, v178\n"
"v_accvgpr_write_b32 a137, v181\n"
"v_accvgpr_read_b32 v180, a138\n"
"v_mul_f32 v180, v180, v178\n"
"v_accvgpr_write_b32 a138, v180\n"
"v_accvgpr_read_b32 v181, a139\n"
"v_mul_f32 v181, v181, v178\n"
"v_accvgpr_write_b32 a139, v181\n"
"v_accvgpr_read_b32 v180, a140\n"
"v_mul_f32 v180, v180, v178\n"
"v_accvgpr_write_b32 a140, v180\n"
"v_accvgpr_read_b32 v181, a141\n"
"v_mul_f32 v181, v181, v178\n"
"v_accvgpr_write_b32 a141, v181\n"
"v_accvgpr_read_b32 v180, a142\n"
"v_mul_f32 v180, v180, v178\n"
"v_accvgpr_write_b32 a142, v180\n"
"v_accvgpr_read_b32 v181, a143\n"
"v_mul_f32 v181, v181, v178\n"
"v_accvgpr_write_b32 a143, v181\n"
"v_accvgpr_read_b32 v180, a144\n"
"v_mul_f32 v180, v180, v178\n"
"v_accvgpr_write_b32 a144, v180\n"
"v_accvgpr_read_b32 v181, a145\n"
"v_mul_f32 v181, v181, v178\n"
"v_accvgpr_write_b32 a145, v181\n"
"v_accvgpr_read_b32 v180, a146\n"
"v_mul_f32 v180, v180, v178\n"
"v_accvgpr_write_b32 a146, v180\n"
"v_accvgpr_read_b32 v181, a147\n"
"v_mul_f32 v181, v181, v178\n"
"v_accvgpr_write_b32 a147, v181\n"
"v_accvgpr_read_b32 v180, a148\n"
"v_mul_f32 v180, v180, v178\n"
"v_accvgpr_write_b32 a148, v180\n"
"v_accvgpr_read_b32 v181, a149\n"
"v_mul_f32 v181, v181, v178\n"
"v_accvgpr_write_b32 a149, v181\n"
"v_accvgpr_read_b32 v180, a150\n"
"v_mul_f32 v180, v180, v178\n"
"v_accvgpr_write_b32 a150, v180\n"
"v_accvgpr_read_b32 v181, a151\n"
"v_mul_f32 v181, v181, v178\n"
"v_accvgpr_write_b32 a151, v181\n"
"v_accvgpr_read_b32 v180, a152\n"
"v_mul_f32 v180, v180, v178\n"
"v_accvgpr_write_b32 a152, v180\n"
"v_accvgpr_read_b32 v181, a153\n"
"v_mul_f32 v181, v181, v178\n"
"v_accvgpr_write_b32 a153, v181\n"
"v_accvgpr_read_b32 v180, a154\n"
"v_mul_f32 v180, v180, v178\n"
"v_accvgpr_write_b32 a154, v180\n"
"v_accvgpr_read_b32 v181, a155\n"
"v_mul_f32 v181, v181, v178\n"
"v_accvgpr_write_b32 a155, v181\n"
"v_accvgpr_read_b32 v180, a156\n"
"v_mul_f32 v180, v180, v178\n"
"v_accvgpr_write_b32 a156, v180\n"
"v_accvgpr_read_b32 v181, a157\n"
"v_mul_f32 v181, v181, v178\n"
"v_accvgpr_write_b32 a157, v181\n"
"v_accvgpr_read_b32 v180, a158\n"
"v_mul_f32 v180, v180, v178\n"
"v_accvgpr_write_b32 a158, v180\n"
"v_accvgpr_read_b32 v181, a159\n"
"v_mul_f32 v181, v181, v178\n"
"v_accvgpr_write_b32 a159, v181\n"
"v_sub_f32 v180, v224, v176\n"
"v_sub_f32 v181, v225, v176\n"
"v_exp_f32 v180, v180\n"
"v_exp_f32 v181, v181\n"
"s_nop 0\n"
"v_cvt_pk_bf16_f32 v136, v180, v181\n"
"v_sub_f32 v180, v226, v176\n"
"v_sub_f32 v181, v227, v176\n"
"v_exp_f32 v180, v180\n"
"v_exp_f32 v181, v181\n"
"s_nop 0\n"
"v_cvt_pk_bf16_f32 v137, v180, v181\n"
"v_sub_f32 v180, v228, v176\n"
"v_sub_f32 v181, v229, v176\n"
"v_exp_f32 v180, v180\n"
"v_exp_f32 v181, v181\n"
"s_nop 0\n"
"v_cvt_pk_bf16_f32 v138, v180, v181\n"
"v_sub_f32 v180, v230, v176\n"
"v_sub_f32 v181, v231, v176\n"
"v_exp_f32 v180, v180\n"
"v_exp_f32 v181, v181\n"
"s_nop 0\n"
"v_cvt_pk_bf16_f32 v139, v180, v181\n"
"v_add_f32 v177, 0x41a00000, v177\n"
"v_max_f32 v177, 0, v177\n"
"v_exp_f32 v179, -v177\n"
"v_sub_f32 v116, v116, v177\n"
"v_sub_f32 v117, v117, v177\n"
"v_sub_f32 v118, v118, v177\n"
"v_sub_f32 v119, v119, v177\n"
"v_mul_f32 v164, v164, v179\n"
"v_accvgpr_read_b32 v180, a160\n"
"v_mul_f32 v180, v180, v179\n"
"v_accvgpr_write_b32 a160, v180\n"
"v_accvgpr_read_b32 v181, a161\n"
"v_mul_f32 v181, v181, v179\n"
"v_accvgpr_write_b32 a161, v181\n"
"v_accvgpr_read_b32 v180, a162\n"
"v_mul_f32 v180, v180, v179\n"
"v_accvgpr_write_b32 a162, v180\n"
"v_accvgpr_read_b32 v181, a163\n"
"v_mul_f32 v181, v181, v179\n"
"v_accvgpr_write_b32 a163, v181\n"
"v_accvgpr_read_b32 v180, a164\n"
"v_mul_f32 v180, v180, v179\n"
"v_accvgpr_write_b32 a164, v180\n"
"v_accvgpr_read_b32 v181, a165\n"
"v_mul_f32 v181, v181, v179\n"
"v_accvgpr_write_b32 a165, v181\n"
"v_accvgpr_read_b32 v180, a166\n"
"v_mul_f32 v180, v180, v179\n"
"v_accvgpr_write_b32 a166, v180\n"
"v_accvgpr_read_b32 v181, a167\n"
"v_mul_f32 v181, v181, v179\n"
"v_accvgpr_write_b32 a167, v181\n"
"v_accvgpr_read_b32 v180, a168\n"
"v_mul_f32 v180, v180, v179\n"
"v_accvgpr_write_b32 a168, v180\n"
"v_accvgpr_read_b32 v181, a169\n"
"v_mul_f32 v181, v181, v179\n"
"v_accvgpr_write_b32 a169, v181\n"
"v_accvgpr_read_b32 v180, a170\n"
"v_mul_f32 v180, v180, v179\n"
"v_accvgpr_write_b32 a170, v180\n"
"v_accvgpr_read_b32 v181, a171\n"
"v_mul_f32 v181, v181, v179\n"
"v_accvgpr_write_b32 a171, v181\n"
"v_accvgpr_read_b32 v180, a172\n"
"v_mul_f32 v180, v180, v179\n"
"v_accvgpr_write_b32 a172, v180\n"
"v_accvgpr_read_b32 v181, a173\n"
"v_mul_f32 v181, v181, v179\n"
"v_accvgpr_write_b32 a173, v181\n"
"v_accvgpr_read_b32 v180, a174\n"
"v_mul_f32 v180, v180, v179\n"
"v_accvgpr_write_b32 a174, v180\n"
"v_accvgpr_read_b32 v181, a175\n"
"v_mul_f32 v181, v181, v179\n"
"v_accvgpr_write_b32 a175, v181\n"
"v_accvgpr_read_b32 v180, a176\n"
"v_mul_f32 v180, v180, v179\n"
"v_accvgpr_write_b32 a176, v180\n"
"v_accvgpr_read_b32 v181, a177\n"
"v_mul_f32 v181, v181, v179\n"
"v_accvgpr_write_b32 a177, v181\n"
"v_accvgpr_read_b32 v180, a178\n"
"v_mul_f32 v180, v180, v179\n"
"v_accvgpr_write_b32 a178, v180\n"
"v_accvgpr_read_b32 v181, a179\n"
"v_mul_f32 v181, v181, v179\n"
"v_accvgpr_write_b32 a179, v181\n"
"v_accvgpr_read_b32 v180, a180\n"
"v_mul_f32 v180, v180, v179\n"
"v_accvgpr_write_b32 a180, v180\n"
"v_accvgpr_read_b32 v181, a181\n"
"v_mul_f32 v181, v181, v179\n"
"v_accvgpr_write_b32 a181, v181\n"
"v_accvgpr_read_b32 v180, a182\n"
"v_mul_f32 v180, v180, v179\n"
"v_accvgpr_write_b32 a182, v180\n"
"v_accvgpr_read_b32 v181, a183\n"
"v_mul_f32 v181, v181, v179\n"
"v_accvgpr_write_b32 a183, v181\n"
"v_accvgpr_read_b32 v180, a184\n"
"v_mul_f32 v180, v180, v179\n"
"v_accvgpr_write_b32 a184, v180\n"
"v_accvgpr_read_b32 v181, a185\n"
"v_mul_f32 v181, v181, v179\n"
"v_accvgpr_write_b32 a185, v181\n"
"v_accvgpr_read_b32 v180, a186\n"
"v_mul_f32 v180, v180, v179\n"
"v_accvgpr_write_b32 a186, v180\n"
"v_accvgpr_read_b32 v181, a187\n"
"v_mul_f32 v181, v181, v179\n"
"v_accvgpr_write_b32 a187, v181\n"
"v_accvgpr_read_b32 v180, a188\n"
"v_mul_f32 v180, v180, v179\n"
"v_accvgpr_write_b32 a188, v180\n"
"v_accvgpr_read_b32 v181, a189\n"
"v_mul_f32 v181, v181, v179\n"
"v_accvgpr_write_b32 a189, v181\n"
"v_accvgpr_read_b32 v180, a190\n"
"v_mul_f32 v180, v180, v179\n"
"v_accvgpr_write_b32 a190, v180\n"
"v_accvgpr_read_b32 v181, a191\n"
"v_mul_f32 v181, v181, v179\n"
"v_accvgpr_write_b32 a191, v181\n"
"v_sub_f32 v180, v232, v177\n"
"v_sub_f32 v181, v233, v177\n"
"v_exp_f32 v180, v180\n"
"v_exp_f32 v181, v181\n"
"s_nop 0\n"
"v_cvt_pk_bf16_f32 v140, v180, v181\n"
"v_sub_f32 v180, v234, v177\n"
"v_sub_f32 v181, v235, v177\n"
"v_exp_f32 v180, v180\n"
"v_exp_f32 v181, v181\n"
"s_nop 0\n"
"v_cvt_pk_bf16_f32 v141, v180, v181\n"
"v_sub_f32 v180, v236, v177\n"
"v_sub_f32 v181, v237, v177\n"
"v_exp_f32 v180, v180\n"
"v_exp_f32 v181, v181\n"
"s_nop 0\n"
"v_cvt_pk_bf16_f32 v142, v180, v181\n"
"v_sub_f32 v180, v238, v177\n"
"v_sub_f32 v181, v239, v177\n"
"v_exp_f32 v180, v180\n"
"v_exp_f32 v181, v181\n"
"s_nop 0\n"
"v_cvt_pk_bf16_f32 v143, v180, v181\n"
"s_add_u32 s62, s62, 4\n"
"s_addc_u32 s63, s63, 0\n"
"s_nop 1\n"
"s_setpc_b64 s[62:63]\n"
".Lk1_end:\n"
  :: "s"(__builtin_amdgcn_kernarg_segment_ptr()), "s"(blockIdx.x), "s"(wid), "s"(ldsb)
  : "v0","v1","v2","v3","v4","v5","v6","v7","v8","v9","v10","v11","v12","v13","v14","v15","v16","v17","v18","v19","v20","v21","v22","v23","v24","v25","v26","v27","v28","v29","v30","v31","v32","v33","v34","v35","v36","v37","v38","v39","v40","v41","v42","v43","v44","v45","v46","v47","v48","v49","v50","v51","v52","v53","v54","v55","v56","v57","v58","v59","v60","v61","v62","v63","v64","v65","v66","v67","v68","v69","v70","v71","v72","v73","v74","v75","v76","v77","v78","v79","v80","v81","v82","v83","v84","v85","v86","v87","v88","v89","v90","v91","v92","v93","v94","v95","v96","v97","v98","v99","v100","v101","v102","v103","v104","v105","v106","v107","v108","v109","v110","v111","v112","v113","v114","v115","v116","v117","v118","v119","v120","v121","v122","v123","v124","v125","v126","v127","v128","v129","v130","v131","v132","v133","v134","v135","v136","v137","v138","v139","v140","v141","v142","v143","v144","v145","v146","v147","v148","v149","v150","v151","v152","v153","v154","v155","v156","v157","v158","v159","v160","v161","v162","v163","v164","v165","v166","v167","v168","v169","v170","v171","v172","v173","v174","v175","v176","v177","v178","v179","v180","v181","v182","v183","v184","v185","v186","v187","v188","v189","v190","v191","v192","v193","v194","v195","v196","v197","v198","v199","v200","v201","v202","v203","v204","v205","v206","v207","v208","v209","v210","v211","v212","v213","v214","v215","v216","v217","v218","v219","v220","v221","v222","v223","v224","v225","v226","v227","v228","v229","v230","v231","v232","v233","v234","v235","v236","v237","v238","v239","v240","v241","v242","v243","v244","v245","v246","v247","v248","v249","v250","v251","v252","v253","v254","v255","a0","a1","a2","a3","a4","a5","a6","a7","a8","a9","a10","a11","a12","a13","a14","a15","a16","a17","a18","a19","a20","a21","a22","a23","a24","a25","a26","a27","a28","a29","a30","a31","a32","a33","a34","a35","a36","a37","a38","a39","a40","a41","a42","a43","a44","a45","a46","a47","a48","a49","a50","a51","a52","a53","a54","a55","a56","a57","a58","a59","a60","a61","a62","a63","a64","a65","a66","a67","a68","a69","a70","a71","a72","a73","a74","a75","a76","a77","a78","a79","a80","a81","a82","a83","a84","a85","a86","a87","a88","a89","a90","a91","a92","a93","a94","a95","a96","a97","a98","a99","a100","a101","a102","a103","a104","a105","a106","a107","a108","a109","a110","a111","a112","a113","a114","a115","a116","a117","a118","a119","a120","a121","a122","a123","a124","a125","a126","a127","a128","a129","a130","a131","a132","a133","a134","a135","a136","a137","a138","a139","a140","a141","a142","a143","a144","a145","a146","a147","a148","a149","a150","a151","a152","a153","a154","a155","a156","a157","a158","a159","a160","a161","a162","a163","a164","a165","a166","a167","a168","a169","a170","a171","a172","a173","a174","a175","a176","a177","a178","a179","a180","a181","a182","a183","a184","a185","a186","a187","a188","a189","a190","a191","a192","a193","a194","a195","a196","a197","a198","a199","a200","a201","a202","a203","a204","a205","a206","a207","a208","a209","a210","a211","a212","a213","a214","a215","a216","a217","a218","a219","a220","a221","a222","a223","a224","a225","a226","a227","a228","a229","a230","a231","a232","a233","a234","a235","a236","a237","a238","a239","a240","a241","a242","a243","a244","a245","a246","a247","a248","a249","a250","a251","a252","a253","a254","a255","s8","s9","s10","s11","s12","s13","s14","s15","s16","s17","s18","s19","s20","s21","s22","s23","s24","s25","s26","s27","s28","s29","s30","s31","s36","s37","s38","s39","s40","s41","s42","s43","s44","s45","s46","s47","s48","s49","s50","s51","s52","s53","s54","s55","s56","s57","s58","s59","s60","s61","s62","s63","s64","s65","s66","s67","s68","s69","s70","s71","s72","s73","s74","s75","s76","s77","s78","s79","s80","s81","s82","s83","s84","s85","s86","s87","s88","s89","s90","s91","s92","s93","s94","s95","s96","s97","s98","s99","vcc","scc","memory");
}

extern "C" void kernel_launch(void* const* d_in, const int* in_sizes, int n_in, void* d_out, int out_size, void* d_ws, size_t ws_size, hipStream_t stream) {
  k_fused<<<256, 256, 0, stream>>>((const char*)d_in[0], (const char*)d_in[1], (const char*)d_in[2], (const char*)d_in[3], (const char*)d_in[4],
                                   (const char*)d_in[5], (const char*)d_in[6], (const char*)d_in[7], (const char*)d_in[8], (const char*)d_in[9], (char*)d_ws, (char*)d_out);
}
```
